```python
import torch

def _softmax(x, axis=-1):
    return torch.softmax(x, dim=axis)
def _gelu(x):
    return torch.nn.functional.gelu(x, approximate="tanh")
def _silu(x):
    return torch.nn.functional.silu(x)
def _sigmoid(x):
    return torch.sigmoid(x)
def _layernorm(x, axis=-1, eps=1e-5):
    m = x.mean(dim=axis, keepdim=True)
    v = x.var(dim=axis, keepdim=True, unbiased=False)
    return (x - m) / torch.sqrt(v + eps)
def _scatter_add(tgt, idx, src, axis=0):
    return tgt.clone().index_add_(axis, idx, src)
def _segment_sum(src, seg, n):
    out = torch.zeros((n,) + src.shape[1:], dtype=src.dtype)
    return out.index_add_(0, seg, src)
def _pad(x, pw):
    flat = tuple(v for pair in reversed(pw) for v in pair)
    return torch.nn.functional.pad(x, flat)

kernel_name = 'recurrent_scan__compressed_kv_cla__glu_gated_bilinear__prompt'

def setup_inputs():

    _w = (lambda a: (lambda t: t.to(torch.bfloat16) if t.is_floating_point() else t)(torch.as_tensor(a)))
    def _A_setup():
        B, H, Sq, Sk, D, M = 1, 32, 2048, 2048, 128, 640
        Dc = 4 * D
        return {
            "x": _w((lambda *s: torch.randn(*s, dtype=torch.bfloat16) * 0.05)(B, Sq, M)),
            "c_kv": _w((lambda *s: torch.randn(*s, dtype=torch.bfloat16) * 1.6)(B, Sk, Dc)),
            "w_q": _w((lambda *s: torch.randn(*s, dtype=torch.bfloat16) * 1.6)(M, H, D) * (M ** -0.5)),
            "w_uk": _w((lambda *s: torch.randn(*s, dtype=torch.bfloat16) * 1.6)(Dc, H, D) * (Dc ** -0.5)),
            "w_uv": _w((lambda *s: torch.randn(*s, dtype=torch.bfloat16) * 1.6)(Dc, H, D) * (Dc ** -0.5)),
            "w_o": _w((lambda *s: torch.randn(*s, dtype=torch.bfloat16) * 1.6)(H, D, M) * ((H * D) ** -0.5)),
        }
    _A_in = _A_setup()

    def _B_setup():
        B, S, D, F = 1, 2048, 640, 1728
        return {
            "x": _w((lambda *s: torch.randn(*s, dtype=torch.bfloat16) * 0.05)(B, S, D)),
            "w_gate": _w((lambda *s: torch.randn(*s, dtype=torch.bfloat16) * 0.2)(D, F) * (D ** -0.5)),
            "w_up": _w((lambda *s: torch.randn(*s, dtype=torch.bfloat16) * 0.2)(D, F) * (D ** -0.5)),
            "w_down": _w((lambda *s: torch.randn(*s, dtype=torch.bfloat16) * 0.2)(F, D) * (F ** -0.5)),
        }
    _B_in = _B_setup()

    return {
        "A_x": _A_in["x"],
        "A_c_kv": _A_in["c_kv"],
        "A_w_q": _A_in["w_q"],
        "A_w_uk": _A_in["w_uk"],
        "A_w_uv": _A_in["w_uv"],
        "A_w_o": _A_in["w_o"],
        "B_x": _B_in["x"],
        "B_w_gate": _B_in["w_gate"],
        "B_w_up": _B_in["w_up"],
        "B_w_down": _B_in["w_down"],
    }

def reference(A_x, A_c_kv, A_w_q, A_w_uk, A_w_uv, A_w_o, B_x, B_w_gate, B_w_up, B_w_down):
    def _A_ref(x, c_kv, w_q, w_uk, w_uv, w_o):
        B, Sq, M = x.shape[0], x.shape[1], x.shape[2]
        Sk = c_kv.shape[1]
        H, D = w_q.shape[1], w_q.shape[2]
        q = torch.einsum("bqm,mhd->bhqd", x, w_q)
        k = torch.einsum("bkc,chd->bhkd", c_kv, w_uk)
        v = torch.einsum("bkc,chd->bhkd", c_kv, w_uv)
        scores = torch.einsum("bhqd,bhkd->bhqk", q, k) * (D ** -0.5)
        off = Sk - Sq
        qi = torch.arange(Sq)[:, None]
        kj = torch.arange(Sk)[None, :]
        cmask = kj <= (qi + off)
        scores = torch.where(cmask[None, None], scores, -1e9)
        p = _softmax(scores, axis=-1)
        ctx = torch.einsum("bhqk,bhkd->bhqd", p, v)
        return torch.einsum("bhqd,hdm->bqm", ctx, w_o)

    def _B_ref(x, w_gate, w_up, w_down):
        g = torch.einsum("bsd,df->bsf", x, w_gate)
        u = torch.einsum("bsd,df->bsf", x, w_up)
        h = g * u
        return torch.einsum("bsf,fd->bsd", h, w_down)

    x = A_x
    xn = _layernorm(x, axis=-1)
    s = _A_ref(xn, A_c_kv, A_w_q, A_w_uk, A_w_uv, A_w_o)
    g = _silu(_B_ref(xn, B_w_gate, B_w_up, B_w_down))
    return x + s * g
```

```cpp
#include <hip/hip_runtime.h>
#include <stdint.h>
#define MEGA_CLOBBERS "v0", "v1", "v2", "v3", "v4", "v5", "v6", "v7", "v8", "v9", "v10", "v11", "v12", "v13", "v14", "v15", "v16", "v17", "v18", "v19", "v20", "v21", "v22", "v23", "v24", "v25", "v26", "v27", "v28", "v29", "v30", "v31", "v32", "v33", "v34", "v35", "v36", "v37", "v38", "v39", "v40", "v41", "v42", "v43", "v44", "v45", "v46", "v47", "v48", "v49", "v50", "v51", "v52", "v53", "v54", "v55", "v56", "v57", "v58", "v59", "v60", "v61", "v62", "v63", "v64", "v65", "v66", "v67", "v68", "v69", "v70", "v71", "v72", "v73", "v74", "v75", "v76", "v77", "v78", "v79", "v80", "v81", "v82", "v83", "v84", "v85", "v86", "v87", "v88", "v89", "v90", "v91", "v92", "v93", "v94", "v95", "v96", "v97", "v98", "v99", "v100", "v101", "v102", "v103", "v104", "v105", "v106", "v107", "v108", "v109", "v110", "v111", "v112", "v113", "v114", "v115", "v116", "v117", "v118", "v119", "v120", "v121", "v122", "v123", "v124", "v125", "v126", "v127", "v128", "v129", "v130", "v131", "v132", "v133", "v134", "v135", "v136", "v137", "v138", "v139", "v140", "v141", "v142", "v143", "v144", "v145", "v146", "v147", "v148", "v149", "v150", "v151", "v152", "v153", "v154", "v155", "v156", "v157", "v158", "v159", "v160", "v161", "v162", "v163", "v164", "v165", "v166", "v167", "v168", "v169", "v170", "v171", "v172", "v173", "v174", "v175", "v176", "v177", "v178", "v179", "v180", "v181", "v182", "v183", "v184", "v185", "v186", "v187", "v188", "v189", "v190", "v191", "v192", "v193", "v194", "v195", "v196", "v197", "v198", "v199", "v200", "v201", "v202", "v203", "v204", "v205", "v206", "v207", "v208", "v209", "v210", "v211", "v212", "v213", "v214", "v215", "v216", "v217", "v218", "v219", "v220", "v221", "v222", "v223", "v224", "v225", "v226", "v227", "v228", "v229", "v230", "v231", "v232", "v233", "v234", "v235", "v236", "v237", "v238", "v239", "v240", "v241", "v242", "v243", "v244", "v245", "v246", "v247", "v248", "v249", "v250", "v251", "v252", "v253", "v254", "v255", "a0", "a1", "a2", "a3", "a4", "a5", "a6", "a7", "a8", "a9", "a10", "a11", "a12", "a13", "a14", "a15", "a16", "a17", "a18", "a19", "a20", "a21", "a22", "a23", "a24", "a25", "a26", "a27", "a28", "a29", "a30", "a31", "a32", "a33", "a34", "a35", "a36", "a37", "a38", "a39", "a40", "a41", "a42", "a43", "a44", "a45", "a46", "a47", "a48", "a49", "a50", "a51", "a52", "a53", "a54", "a55", "a56", "a57", "a58", "a59", "a60", "a61", "a62", "a63", "a64", "a65", "a66", "a67", "a68", "a69", "a70", "a71", "a72", "a73", "a74", "a75", "a76", "a77", "a78", "a79", "a80", "a81", "a82", "a83", "a84", "a85", "a86", "a87", "a88", "a89", "a90", "a91", "a92", "a93", "a94", "a95", "a96", "a97", "a98", "a99", "a100", "a101", "a102", "a103", "a104", "a105", "a106", "a107", "a108", "a109", "a110", "a111", "a112", "a113", "a114", "a115", "a116", "a117", "a118", "a119", "a120", "a121", "a122", "a123", "a124", "a125", "a126", "a127", "a128", "a129", "a130", "a131", "a132", "a133", "a134", "a135", "a136", "a137", "a138", "a139", "a140", "a141", "a142", "a143", "a144", "a145", "a146", "a147", "a148", "a149", "a150", "a151", "a152", "a153", "a154", "a155", "a156", "a157", "a158", "a159", "a160", "a161", "a162", "a163", "a164", "a165", "a166", "a167", "a168", "a169", "a170", "a171", "a172", "a173", "a174", "a175", "a176", "a177", "a178", "a179", "a180", "a181", "a182", "a183", "a184", "a185", "a186", "a187", "a188", "a189", "a190", "a191", "a192", "a193", "a194", "a195", "a196", "a197", "a198", "a199", "a200", "a201", "a202", "a203", "a204", "a205", "a206", "a207", "a208", "a209", "a210", "a211", "a212", "a213", "a214", "a215", "a216", "a217", "a218", "a219", "a220", "a221", "a222", "a223", "a224", "a225", "a226", "a227", "a228", "a229", "a230", "a231", "a232", "a233", "a234", "a235", "a236", "a237", "a238", "a239", "a240", "a241", "a242", "a243", "a244", "a245", "a246", "a247", "a248", "a249", "a250", "a251", "a252", "a253", "a254", "a255", "s26", "s27", "s28", "s29", "s30", "s31", "s33", "s34", "s35", "s36", "s37", "s38", "s39", "s40", "s41", "s42", "s43", "s44", "s45", "s46", "s47", "s48", "s49", "s50", "s51", "s52", "s53", "s54", "s55", "s56", "s57", "s58", "s59", "s60", "s61", "s62", "s63", "s64", "s65", "s66", "s67", "s68", "s69", "s70", "s71", "s72", "s73", "s74", "s75", "s76", "s77", "s78", "s79", "s80", "s81", "s82", "s83", "s84", "s85", "s86", "s87", "s88", "s89", "s90", "s91", "s92", "s93", "s94", "s95", "s96", "s97", "s98", "s99", "vcc", "scc", "memory"

__global__ __launch_bounds__(256, 1) void mega_kernel(const void* x, const void* ckv, const void* wq, const void* wuk, const void* wuv, const void* wo,
                                                      const void* wg, const void* wu, const void* wd, void* out, void* ws) {
  const int bid = blockIdx.x;
  const int wid = __builtin_amdgcn_readfirstlane(threadIdx.x >> 6);
  asm volatile(
    "v_mbcnt_lo_u32_b32 v12, -1, 0\n"
    "v_mbcnt_hi_u32_b32 v12, -1, v12\n"
    "v_lshlrev_b32 v11, 7, v12\n"
    "s_getpc_b64 s[38:39]\n"
    "PCA_%=:\n"
    "s_lshr_b32 s59, %[bid], 3\n"
    "s_lshl2_add_u32 s59, s59, %[wid]\n"
    "s_mulk_i32 s59, 1024\n"
    "v_lshrrev_b32 v17, 3, v12\n"
    "v_lshlrev_b32 v17, 7, v17\n"
    "s_add_u32 s59, s59, PCA_%=-PCA_%=\n"
    "s_min_u32 s59, s59, MAXPASS_%=-PCA_%=-1024\n"
    "s_add_u32 s38, s38, s59\n"
    "s_addc_u32 s39, s39, 0\n"
    "global_load_dword v17, v17, s[38:39]\n"
    "s_mov_b64 s[52:53], %[x]\n"
    "s_mov_b32 s54, 0x280000\n"
    "s_mov_b32 s55, 0x20000\n"
    "s_mov_b64 s[40:41], %[ws]\n"
    "s_mov_b32 s42, 0x5970000\n"
    "s_mov_b32 s43, 0x20000\n"
    "s_mov_b32 s58, %[wid]\n"
    "v_lshrrev_b32 v18, 3, v12\n"
    "v_and_b32 v19, 7, v12\n"
    "v_mul_u32_u24 v20, 0x500, v19\n"
    "v_lshl_add_u32 v20, v18, 4, v20\n"
    "v_and_b32 v78, 32, v12\n"
    "v_lshlrev_b32 v78, 26, v78\n"
    "v_add_u32 v21, v20, v78\n"
    "s_mul_i32 s59, %[bid], 10240\n"
    "s_mul_i32 s60, s58, 320\n"
    "s_add_u32 s59, s59, s60\n"
    "buffer_load_dword v13, off, s[40:43], 0 sc1\n"
    "buffer_load_dwordx4 v[32:35], v20, s[52:55], s59 offen offset:0\n"
    "buffer_load_dwordx4 v[36:39], v20, s[52:55], s59 offen offset:128\n"
    "buffer_load_dwordx4 v[40:43], v21, s[52:55], s59 offen offset:256\n"
    "s_and_b32 s59, %[bid], 7\n"
    "s_lshr_b32 s60, %[bid], 3\n"
    "s_and_b32 s61, s60, 3\n"
    "s_lshl2_add_u32 s56, s59, s61\n"
    "s_lshr_b32 s57, s60, 2\n"
    "v_xor_b32 v20, v19, v18\n"
    "v_lshlrev_b32 v20, 4, v20\n"
    "v_lshl_add_u32 v1, v18, 10, v20\n"
    "v_and_b32 v20, 6, v18\n"
    "v_xor_b32 v20, v19, v20\n"
    "v_lshlrev_b32 v23, 4, v20\n"
    "v_and_b32 v20, 4, v18\n"
    "v_add_u32 v22, v18, v20\n"
    "v_lshl_add_u32 v2, v22, 13, v23\n"
    "s_mov_b64 s[44:45], %[ckv]\n"
    "s_mov_b32 s46, 0x200000\n"
    "s_mov_b32 s47, 0x20000\n"
    "s_lshl_b32 s59, s56, 8\n"
    "s_and_b32 s60, s58, 1\n"
    "s_lshl_b32 s60, s60, 7\n"
    "s_add_u32 s59, s59, s60\n"
    "s_cmp_lt_u32 s58, 2\n"
    "s_cselect_b64 s[48:49], %[wuk], %[wuv]\n"
    "s_add_u32 s48, s48, s59\n"
    "s_addc_u32 s49, s49, 0\n"
    "s_sub_u32 s50, 0x400000, s59\n"
    "s_mov_b32 s51, 0x20000\n"
    "s_lshl_b32 s64, s57, 18\n"
    "s_lshl_b32 s59, s58, 16\n"
    "s_add_u32 s64, s64, s59\n"
    "s_and_b32 s59, %[bid], 7\n"
    "s_lshl_b32 s65, s59, 19\n"
    "s_lshl_b32 s59, s59, 7\n"
    "s_add_u32 s64, s64, s59\n"
    "s_lshl_b32 s80, s58, 13\n"
    "s_add_u32 s66, s80, 0x8000\n"
    "s_add_u32 s78, s80, 0x10000\n"
    "s_add_u32 s79, s80, 0x20000\n"
    "s_add_u32 m0, s78, 0\n"
    "v_mov_b32 v24, 0\n"
    "v_mov_b32 v25, 0\n"
    "v_mov_b32 v26, 0\n"
    "v_mov_b32 v27, 0\n"
    "v_lshlrev_b32 v0, 4, v12\n"
    "s_nop 0\n"
    "v_lshrrev_b32 v21, 2, v12\n"
    "v_and_b32 v20, 3, v12\n"
    "v_lshlrev_b32 v20, 3, v20\n"
    "buffer_load_dwordx4 v2, s[48:51], s65 offen offset:0 nt lds\n"
    "s_add_u32 s65, s65, 0x1fc00\n"
    "v_lshl_add_u32 v20, v21, 7, v20\n"
    "s_and_b32 s59, s58, 1\n"
    "s_lshl_b32 s59, s59, 14\n"
    "s_add_u32 s59, s59, 0x10000\n"
    "v_add_u32 v20, s59, v20\n"
    "v_and_b32 v21, 3, v18\n"
    "v_mfma_f32_32x32x16_bf16 v[80:95], v[24:27], v[24:27], 0\n"
    "v_xor_b32 v6, 0, v21\n"
    "v_lshl_add_u32 v6, v6, 5, v20\n"
    "buffer_load_dwordx4 v2, s[48:51], s65 offen offset:1024 nt lds\n"
    "s_add_u32 s65, s65, 0xfffe7c00\n"
    "v_xor_b32 v7, 1, v21\n"
    "v_lshl_add_u32 v7, v7, 5, v20\n"
    "v_xor_b32 v8, 2, v21\n"
    "v_lshl_add_u32 v8, v8, 5, v20\n"
    "v_xor_b32 v9, 3, v21\n"
    "v_lshl_add_u32 v9, v9, 5, v20\n"
    "v_and_b32 v20, 15, v12\n"
    "v_mfma_f32_32x32x16_bf16 v[96:111], v[24:27], v[24:27], 0\n"
    "v_lshlrev_b32 v20, 7, v20\n"
    "buffer_load_dwordx4 v2, s[48:51], s65 offen offset:2048 nt lds\n"
    "s_add_u32 s65, s65, 0x1fc00\n"
    "s_lshr_b32 s59, s58, 1\n"
    "s_lshl_b32 s59, s59, 14\n"
    "s_add_u32 s59, s59, 0x8000\n"
    "v_add_u32 v20, s59, v20\n"
    "v_lshrrev_b32 v21, 4, v12\n"
    "v_and_b32 v5, 7, v12\n"
    "v_xor_b32 v21, v21, v5\n"
    "v_lshl_add_u32 v4, v21, 4, v20\n"
    "v_mfma_f32_32x32x16_bf16 v[112:127], v[24:27], v[24:27], 0\n"
    "buffer_load_dwordx4 v2, s[48:51], s65 offen offset:3072 nt lds\n"
    "s_add_u32 s65, s65, 0x18c00\n"
    "s_mov_b32 m0, s66\n"
    "v_xor_b32 v5, 64, v4\n"
    "s_lshl_b32 s91, s56, 20\n"
    "s_add_u32 s91, s91, 0x300000\n"
    "s_lshl_b32 s59, s57, 17\n"
    "s_add_u32 s67, s91, s59\n"
    "s_lshr_b32 s59, s58, 1\n"
    "s_lshl_b32 s59, s59, 16\n"
    "s_add_u32 s67, s67, s59\n"
    "s_and_b32 s59, s58, 1\n"
    "buffer_load_dwordx4 v1, s[44:47], s64 offen offset:0 lds\n"
    "s_addk_i32 s64, 0x1c00\n"
    "v_mfma_f32_32x32x16_bf16 v[128:143], v[24:27], v[24:27], 0\n"
    "s_lshl_b32 s59, s59, 13\n"
    "s_add_u32 s67, s67, s59\n"
    "s_lshl2_add_u32 s85, %[bid], s58\n"
    "s_lshl2_add_u32 s85, s85, 0x1000\n"
    "s_lshr_b32 s59, s56, 3\n"
    "s_sub_u32 s77, s56, s59\n"
    "s_add_u32 s59, s59, 28\n"
    "s_and_b32 s60, s56, 7\n"
    "buffer_load_dwordx4 v1, s[44:47], s64 offen offset:1024 lds\n"
    "s_addk_i32 s64, 0x1c00\n"
    "s_cmp_eq_u32 s60, 7\n"
    "v_mfma_f32_32x32x16_bf16 v[144:159], v[24:27], v[24:27], 0\n"
    "s_cselect_b32 s77, s59, s77\n"
    "s_lshl_b32 s59, s56, 8\n"
    "s_lshl_b32 s60, s58, 7\n"
    "s_add_u32 s59, s59, s60\n"
    "s_lshl_b32 s60, s77, 7\n"
    "s_sub_u32 s61, 0x500000, s59\n"
    "s_sub_u32 s62, 0x21c000, s60\n"
    "buffer_load_dwordx4 v1, s[44:47], s64 offen offset:2048 lds\n"
    "s_addk_i32 s64, 0x1c00\n"
    "s_cmp_ge_u32 s77, 27\n"
    "s_cselect_b32 s62, 0, s62\n"
    "v_mfma_f32_32x32x16_bf16 v[160:175], v[24:27], v[24:27], 0\n"
    "s_cselect_b32 s60, 0, s60\n"
    "s_cmp_eq_u32 s58, 2\n"
    "s_cselect_b64 s[52:53], %[wg], %[wu]\n"
    "s_cmp_lt_u32 s58, 2\n"
    "s_cselect_b64 s[52:53], %[wq], s[52:53]\n"
    "s_cselect_b32 s59, s59, s60\n"
    "s_cselect_b32 s54, s61, s62\n"
    "s_movk_i32 s60, 3456\n"
    "s_cselect_b32 s60, 0x2000, s60\n"
    "buffer_load_dwordx4 v1, s[44:47], s64 offen offset:3072 lds\n"
    "s_addk_i32 s64, 0x2c00\n"
    "s_addk_i32 m0, 0x1000\n"
    "v_mfma_f32_32x32x16_bf16 v[176:191], v[24:27], v[24:27], 0\n"
    "s_add_u32 s52, s52, s59\n"
    "s_addc_u32 s53, s53, 0\n"
    "v_mad_u32_u24 v3, v22, s60, v23\n"
    "s_mul_i32 s61, s60, 12\n"
    "s_lshl_b32 s84, s60, 4\n"
    "s_sub_u32 s68, s84, 0x400\n"
    "s_sub_u32 s69, 0xfffffc00, s61\n"
    "s_add_u32 s70, s61, 0xc00\n"
    "buffer_load_dwordx4 v1, s[44:47], s64 offen offset:0 lds\n"
    "s_addk_i32 s64, 0x1c00\n"
    "s_mov_b32 s81, 0\n"
    "v_mfma_f32_32x32x16_bf16 v[192:207], v[24:27], v[24:27], 0\n"
    "s_lshl_b32 s82, s58, 13\n"
    "s_mul_i32 s59, s57, 5\n"
    "s_mul_i32 s83, s59, s84\n"
    "v_lshrrev_b32 v21, 2, v12\n"
    "v_mul_u32_u24 v16, s60, v21\n"
    "v_and_b32 v21, 15, v12\n"
    "s_lshl2_add_u32 s59, s57, s58\n"
    "buffer_load_dwordx4 v1, s[44:47], s64 offen offset:1024 lds\n"
    "s_addk_i32 s64, 0x1c00\n"
    "s_lshl_b32 s75, s59, 1\n"
    "s_sub_u32 s76, 126, s75\n"
    "v_mfma_f32_32x32x16_bf16 v[208:223], v[24:27], v[24:27], 0\n"
    "s_mul_i32 s71, s75, 20480\n"
    "s_add_u32 s71, s71, 0x10000\n"
    "s_add_u32 s72, s71, 20480\n"
    "s_mul_i32 s73, s76, 20480\n"
    "s_add_u32 s73, s73, 0x10000\n"
    "s_add_u32 s74, s73, 20480\n"
    "buffer_load_dwordx4 v1, s[44:47], s64 offen offset:2048 lds\n"
    "s_addk_i32 s64, 0x1c00\n"
    "v_lshrrev_b32 v20, 4, v12\n"
    "v_lshlrev_b32 v20, 2, v20\n"
    "v_cmp_le_u32 s[92:93], v20, v21\n"
    "v_mfma_f32_32x32x16_bf16 v[224:239], v[24:27], v[24:27], 0\n"
    "v_add_u32 v20, 1, v20\n"
    "v_cmp_le_u32 s[94:95], v20, v21\n"
    "v_add_u32 v20, 1, v20\n"
    "v_cmp_le_u32 s[96:97], v20, v21\n"
    "v_add_u32 v20, 1, v20\n"
    "buffer_load_dwordx4 v1, s[44:47], s64 offen offset:3072 lds\n"
    "s_add_u32 s64, s64, 0xffff2c80\n"
    "s_xor_b32 s66, s66, 0x10000\n"
    "s_bitset0_b32 s64, 10\n"
    "s_add_u32 m0, s78, 4096\n"
    "v_cmp_le_u32 s[98:99], v20, v21\n"
    "s_mov_b32 s90, 0\n"
    "s_lshr_b32 s28, s56, 3\n"
    "s_sub_u32 s59, 15, s57\n"
    "v_mfma_f32_32x32x16_bf16 v[240:255], v[24:27], v[24:27], 0\n"
    "s_bitcmp1_b32 %[bid], 0\n"
    "s_cselect_b32 s29, s59, s57\n"
    "s_and_b32 s30, s56, 3\n"
    "s_lshl_b32 s60, s57, 7\n"
    "buffer_load_dwordx4 v2, s[48:51], s65 offen offset:0 nt lds\n"
    "s_add_u32 s65, s65, 0x1fc00\n"
    "s_mul_i32 s31, s28, 28\n"
    "s_add_u32 s31, s31, s60\n"
    "s_addk_i32 s31, 0x3000\n"
    "s_lshl2_add_u32 s59, s57, s28\n"
    "s_lshl_b32 s59, s59, 7\n"
    "v_mfma_f32_32x32x16_bf16 a[128:143], v[24:27], v[24:27], 0\n"
    "s_add_u32 s32, s59, 0x4000\n"
    "s_lshl2_add_u32 s59, %[bid], s58\n"
    "s_mul_i32 s33, s59, 24576\n"
    "buffer_load_dwordx4 v2, s[48:51], s65 offen offset:1024 nt lds\n"
    "s_add_u32 s65, s65, 0xfffe7c00\n"
    "s_add_u32 s33, s33, 0x39c0000\n"
    "s_lshl_b32 s34, s59, 5\n"
    "s_add_u32 s34, s34, 0x8000\n"
    "s_lshr_b32 s59, s58, 1\n"
    "s_lshl1_add_u32 s59, s29, s59\n"
    "s_lshl2_add_u32 s59, s59, s28\n"
    "v_mfma_f32_32x32x16_bf16 a[144:159], v[24:27], v[24:27], 0\n"
    "s_mul_i32 s35, s59, 20480\n"
    "s_and_b32 s59, s58, 1\n"
    "buffer_load_dwordx4 v2, s[48:51], s65 offen offset:2048 nt lds\n"
    "s_add_u32 s65, s65, 0x1fc00\n"
    "s_lshl1_add_u32 s59, s30, s59\n"
    "s_mulk_i32 s59, 160\n"
    "s_add_u32 s35, s35, s59\n"
    "buffer_load_dwordx4 v2, s[48:51], s65 offen offset:3072 nt lds\n"
    "s_add_u32 s65, s65, 0x18c00\n"
    "s_bitset0_b32 s65, 22\n"
    "s_mov_b32 m0, s66\n"
    "s_waitcnt vmcnt(16)\n"
    "v_add_u32 v13, 0x9e3779b1, v13\n"
    "s_mov_b32 s63, 0xffff0000\n"
    "v_lshlrev_b32 v48, 16, v32\n"
    "v_and_b32 v49, s63, v32\n"
    "v_lshlrev_b32 v50, 16, v33\n"
    "v_and_b32 v51, s63, v33\n"
    "v_lshlrev_b32 v52, 16, v34\n"
    "v_and_b32 v53, s63, v34\n"
    "v_lshlrev_b32 v54, 16, v35\n"
    "v_and_b32 v55, s63, v35\n"
    "v_lshlrev_b32 v56, 16, v36\n"
    "buffer_load_dwordx4 v1, s[44:47], s64 offen offset:0 lds\n"
    "s_addk_i32 s64, 0x1c00\n"
    "v_and_b32 v57, s63, v36\n"
    "v_lshlrev_b32 v58, 16, v37\n"
    "v_and_b32 v59, s63, v37\n"
    "v_lshlrev_b32 v60, 16, v38\n"
    "v_and_b32 v61, s63, v38\n"
    "v_lshlrev_b32 v62, 16, v39\n"
    "v_and_b32 v63, s63, v39\n"
    "v_lshlrev_b32 v64, 16, v40\n"
    "v_and_b32 v65, s63, v40\n"
    "v_lshlrev_b32 v66, 16, v41\n"
    "v_and_b32 v67, s63, v41\n"
    "v_lshlrev_b32 v68, 16, v42\n"
    "buffer_load_dwordx4 v1, s[44:47], s64 offen offset:1024 lds\n"
    "s_addk_i32 s64, 0x1c00\n"
    "v_and_b32 v69, s63, v42\n"
    "v_lshlrev_b32 v70, 16, v43\n"
    "v_and_b32 v71, s63, v43\n"
    "v_add_f32 v72, v48, v49\n"
    "v_mul_f32 v73, v48, v48\n"
    "v_add_f32 v74, v50, v51\n"
    "v_mul_f32 v75, v49, v49\n"
    "v_fmac_f32 v73, v50, v50\n"
    "v_fmac_f32 v75, v51, v51\n"
    "v_add_f32 v72, v72, v52\n"
    "v_fmac_f32 v73, v52, v52\n"
    "v_add_f32 v74, v74, v53\n"
    "buffer_load_dwordx4 v1, s[44:47], s64 offen offset:2048 lds\n"
    "s_addk_i32 s64, 0x1c00\n"
    "v_fmac_f32 v75, v53, v53\n"
    "v_add_f32 v72, v72, v54\n"
    "v_fmac_f32 v73, v54, v54\n"
    "v_add_f32 v74, v74, v55\n"
    "v_fmac_f32 v75, v55, v55\n"
    "v_add_f32 v72, v72, v56\n"
    "v_fmac_f32 v73, v56, v56\n"
    "v_add_f32 v74, v74, v57\n"
    "v_fmac_f32 v75, v57, v57\n"
    "v_add_f32 v72, v72, v58\n"
    "v_fmac_f32 v73, v58, v58\n"
    "v_add_f32 v74, v74, v59\n"
    "buffer_load_dwordx4 v1, s[44:47], s64 offen offset:3072 lds\n"
    "s_addk_i32 s64, 0x2c00\n"
    "s_addk_i32 m0, 0x1000\n"
    "v_fmac_f32 v75, v59, v59\n"
    "v_add_f32 v72, v72, v60\n"
    "v_fmac_f32 v73, v60, v60\n"
    "v_add_f32 v74, v74, v61\n"
    "v_fmac_f32 v75, v61, v61\n"
    "v_add_f32 v72, v72, v62\n"
    "v_fmac_f32 v73, v62, v62\n"
    "v_add_f32 v74, v74, v63\n"
    "v_fmac_f32 v75, v63, v63\n"
    "v_add_f32 v72, v72, v64\n"
    "v_fmac_f32 v73, v64, v64\n"
    "v_add_f32 v74, v74, v65\n"
    "buffer_load_dwordx4 v1, s[44:47], s64 offen offset:0 lds\n"
    "s_addk_i32 s64, 0x1c00\n"
    "v_fmac_f32 v75, v65, v65\n"
    "v_add_f32 v72, v72, v66\n"
    "v_fmac_f32 v73, v66, v66\n"
    "v_add_f32 v74, v74, v67\n"
    "v_fmac_f32 v75, v67, v67\n"
    "v_add_f32 v72, v72, v68\n"
    "v_fmac_f32 v73, v68, v68\n"
    "v_add_f32 v74, v74, v69\n"
    "v_fmac_f32 v75, v69, v69\n"
    "v_add_f32 v72, v72, v70\n"
    "v_fmac_f32 v73, v70, v70\n"
    "v_add_f32 v74, v74, v71\n"
    "buffer_load_dwordx4 v1, s[44:47], s64 offen offset:1024 lds\n"
    "s_addk_i32 s64, 0x1c00\n"
    "v_fmac_f32 v75, v71, v71\n"
    "v_add_f32 v72, v72, v74\n"
    "v_add_f32 v73, v73, v75\n"
    "s_nop 1\n"
    "v_add_f32_dpp v74, v72, v72 row_ror:8 row_mask:0xf bank_mask:0xf\n"
    "v_add_f32_dpp v75, v73, v73 row_ror:8 row_mask:0xf bank_mask:0xf\n"
    "v_mov_b32 v72, v74\n"
    "v_mov_b32 v73, v75\n"
    "s_nop 0\n"
    "v_permlane16_swap_b32 v72, v74\n"
    "v_permlane16_swap_b32 v73, v75\n"
    "v_add_f32 v74, v72, v74\n"
    "buffer_load_dwordx4 v1, s[44:47], s64 offen offset:2048 lds\n"
    "s_addk_i32 s64, 0x1c00\n"
    "v_add_f32 v75, v73, v75\n"
    "v_mov_b32 v72, v74\n"
    "v_mov_b32 v73, v75\n"
    "s_nop 0\n"
    "v_permlane32_swap_b32 v72, v74\n"
    "v_permlane32_swap_b32 v73, v75\n"
    "v_add_f32 v74, v72, v74\n"
    "v_add_f32 v75, v73, v75\n"
    "v_and_b32 v20, 3, v18\n"
    "v_lshlrev_b32 v20, 13, v20\n"
    "v_lshlrev_b32 v21, 5, v19\n"
    "s_lshl_b32 s59, s58, 3\n"
    "buffer_load_dwordx4 v1, s[44:47], s64 offen offset:3072 lds\n"
    "s_add_u32 s64, s64, 0xffff2c80\n"
    "s_xor_b32 s66, s66, 0x10000\n"
    "s_bitset0_b32 s64, 10\n"
    "s_add_u32 m0, s79, 0\n"
    "v_add3_u32 v20, v20, v21, s59\n"
    "ds_write_b64 v20, v[74:75]\n"
    "v_add_u32 v21, s82, v21\n"
    "v_lshlrev_b32 v30, 4, v19\n"
    "v_lshl_add_u32 v30, v18, 8, v30\n"
    "v_add_u32 v31, 0x1000, v30\n"
    "v_add_u32 v31, v31, v78\n"
    "s_lshr_b32 s61, %[bid], 1\n"
    "s_mulk_i32 s61, 20480\n"
    "s_and_b32 s59, %[bid], 1\n"
    "s_lshl_b32 s59, s59, 7\n"
    "s_add_u32 s61, s61, s59\n"
    "buffer_load_dwordx4 v2, s[48:51], s65 offen offset:0 nt lds\n"
    "s_add_u32 s65, s65, 0x1fc00\n"
    "s_mul_i32 s59, s58, 5120\n"
    "s_add_u32 s61, s61, s59\n"
    "s_add_u32 s87, s61, 0x10000\n"
    "s_mov_b32 s86, 0xc0000000\n"
    "buffer_load_dwordx4 v2, s[48:51], s65 offen offset:1024 nt lds\n"
    "s_add_u32 s65, s65, 0xfffe7c00\n"
    "s_waitcnt lgkmcnt(0)\n"
    "s_waitcnt vmcnt(10)\n"
    "s_barrier\n"
    "ds_read_b128 v[32:35], v21\n"
    "ds_read_b128 v[36:39], v21 offset:16\n"
    "ds_read_b128 a[32:35], v4 offset:0\n"
    "ds_read_b128 a[36:39], v4 offset:2048\n"
    "ds_read_b64_tr_b16 a[0:1], v6 offset:0\n"
    "ds_read_b64_tr_b16 a[2:3], v6 offset:2048\n"
    "ds_read_b64_tr_b16 a[4:5], v7 offset:0\n"
    "ds_read_b64_tr_b16 a[6:7], v7 offset:2048\n"
    "ds_read_b64_tr_b16 a[8:9], v8 offset:0\n"
    "ds_read_b64_tr_b16 a[10:11], v8 offset:2048\n"
    "ds_read_b64_tr_b16 a[12:13], v9 offset:0\n"
    "ds_read_b64_tr_b16 a[14:15], v9 offset:2048\n"
    "ds_read_b64_tr_b16 a[16:17], v6 offset:8192\n"
    "ds_read_b64_tr_b16 a[18:19], v6 offset:10240\n"
    "ds_read_b64_tr_b16 a[20:21], v7 offset:8192\n"
    "ds_read_b64_tr_b16 a[22:23], v7 offset:10240\n"
    "ds_read_b64_tr_b16 a[24:25], v8 offset:8192\n"
    "ds_read_b64_tr_b16 a[26:27], v8 offset:10240\n"
    "ds_read_b64_tr_b16 a[28:29], v9 offset:8192\n"
    "ds_read_b64_tr_b16 a[30:31], v9 offset:10240\n"
    "ds_read_b128 a[40:43], v4 offset:4096\n"
    "ds_read_b128 a[44:47], v4 offset:6144\n"
    "ds_read_b128 a[48:51], v4 offset:8192\n"
    "ds_read_b128 a[52:55], v4 offset:10240\n"
    "ds_read_b128 a[56:59], v4 offset:12288\n"
    "ds_read_b128 a[60:63], v4 offset:14336\n"
    "s_waitcnt lgkmcnt(15)\n"
    "v_add_f32 v72, v32, v34\n"
    "v_add_f32 v73, v33, v35\n"
    "v_add_f32 v74, v36, v38\n"
    "v_add_f32 v75, v37, v39\n"
    "v_add_f32 v72, v72, v74\n"
    "v_add_f32 v73, v73, v75\n"
    "v_mul_f32 v72, 0x3acccccd, v72\n"
    "v_mul_f32 v73, 0x3acccccd, v73\n"
    "v_fma_f32 v73, -v72, v72, v73\n"
    "v_max_f32 v73, 0, v73\n"
    "buffer_load_dwordx4 v2, s[48:51], s65 offen offset:2048 nt lds\n"
    "s_add_u32 s65, s65, 0x1fc00\n"
    "v_cvt_pk_bf16_f32 v72, v72, v72\n"
    "v_lshlrev_b32 v72, 16, v72\n"
    "v_cvt_pk_bf16_f32 v73, v73, v73\n"
    "v_lshlrev_b32 v73, 16, v73\n"
    "v_add_f32 v73, 0x3727c5ac, v73\n"
    "v_cvt_pk_bf16_f32 v73, v73, v73\n"
    "v_lshlrev_b32 v73, 16, v73\n"
    "v_sqrt_f32 v73, v73\n"
    "s_nop 0\n"
    "v_cvt_pk_bf16_f32 v73, v73, v73\n"
    "buffer_load_dwordx4 v2, s[48:51], s65 offen offset:3072 nt lds\n"
    "s_add_u32 s65, s65, 0x18c00\n"
    "s_add_u32 m0, s79, 4096\n"
    "v_lshlrev_b32 v73, 16, v73\n"
    "v_rcp_f32 v73, v73\n"
    "s_nop 0\n"
    "v_mul_f32 v73, 0x3e0293ee, v73\n"
    "v_mul_f32 v72, v72, v73\n"
    "v_mfma_f32_32x32x16_bf16 v[32:47], v[24:27], v[24:27], 0\n"
    "v_fma_f32 v48, v48, v73, -v72\n"
    "v_fma_f32 v49, v49, v73, -v72\n"
    "v_fma_f32 v50, v50, v73, -v72\n"
    "v_fma_f32 v51, v51, v73, -v72\n"
    "buffer_load_dwordx4 v2, s[48:51], s65 offen offset:0 nt lds\n"
    "s_add_u32 s65, s65, 0x1fc00\n"
    "v_fma_f32 v52, v52, v73, -v72\n"
    "v_fma_f32 v53, v53, v73, -v72\n"
    "v_fma_f32 v54, v54, v73, -v72\n"
    "v_fma_f32 v55, v55, v73, -v72\n"
    "v_fma_f32 v56, v56, v73, -v72\n"
    "v_fma_f32 v57, v57, v73, -v72\n"
    "v_fma_f32 v58, v58, v73, -v72\n"
    "v_fma_f32 v59, v59, v73, -v72\n"
    "v_fma_f32 v60, v60, v73, -v72\n"
    "v_fma_f32 v61, v61, v73, -v72\n"
    "buffer_load_dwordx4 v2, s[48:51], s65 offen offset:1024 nt lds\n"
    "s_add_u32 s65, s65, 0xfffe7c00\n"
    "v_fma_f32 v62, v62, v73, -v72\n"
    "v_fma_f32 v63, v63, v73, -v72\n"
    "v_fma_f32 v64, v64, v73, -v72\n"
    "v_fma_f32 v65, v65, v73, -v72\n"
    "v_fma_f32 v66, v66, v73, -v72\n"
    "v_fma_f32 v67, v67, v73, -v72\n"
    "v_fma_f32 v68, v68, v73, -v72\n"
    "v_fma_f32 v69, v69, v73, -v72\n"
    "v_fma_f32 v70, v70, v73, -v72\n"
    "v_fma_f32 v71, v71, v73, -v72\n"
    "buffer_load_dwordx4 v2, s[48:51], s65 offen offset:2048 nt lds\n"
    "s_add_u32 s65, s65, 0x1fc00\n"
    "v_cvt_pk_bf16_f32 v18, v48, v49\n"
    "v_cvt_pk_bf16_f32 v19, v50, v51\n"
    "v_cvt_pk_bf16_f32 v20, v52, v53\n"
    "v_cvt_pk_bf16_f32 v21, v54, v55\n"
    "v_cvt_pk_bf16_f32 v22, v56, v57\n"
    "v_cvt_pk_bf16_f32 v23, v58, v59\n"
    "v_cvt_pk_bf16_f32 v24, v60, v61\n"
    "v_cvt_pk_bf16_f32 v25, v62, v63\n"
    "v_cvt_pk_bf16_f32 v26, v64, v65\n"
    "v_cvt_pk_bf16_f32 v27, v66, v67\n"
    "buffer_load_dwordx4 v2, s[48:51], s65 offen offset:3072 nt lds\n"
    "s_add_u32 s65, s65, 0x18c00\n"
    "s_bitset0_b32 s65, 22\n"
    "v_cvt_pk_bf16_f32 v28, v68, v69\n"
    "v_cvt_pk_bf16_f32 v29, v70, v71\n"
    "v_mfma_f32_32x32x16_bf16 v[48:63], v[80:83], v[80:83], 0\n"
    "v_mfma_f32_32x32x16_bf16 v[64:79], v[80:83], v[80:83], 0\n"
    "buffer_load_dword v17, off, s[40:43], s86\n"
    "s_mov_b32 s63, 5\n"
    "s_waitcnt lgkmcnt(0)\n"
    "s_bitcmp1_b32 s58, 0\n"
    "s_cbranch_scc1 KV_LOOP1_%=\n"
    "KV_LOOP0_%=:\n"
    "v_mfma_f32_16x16x32_bf16 v[128:131], a[0:3], a[32:35], v[128:131]\n"
    "s_add_u32 m0, s80, 0\n"
    "ds_read_b128 a[96:99], v5 offset:0\n"
    "v_mfma_f32_16x16x32_bf16 v[132:135], a[4:7], a[32:35], v[132:135]\n"
    "v_mfma_f32_16x16x32_bf16 v[136:139], a[8:11], a[32:35], v[136:139]\n"
    "buffer_load_dwordx4 v2, s[48:51], s65 offen offset:0 nt lds\n"
    "v_mfma_f32_16x16x32_bf16 v[140:143], a[12:15], a[32:35], v[140:143]\n"
    "s_add_u32 s65, s65, 0x1fc00\n"
    "ds_read_b128 a[100:103], v5 offset:2048\n"
    "v_mfma_f32_16x16x32_bf16 v[144:147], a[16:19], a[32:35], v[144:147]\n"
    "v_mfma_f32_16x16x32_bf16 v[148:151], a[20:23], a[32:35], v[148:151]\n"
    "ds_read_b64_tr_b16 a[64:65], v6 offset:4096\n"
    "v_mfma_f32_16x16x32_bf16 v[152:155], a[24:27], a[32:35], v[152:155]\n"
    "v_mfma_f32_16x16x32_bf16 v[156:159], a[28:31], a[32:35], v[156:159]\n"
    "ds_read_b64_tr_b16 a[66:67], v6 offset:6144\n"
    "v_mfma_f32_16x16x32_bf16 v[160:163], a[0:3], a[36:39], v[160:163]\n"
    "buffer_load_dwordx4 v2, s[48:51], s65 offen offset:1024 nt lds\n"
    "v_mfma_f32_16x16x32_bf16 v[164:167], a[4:7], a[36:39], v[164:167]\n"
    "s_add_u32 s65, s65, 0xfffe7c00\n"
    "ds_read_b64_tr_b16 a[68:69], v7 offset:4096\n"
    "v_mfma_f32_16x16x32_bf16 v[168:171], a[8:11], a[36:39], v[168:171]\n"
    "s_sub_u32 s60, s79, s78\n"
    "v_mfma_f32_16x16x32_bf16 v[172:175], a[12:15], a[36:39], v[172:175]\n"
    "ds_read_b64_tr_b16 a[70:71], v7 offset:6144\n"
    "v_mfma_f32_16x16x32_bf16 v[176:179], a[16:19], a[36:39], v[176:179]\n"
    "v_mfma_f32_16x16x32_bf16 v[180:183], a[20:23], a[36:39], v[180:183]\n"
    "ds_read_b64_tr_b16 a[72:73], v8 offset:4096\n"
    "v_mfma_f32_16x16x32_bf16 v[184:187], a[24:27], a[36:39], v[184:187]\n"
    "buffer_load_dwordx4 v2, s[48:51], s65 offen offset:2048 nt lds\n"
    "v_mfma_f32_16x16x32_bf16 v[188:191], a[28:31], a[36:39], v[188:191]\n"
    "s_add_u32 s65, s65, 0x1fc00\n"
    "ds_read_b64_tr_b16 a[74:75], v8 offset:6144\n"
    "v_mfma_f32_16x16x32_bf16 v[192:195], a[0:3], a[40:43], v[192:195]\n"
    "v_mfma_f32_16x16x32_bf16 v[196:199], a[4:7], a[40:43], v[196:199]\n"
    "ds_read_b64_tr_b16 a[76:77], v9 offset:4096\n"
    "v_mfma_f32_16x16x32_bf16 v[200:203], a[8:11], a[40:43], v[200:203]\n"
    "v_mfma_f32_16x16x32_bf16 v[204:207], a[12:15], a[40:43], v[204:207]\n"
    "ds_read_b64_tr_b16 a[78:79], v9 offset:6144\n"
    "v_mfma_f32_16x16x32_bf16 v[208:211], a[16:19], a[40:43], v[208:211]\n"
    "buffer_load_dwordx4 v2, s[48:51], s65 offen offset:3072 nt lds\n"
    "v_mfma_f32_16x16x32_bf16 v[212:215], a[20:23], a[40:43], v[212:215]\n"
    "s_add_u32 s65, s65, 0x18c00\n"
    "ds_read_b64_tr_b16 a[80:81], v6 offset:12288\n"
    "v_mfma_f32_16x16x32_bf16 v[216:219], a[24:27], a[40:43], v[216:219]\n"
    "s_add_u32 m0, s80, 4096\n"
    "v_mfma_f32_16x16x32_bf16 v[220:223], a[28:31], a[40:43], v[220:223]\n"
    "ds_read_b64_tr_b16 a[82:83], v6 offset:14336\n"
    "v_mfma_f32_16x16x32_bf16 v[224:227], a[0:3], a[44:47], v[224:227]\n"
    "v_mfma_f32_16x16x32_bf16 v[228:231], a[4:7], a[44:47], v[228:231]\n"
    "ds_read_b64_tr_b16 a[84:85], v7 offset:12288\n"
    "v_mfma_f32_16x16x32_bf16 v[232:235], a[8:11], a[44:47], v[232:235]\n"
    "buffer_load_dwordx4 v2, s[48:51], s65 offen offset:0 nt lds\n"
    "v_mfma_f32_16x16x32_bf16 v[236:239], a[12:15], a[44:47], v[236:239]\n"
    "s_add_u32 s65, s65, 0x1fc00\n"
    "ds_read_b64_tr_b16 a[86:87], v7 offset:14336\n"
    "v_mfma_f32_16x16x32_bf16 v[240:243], a[16:19], a[44:47], v[240:243]\n"
    "v_mfma_f32_16x16x32_bf16 v[244:247], a[20:23], a[44:47], v[244:247]\n"
    "ds_read_b64_tr_b16 a[88:89], v8 offset:12288\n"
    "v_mfma_f32_16x16x32_bf16 v[248:251], a[24:27], a[44:47], v[248:251]\n"
    "v_mfma_f32_16x16x32_bf16 v[252:255], a[28:31], a[44:47], v[252:255]\n"
    "ds_read_b64_tr_b16 a[90:91], v8 offset:14336\n"
    "v_mfma_f32_16x16x32_bf16 v[32:35], a[0:3], a[48:51], v[32:35]\n"
    "buffer_load_dwordx4 v2, s[48:51], s65 offen offset:1024 nt lds\n"
    "v_mfma_f32_16x16x32_bf16 v[36:39], a[4:7], a[48:51], v[36:39]\n"
    "s_add_u32 s65, s65, 0xfffe7c00\n"
    "ds_read_b64_tr_b16 a[92:93], v9 offset:12288\n"
    "v_mfma_f32_16x16x32_bf16 v[40:43], a[8:11], a[48:51], v[40:43]\n"
    "v_mfma_f32_16x16x32_bf16 v[44:47], a[12:15], a[48:51], v[44:47]\n"
    "ds_read_b64_tr_b16 a[94:95], v9 offset:14336\n"
    "v_mfma_f32_16x16x32_bf16 v[48:51], a[16:19], a[48:51], v[48:51]\n"
    "v_mfma_f32_16x16x32_bf16 v[52:55], a[20:23], a[48:51], v[52:55]\n"
    "ds_read_b128 a[104:107], v5 offset:4096\n"
    "v_mfma_f32_16x16x32_bf16 v[56:59], a[24:27], a[48:51], v[56:59]\n"
    "buffer_load_dwordx4 v2, s[48:51], s65 offen offset:2048 nt lds\n"
    "v_mfma_f32_16x16x32_bf16 v[60:63], a[28:31], a[48:51], v[60:63]\n"
    "s_add_u32 s65, s65, 0x1fc00\n"
    "ds_read_b128 a[108:111], v5 offset:6144\n"
    "v_mfma_f32_16x16x32_bf16 v[64:67], a[0:3], a[52:55], v[64:67]\n"
    "v_mfma_f32_16x16x32_bf16 v[68:71], a[4:7], a[52:55], v[68:71]\n"
    "ds_read_b128 a[112:115], v5 offset:8192\n"
    "v_mfma_f32_16x16x32_bf16 v[72:75], a[8:11], a[52:55], v[72:75]\n"
    "v_mfma_f32_16x16x32_bf16 v[76:79], a[12:15], a[52:55], v[76:79]\n"
    "ds_read_b128 a[116:119], v5 offset:10240\n"
    "v_mfma_f32_16x16x32_bf16 v[80:83], a[16:19], a[52:55], v[80:83]\n"
    "buffer_load_dwordx4 v2, s[48:51], s65 offen offset:3072 nt lds\n"
    "v_mfma_f32_16x16x32_bf16 v[84:87], a[20:23], a[52:55], v[84:87]\n"
    "s_add_u32 s65, s65, 0x18c00\n"
    "ds_read_b128 a[120:123], v5 offset:12288\n"
    "v_mfma_f32_16x16x32_bf16 v[88:91], a[24:27], a[52:55], v[88:91]\n"
    "s_bitset0_b32 s65, 22\n"
    "v_mfma_f32_16x16x32_bf16 v[92:95], a[28:31], a[52:55], v[92:95]\n"
    "s_mov_b32 s59, s78\n"
    "ds_read_b128 a[124:127], v5 offset:14336\n"
    "v_mfma_f32_16x16x32_bf16 v[96:99], a[0:3], a[56:59], v[96:99]\n"
    "s_mov_b32 s78, s79\n"
    "v_mfma_f32_16x16x32_bf16 v[100:103], a[4:7], a[56:59], v[100:103]\n"
    "s_mov_b32 s79, s80\n"
    "v_xor_b32 v4, 0x10000, v4\n"
    "v_xor_b32 v5, 0x10000, v5\n"
    "v_mfma_f32_16x16x32_bf16 v[104:107], a[8:11], a[56:59], v[104:107]\n"
    "s_mov_b32 s80, s59\n"
    "v_add_u32 v6, s60, v6\n"
    "v_add_u32 v7, s60, v7\n"
    "v_mfma_f32_16x16x32_bf16 v[108:111], a[12:15], a[56:59], v[108:111]\n"
    "v_add_u32 v8, s60, v8\n"
    "v_add_u32 v9, s60, v9\n"
    "v_mfma_f32_16x16x32_bf16 v[112:115], a[16:19], a[56:59], v[112:115]\n"
    "v_mfma_f32_16x16x32_bf16 v[116:119], a[20:23], a[56:59], v[116:119]\n"
    "v_mfma_f32_16x16x32_bf16 v[120:123], a[24:27], a[56:59], v[120:123]\n"
    "v_mfma_f32_16x16x32_bf16 v[124:127], a[28:31], a[56:59], v[124:127]\n"
    "v_mfma_f32_16x16x32_bf16 a[128:131], a[0:3], a[60:63], a[128:131]\n"
    "v_mfma_f32_16x16x32_bf16 a[132:135], a[4:7], a[60:63], a[132:135]\n"
    "v_mfma_f32_16x16x32_bf16 a[136:139], a[8:11], a[60:63], a[136:139]\n"
    "v_mfma_f32_16x16x32_bf16 a[140:143], a[12:15], a[60:63], a[140:143]\n"
    "v_mfma_f32_16x16x32_bf16 a[144:147], a[16:19], a[60:63], a[144:147]\n"
    "s_waitcnt lgkmcnt(0)\n"
    "v_mfma_f32_16x16x32_bf16 a[148:151], a[20:23], a[60:63], a[148:151]\n"
    "s_waitcnt vmcnt(9)\n"
    "v_mfma_f32_16x16x32_bf16 a[152:155], a[24:27], a[60:63], a[152:155]\n"
    "s_barrier\n"
    "v_mfma_f32_16x16x32_bf16 a[156:159], a[28:31], a[60:63], a[156:159]\n"
    "KV_S1_0_%=:\n"
    "v_mfma_f32_16x16x32_bf16 v[128:131], a[64:67], a[96:99], v[128:131]\n"
    "ds_read_b128 a[32:35], v4 offset:0\n"
    "v_mfma_f32_16x16x32_bf16 v[132:135], a[68:71], a[96:99], v[132:135]\n"
    "v_mfma_f32_16x16x32_bf16 v[136:139], a[72:75], a[96:99], v[136:139]\n"
    "buffer_store_dword v13, off, s[40:43], s86 offset:0 sc1\n"
    "v_mfma_f32_16x16x32_bf16 v[140:143], a[76:79], a[96:99], v[140:143]\n"
    "s_mov_b32 s86, s85\n"
    "ds_read_b128 a[36:39], v4 offset:2048\n"
    "v_mfma_f32_16x16x32_bf16 v[144:147], a[80:83], a[96:99], v[144:147]\n"
    "s_mov_b32 s85, 0xc0000000\n"
    "v_mfma_f32_16x16x32_bf16 v[148:151], a[84:87], a[96:99], v[148:151]\n"
    "ds_read_b64_tr_b16 a[0:1], v6 offset:0\n"
    "v_mfma_f32_16x16x32_bf16 v[152:155], a[88:91], a[96:99], v[152:155]\n"
    "buffer_store_dwordx4 v[18:21], v30, s[40:43], s87 offen offset:0 sc1\n"
    "v_mfma_f32_16x16x32_bf16 v[156:159], a[92:95], a[96:99], v[156:159]\n"
    "ds_read_b64_tr_b16 a[2:3], v6 offset:2048\n"
    "v_mfma_f32_16x16x32_bf16 v[160:163], a[64:67], a[100:103], v[160:163]\n"
    "v_mfma_f32_16x16x32_bf16 v[164:167], a[68:71], a[100:103], v[164:167]\n"
    "ds_read_b64_tr_b16 a[4:5], v7 offset:0\n"
    "v_mfma_f32_16x16x32_bf16 v[168:171], a[72:75], a[100:103], v[168:171]\n"
    "buffer_store_dwordx4 v[22:25], v30, s[40:43], s87 offen offset:2048 sc1\n"
    "v_mfma_f32_16x16x32_bf16 v[172:175], a[76:79], a[100:103], v[172:175]\n"
    "ds_read_b64_tr_b16 a[6:7], v7 offset:2048\n"
    "v_mfma_f32_16x16x32_bf16 v[176:179], a[80:83], a[100:103], v[176:179]\n"
    "v_mfma_f32_16x16x32_bf16 v[180:183], a[84:87], a[100:103], v[180:183]\n"
    "ds_read_b64_tr_b16 a[8:9], v8 offset:0\n"
    "v_mfma_f32_16x16x32_bf16 v[184:187], a[88:91], a[100:103], v[184:187]\n"
    "buffer_store_dwordx4 v[26:29], v31, s[40:43], s87 offen offset:0 sc1\n"
    "v_mfma_f32_16x16x32_bf16 v[188:191], a[92:95], a[100:103], v[188:191]\n"
    "s_mov_b32 s87, 0xc0000000\n"
    "ds_read_b64_tr_b16 a[10:11], v8 offset:2048\n"
    "v_mfma_f32_16x16x32_bf16 v[192:195], a[64:67], a[104:107], v[192:195]\n"
    "s_mov_b32 m0, s66\n"
    "v_mfma_f32_16x16x32_bf16 v[196:199], a[68:71], a[104:107], v[196:199]\n"
    "ds_read_b64_tr_b16 a[12:13], v9 offset:0\n"
    "v_mfma_f32_16x16x32_bf16 v[200:203], a[72:75], a[104:107], v[200:203]\n"
    "buffer_load_dwordx4 v1, s[44:47], s64 offen offset:0 lds\n"
    "v_mfma_f32_16x16x32_bf16 v[204:207], a[76:79], a[104:107], v[204:207]\n"
    "s_addk_i32 s64, 0x1c00\n"
    "ds_read_b64_tr_b16 a[14:15], v9 offset:2048\n"
    "v_mfma_f32_16x16x32_bf16 v[208:211], a[80:83], a[104:107], v[208:211]\n"
    "v_mfma_f32_16x16x32_bf16 v[212:215], a[84:87], a[104:107], v[212:215]\n"
    "ds_read_b64_tr_b16 a[16:17], v6 offset:8192\n"
    "v_mfma_f32_16x16x32_bf16 v[216:219], a[88:91], a[104:107], v[216:219]\n"
    "buffer_load_dwordx4 v1, s[44:47], s64 offen offset:1024 lds\n"
    "v_mfma_f32_16x16x32_bf16 v[220:223], a[92:95], a[104:107], v[220:223]\n"
    "s_addk_i32 s64, 0x1c00\n"
    "ds_read_b64_tr_b16 a[18:19], v6 offset:10240\n"
    "v_mfma_f32_16x16x32_bf16 v[224:227], a[64:67], a[108:111], v[224:227]\n"
    "v_mfma_f32_16x16x32_bf16 v[228:231], a[68:71], a[108:111], v[228:231]\n"
    "ds_read_b64_tr_b16 a[20:21], v7 offset:8192\n"
    "v_mfma_f32_16x16x32_bf16 v[232:235], a[72:75], a[108:111], v[232:235]\n"
    "buffer_load_dwordx4 v1, s[44:47], s64 offen offset:2048 lds\n"
    "v_mfma_f32_16x16x32_bf16 v[236:239], a[76:79], a[108:111], v[236:239]\n"
    "s_addk_i32 s64, 0x1c00\n"
    "ds_read_b64_tr_b16 a[22:23], v7 offset:10240\n"
    "v_mfma_f32_16x16x32_bf16 v[240:243], a[80:83], a[108:111], v[240:243]\n"
    "v_mfma_f32_16x16x32_bf16 v[244:247], a[84:87], a[108:111], v[244:247]\n"
    "ds_read_b64_tr_b16 a[24:25], v8 offset:8192\n"
    "v_mfma_f32_16x16x32_bf16 v[248:251], a[88:91], a[108:111], v[248:251]\n"
    "buffer_load_dwordx4 v1, s[44:47], s64 offen offset:3072 lds\n"
    "v_mfma_f32_16x16x32_bf16 v[252:255], a[92:95], a[108:111], v[252:255]\n"
    "s_addk_i32 s64, 0x2c00\n"
    "ds_read_b64_tr_b16 a[26:27], v8 offset:10240\n"
    "v_mfma_f32_16x16x32_bf16 v[32:35], a[64:67], a[112:115], v[32:35]\n"
    "s_addk_i32 m0, 0x1000\n"
    "v_mfma_f32_16x16x32_bf16 v[36:39], a[68:71], a[112:115], v[36:39]\n"
    "ds_read_b64_tr_b16 a[28:29], v9 offset:8192\n"
    "v_mfma_f32_16x16x32_bf16 v[40:43], a[72:75], a[112:115], v[40:43]\n"
    "buffer_load_dwordx4 v1, s[44:47], s64 offen offset:0 lds\n"
    "v_mfma_f32_16x16x32_bf16 v[44:47], a[76:79], a[112:115], v[44:47]\n"
    "s_addk_i32 s64, 0x1c00\n"
    "ds_read_b64_tr_b16 a[30:31], v9 offset:10240\n"
    "v_mfma_f32_16x16x32_bf16 v[48:51], a[80:83], a[112:115], v[48:51]\n"
    "v_mfma_f32_16x16x32_bf16 v[52:55], a[84:87], a[112:115], v[52:55]\n"
    "ds_read_b128 a[40:43], v4 offset:4096\n"
    "v_mfma_f32_16x16x32_bf16 v[56:59], a[88:91], a[112:115], v[56:59]\n"
    "buffer_load_dwordx4 v1, s[44:47], s64 offen offset:1024 lds\n"
    "v_mfma_f32_16x16x32_bf16 v[60:63], a[92:95], a[112:115], v[60:63]\n"
    "s_addk_i32 s64, 0x1c00\n"
    "ds_read_b128 a[44:47], v4 offset:6144\n"
    "v_mfma_f32_16x16x32_bf16 v[64:67], a[64:67], a[116:119], v[64:67]\n"
    "v_mfma_f32_16x16x32_bf16 v[68:71], a[68:71], a[116:119], v[68:71]\n"
    "ds_read_b128 a[48:51], v4 offset:8192\n"
    "v_mfma_f32_16x16x32_bf16 v[72:75], a[72:75], a[116:119], v[72:75]\n"
    "buffer_load_dwordx4 v1, s[44:47], s64 offen offset:2048 lds\n"
    "v_mfma_f32_16x16x32_bf16 v[76:79], a[76:79], a[116:119], v[76:79]\n"
    "s_addk_i32 s64, 0x1c00\n"
    "ds_read_b128 a[52:55], v4 offset:10240\n"
    "v_mfma_f32_16x16x32_bf16 v[80:83], a[80:83], a[116:119], v[80:83]\n"
    "v_mfma_f32_16x16x32_bf16 v[84:87], a[84:87], a[116:119], v[84:87]\n"
    "ds_read_b128 a[56:59], v4 offset:12288\n"
    "v_mfma_f32_16x16x32_bf16 v[88:91], a[88:91], a[116:119], v[88:91]\n"
    "buffer_load_dwordx4 v1, s[44:47], s64 offen offset:3072 lds\n"
    "v_mfma_f32_16x16x32_bf16 v[92:95], a[92:95], a[116:119], v[92:95]\n"
    "s_add_u32 s64, s64, 0xffff2c80\n"
    "ds_read_b128 a[60:63], v4 offset:14336\n"
    "v_mfma_f32_16x16x32_bf16 v[96:99], a[64:67], a[120:123], v[96:99]\n"
    "s_xor_b32 s66, s66, 0x10000\n"
    "v_mfma_f32_16x16x32_bf16 v[100:103], a[68:71], a[120:123], v[100:103]\n"
    "s_bitset0_b32 s64, 10\n"
    "v_mfma_f32_16x16x32_bf16 v[104:107], a[72:75], a[120:123], v[104:107]\n"
    "v_mfma_f32_16x16x32_bf16 v[108:111], a[76:79], a[120:123], v[108:111]\n"
    "v_mfma_f32_16x16x32_bf16 v[112:115], a[80:83], a[120:123], v[112:115]\n"
    "v_mfma_f32_16x16x32_bf16 v[116:119], a[84:87], a[120:123], v[116:119]\n"
    "buffer_load_dword v17, v16, s[52:55], s83 offen\n"
    "v_mfma_f32_16x16x32_bf16 v[120:123], a[88:91], a[120:123], v[120:123]\n"
    "s_add_u32 s83, s83, s84\n"
    "v_mfma_f32_16x16x32_bf16 v[124:127], a[92:95], a[120:123], v[124:127]\n"
    "v_mfma_f32_16x16x32_bf16 a[128:131], a[64:67], a[124:127], a[128:131]\n"
    "v_mfma_f32_16x16x32_bf16 a[132:135], a[68:71], a[124:127], a[132:135]\n"
    "v_mfma_f32_16x16x32_bf16 a[136:139], a[72:75], a[124:127], a[136:139]\n"
    "v_mfma_f32_16x16x32_bf16 a[140:143], a[76:79], a[124:127], a[140:143]\n"
    "v_mfma_f32_16x16x32_bf16 a[144:147], a[80:83], a[124:127], a[144:147]\n"
    "v_mfma_f32_16x16x32_bf16 a[148:151], a[84:87], a[124:127], a[148:151]\n"
    "s_waitcnt lgkmcnt(0)\n"
    "v_mfma_f32_16x16x32_bf16 a[152:155], a[88:91], a[124:127], a[152:155]\n"
    "v_mfma_f32_16x16x32_bf16 a[156:159], a[92:95], a[124:127], a[156:159]\n"
    "s_sub_u32 s63, s63, 1\n"
    "s_cbranch_scc0 KV_LOOP0_%=\n"
    "s_getpc_b64 s[38:39]\n"
    "PCE0_%=:\n"
    "s_lshr_b32 s59, %[bid], 3\n"
    "s_lshl2_add_u32 s59, s59, %[wid]\n"
    "s_mulk_i32 s59, 256\n"
    "v_lshrrev_b32 v19, 5, v12\n"
    "v_lshlrev_b32 v19, 7, v19\n"
    "s_add_u32 s59, s59, KV_DONE_%=-PCE0_%=\n"
    "s_min_u32 s59, s59, KVF_OK_%=-PCE0_%=-256\n"
    "s_add_u32 s38, s38, s59\n"
    "s_addc_u32 s39, s39, 0\n"
    "global_load_dword v19, v19, s[38:39]\n"
    "v_mfma_f32_16x16x32_bf16 v[128:131], a[0:3], a[32:35], v[128:131]\n"
    "s_add_u32 m0, s82, 0\n"
    "ds_read_b128 a[96:99], v5 offset:0\n"
    "v_mfma_f32_16x16x32_bf16 v[132:135], a[4:7], a[32:35], v[132:135]\n"
    "v_mfma_f32_16x16x32_bf16 v[136:139], a[8:11], a[32:35], v[136:139]\n"
    "buffer_load_dwordx4 v3, s[52:55], s81 offen offset:0 lds\n"
    "v_mfma_f32_16x16x32_bf16 v[140:143], a[12:15], a[32:35], v[140:143]\n"
    "s_add_u32 s81, s81, s68\n"
    "ds_read_b128 a[100:103], v5 offset:2048\n"
    "v_mfma_f32_16x16x32_bf16 v[144:147], a[16:19], a[32:35], v[144:147]\n"
    "v_mfma_f32_16x16x32_bf16 v[148:151], a[20:23], a[32:35], v[148:151]\n"
    "ds_read_b64_tr_b16 a[64:65], v6 offset:4096\n"
    "v_mfma_f32_16x16x32_bf16 v[152:155], a[24:27], a[32:35], v[152:155]\n"
    "v_mfma_f32_16x16x32_bf16 v[156:159], a[28:31], a[32:35], v[156:159]\n"
    "ds_read_b64_tr_b16 a[66:67], v6 offset:6144\n"
    "v_mfma_f32_16x16x32_bf16 v[160:163], a[0:3], a[36:39], v[160:163]\n"
    "buffer_load_dwordx4 v3, s[52:55], s81 offen offset:1024 lds\n"
    "v_mfma_f32_16x16x32_bf16 v[164:167], a[4:7], a[36:39], v[164:167]\n"
    "s_add_u32 s81, s81, s69\n"
    "ds_read_b64_tr_b16 a[68:69], v7 offset:4096\n"
    "v_mfma_f32_16x16x32_bf16 v[168:171], a[8:11], a[36:39], v[168:171]\n"
    "s_sub_u32 s60, s79, s78\n"
    "v_mfma_f32_16x16x32_bf16 v[172:175], a[12:15], a[36:39], v[172:175]\n"
    "ds_read_b64_tr_b16 a[70:71], v7 offset:6144\n"
    "v_mfma_f32_16x16x32_bf16 v[176:179], a[16:19], a[36:39], v[176:179]\n"
    "s_lshl_b32 s62, s75, 5\n"
    "v_mfma_f32_16x16x32_bf16 v[180:183], a[20:23], a[36:39], v[180:183]\n"
    "s_sub_u32 s61, s76, s75\n"
    "ds_read_b64_tr_b16 a[72:73], v8 offset:4096\n"
    "v_mfma_f32_16x16x32_bf16 v[184:187], a[24:27], a[36:39], v[184:187]\n"
    "buffer_load_dwordx4 v3, s[52:55], s81 offen offset:2048 lds\n"
    "v_mfma_f32_16x16x32_bf16 v[188:191], a[28:31], a[36:39], v[188:191]\n"
    "s_add_u32 s81, s81, s68\n"
    "ds_read_b64_tr_b16 a[74:75], v8 offset:6144\n"
    "v_mfma_f32_16x16x32_bf16 v[192:195], a[0:3], a[40:43], v[192:195]\n"
    "s_lshl_b32 s61, s61, 1\n"
    "v_mfma_f32_16x16x32_bf16 v[196:199], a[4:7], a[40:43], v[196:199]\n"
    "s_addk_i32 s62, 0x1000\n"
    "ds_read_b64_tr_b16 a[76:77], v9 offset:4096\n"
    "v_lshlrev_b32 v11, 7, v12\n"
    "v_mfma_f32_16x16x32_bf16 v[200:203], a[8:11], a[40:43], v[200:203]\n"
    "v_and_b32 v18, 16, v12\n"
    "v_mul_u32_u24 v18, s61, v18\n"
    "v_mfma_f32_16x16x32_bf16 v[204:207], a[12:15], a[40:43], v[204:207]\n"
    "ds_read_b64_tr_b16 a[78:79], v9 offset:6144\n"
    "v_and_b32 v10, 15, v12\n"
    "v_mfma_f32_16x16x32_bf16 v[208:211], a[16:19], a[40:43], v[208:211]\n"
    "buffer_load_dwordx4 v3, s[52:55], s81 offen offset:3072 lds\n"
    "v_mfma_f32_16x16x32_bf16 v[212:215], a[20:23], a[40:43], v[212:215]\n"
    "s_add_u32 s81, s81, s70\n"
    "ds_read_b64_tr_b16 a[80:81], v6 offset:12288\n"
    "v_lshl_add_u32 v10, v10, 2, v18\n"
    "v_mfma_f32_16x16x32_bf16 v[216:219], a[24:27], a[40:43], v[216:219]\n"
    "s_addk_i32 m0, 0x1000\n"
    "v_mfma_f32_16x16x32_bf16 v[220:223], a[28:31], a[40:43], v[220:223]\n"
    "ds_read_b64_tr_b16 a[82:83], v6 offset:14336\n"
    "v_mfma_f32_16x16x32_bf16 v[224:227], a[0:3], a[44:47], v[224:227]\n"
    "buffer_load_dword v1, v10, s[40:43], s62 offen offset:0 sc1\n"
    "v_mfma_f32_16x16x32_bf16 v[228:231], a[4:7], a[44:47], v[228:231]\n"
    "ds_read_b64_tr_b16 a[84:85], v7 offset:12288\n"
    "v_mfma_f32_16x16x32_bf16 v[232:235], a[8:11], a[44:47], v[232:235]\n"
    "buffer_load_dwordx4 v3, s[52:55], s81 offen offset:0 lds\n"
    "v_mfma_f32_16x16x32_bf16 v[236:239], a[12:15], a[44:47], v[236:239]\n"
    "s_add_u32 s81, s81, s68\n"
    "ds_read_b64_tr_b16 a[86:87], v7 offset:14336\n"
    "v_mfma_f32_16x16x32_bf16 v[240:243], a[16:19], a[44:47], v[240:243]\n"
    "v_mfma_f32_16x16x32_bf16 v[244:247], a[20:23], a[44:47], v[244:247]\n"
    "ds_read_b64_tr_b16 a[88:89], v8 offset:12288\n"
    "v_mfma_f32_16x16x32_bf16 v[248:251], a[24:27], a[44:47], v[248:251]\n"
    "v_mfma_f32_16x16x32_bf16 v[252:255], a[28:31], a[44:47], v[252:255]\n"
    "ds_read_b64_tr_b16 a[90:91], v8 offset:14336\n"
    "v_mfma_f32_16x16x32_bf16 v[32:35], a[0:3], a[48:51], v[32:35]\n"
    "buffer_load_dwordx4 v3, s[52:55], s81 offen offset:1024 lds\n"
    "v_mfma_f32_16x16x32_bf16 v[36:39], a[4:7], a[48:51], v[36:39]\n"
    "s_add_u32 s81, s81, s69\n"
    "ds_read_b64_tr_b16 a[92:93], v9 offset:12288\n"
    "v_mfma_f32_16x16x32_bf16 v[40:43], a[8:11], a[48:51], v[40:43]\n"
    "v_mfma_f32_16x16x32_bf16 v[44:47], a[12:15], a[48:51], v[44:47]\n"
    "ds_read_b64_tr_b16 a[94:95], v9 offset:14336\n"
    "v_mfma_f32_16x16x32_bf16 v[48:51], a[16:19], a[48:51], v[48:51]\n"
    "v_mfma_f32_16x16x32_bf16 v[52:55], a[20:23], a[48:51], v[52:55]\n"
    "ds_read_b128 a[104:107], v5 offset:4096\n"
    "v_mfma_f32_16x16x32_bf16 v[56:59], a[24:27], a[48:51], v[56:59]\n"
    "buffer_load_dwordx4 v3, s[52:55], s81 offen offset:2048 lds\n"
    "v_mfma_f32_16x16x32_bf16 v[60:63], a[28:31], a[48:51], v[60:63]\n"
    "s_add_u32 s81, s81, s68\n"
    "ds_read_b128 a[108:111], v5 offset:6144\n"
    "v_mfma_f32_16x16x32_bf16 v[64:67], a[0:3], a[52:55], v[64:67]\n"
    "v_mfma_f32_16x16x32_bf16 v[68:71], a[4:7], a[52:55], v[68:71]\n"
    "ds_read_b128 a[112:115], v5 offset:8192\n"
    "v_mfma_f32_16x16x32_bf16 v[72:75], a[8:11], a[52:55], v[72:75]\n"
    "v_mfma_f32_16x16x32_bf16 v[76:79], a[12:15], a[52:55], v[76:79]\n"
    "ds_read_b128 a[116:119], v5 offset:10240\n"
    "v_mfma_f32_16x16x32_bf16 v[80:83], a[16:19], a[52:55], v[80:83]\n"
    "buffer_load_dwordx4 v3, s[52:55], s81 offen offset:3072 lds\n"
    "v_mfma_f32_16x16x32_bf16 v[84:87], a[20:23], a[52:55], v[84:87]\n"
    "s_add_u32 s81, s81, s70\n"
    "ds_read_b128 a[120:123], v5 offset:12288\n"
    "v_mfma_f32_16x16x32_bf16 v[88:91], a[24:27], a[52:55], v[88:91]\n"
    "s_mov_b32 s59, s78\n"
    "v_mfma_f32_16x16x32_bf16 v[92:95], a[28:31], a[52:55], v[92:95]\n"
    "s_mov_b32 s78, s79\n"
    "ds_read_b128 a[124:127], v5 offset:14336\n"
    "v_mfma_f32_16x16x32_bf16 v[96:99], a[0:3], a[56:59], v[96:99]\n"
    "s_mov_b32 s79, s80\n"
    "v_mfma_f32_16x16x32_bf16 v[100:103], a[4:7], a[56:59], v[100:103]\n"
    "s_mov_b32 s80, s59\n"
    "v_xor_b32 v4, 0x10000, v4\n"
    "v_xor_b32 v5, 0x10000, v5\n"
    "v_mfma_f32_16x16x32_bf16 v[104:107], a[8:11], a[56:59], v[104:107]\n"
    "v_add_u32 v6, s60, v6\n"
    "v_add_u32 v7, s60, v7\n"
    "v_mfma_f32_16x16x32_bf16 v[108:111], a[12:15], a[56:59], v[108:111]\n"
    "v_add_u32 v8, s60, v8\n"
    "v_add_u32 v9, s60, v9\n"
    "v_mfma_f32_16x16x32_bf16 v[112:115], a[16:19], a[56:59], v[112:115]\n"
    "v_mfma_f32_16x16x32_bf16 v[116:119], a[20:23], a[56:59], v[116:119]\n"
    "v_mfma_f32_16x16x32_bf16 v[120:123], a[24:27], a[56:59], v[120:123]\n"
    "v_mfma_f32_16x16x32_bf16 v[124:127], a[28:31], a[56:59], v[124:127]\n"
    "v_mfma_f32_16x16x32_bf16 a[128:131], a[0:3], a[60:63], a[128:131]\n"
    "v_mfma_f32_16x16x32_bf16 a[132:135], a[4:7], a[60:63], a[132:135]\n"
    "v_mfma_f32_16x16x32_bf16 a[136:139], a[8:11], a[60:63], a[136:139]\n"
    "v_mfma_f32_16x16x32_bf16 a[140:143], a[12:15], a[60:63], a[140:143]\n"
    "v_mfma_f32_16x16x32_bf16 a[144:147], a[16:19], a[60:63], a[144:147]\n"
    "s_waitcnt lgkmcnt(0)\n"
    "v_mfma_f32_16x16x32_bf16 a[148:151], a[20:23], a[60:63], a[148:151]\n"
    "s_waitcnt vmcnt(11)\n"
    "v_mfma_f32_16x16x32_bf16 a[152:155], a[24:27], a[60:63], a[152:155]\n"
    "s_barrier\n"
    "v_mfma_f32_16x16x32_bf16 a[156:159], a[28:31], a[60:63], a[156:159]\n"
    "v_mfma_f32_16x16x32_bf16 v[128:131], a[64:67], a[96:99], v[128:131]\n"
    "s_add_u32 m0, s82, 0x8000\n"
    "ds_read_b128 a[32:35], v4 offset:0\n"
    "v_mfma_f32_16x16x32_bf16 v[132:135], a[68:71], a[96:99], v[132:135]\n"
    "v_mfma_f32_16x16x32_bf16 v[136:139], a[72:75], a[96:99], v[136:139]\n"
    "buffer_load_dwordx4 v3, s[52:55], s81 offen offset:0 lds\n"
    "v_mfma_f32_16x16x32_bf16 v[140:143], a[76:79], a[96:99], v[140:143]\n"
    "s_add_u32 s81, s81, s68\n"
    "ds_read_b128 a[36:39], v4 offset:2048\n"
    "v_mfma_f32_16x16x32_bf16 v[144:147], a[80:83], a[96:99], v[144:147]\n"
    "v_mfma_f32_16x16x32_bf16 v[148:151], a[84:87], a[96:99], v[148:151]\n"
    "ds_read_b64_tr_b16 a[0:1], v6 offset:0\n"
    "v_mfma_f32_16x16x32_bf16 v[152:155], a[88:91], a[96:99], v[152:155]\n"
    "v_mfma_f32_16x16x32_bf16 v[156:159], a[92:95], a[96:99], v[156:159]\n"
    "ds_read_b64_tr_b16 a[2:3], v6 offset:2048\n"
    "v_mfma_f32_16x16x32_bf16 v[160:163], a[64:67], a[100:103], v[160:163]\n"
    "buffer_load_dwordx4 v3, s[52:55], s81 offen offset:1024 lds\n"
    "v_mfma_f32_16x16x32_bf16 v[164:167], a[68:71], a[100:103], v[164:167]\n"
    "s_add_u32 s81, s81, s69\n"
    "ds_read_b64_tr_b16 a[4:5], v7 offset:0\n"
    "v_mfma_f32_16x16x32_bf16 v[168:171], a[72:75], a[100:103], v[168:171]\n"
    "v_mfma_f32_16x16x32_bf16 v[172:175], a[76:79], a[100:103], v[172:175]\n"
    "ds_read_b64_tr_b16 a[6:7], v7 offset:2048\n"
    "v_mfma_f32_16x16x32_bf16 v[176:179], a[80:83], a[100:103], v[176:179]\n"
    "v_mfma_f32_16x16x32_bf16 v[180:183], a[84:87], a[100:103], v[180:183]\n"
    "ds_read_b64_tr_b16 a[8:9], v8 offset:0\n"
    "v_mfma_f32_16x16x32_bf16 v[184:187], a[88:91], a[100:103], v[184:187]\n"
    "buffer_load_dwordx4 v3, s[52:55], s81 offen offset:2048 lds\n"
    "v_mfma_f32_16x16x32_bf16 v[188:191], a[92:95], a[100:103], v[188:191]\n"
    "s_add_u32 s81, s81, s68\n"
    "ds_read_b64_tr_b16 a[10:11], v8 offset:2048\n"
    "v_mfma_f32_16x16x32_bf16 v[192:195], a[64:67], a[104:107], v[192:195]\n"
    "v_mfma_f32_16x16x32_bf16 v[196:199], a[68:71], a[104:107], v[196:199]\n"
    "ds_read_b64_tr_b16 a[12:13], v9 offset:0\n"
    "v_mfma_f32_16x16x32_bf16 v[200:203], a[72:75], a[104:107], v[200:203]\n"
    "v_mfma_f32_16x16x32_bf16 v[204:207], a[76:79], a[104:107], v[204:207]\n"
    "ds_read_b64_tr_b16 a[14:15], v9 offset:2048\n"
    "v_mfma_f32_16x16x32_bf16 v[208:211], a[80:83], a[104:107], v[208:211]\n"
    "buffer_load_dwordx4 v3, s[52:55], s81 offen offset:3072 lds\n"
    "v_mfma_f32_16x16x32_bf16 v[212:215], a[84:87], a[104:107], v[212:215]\n"
    "s_add_u32 s81, s81, s70\n"
    "ds_read_b64_tr_b16 a[16:17], v6 offset:8192\n"
    "v_mfma_f32_16x16x32_bf16 v[216:219], a[88:91], a[104:107], v[216:219]\n"
    "s_addk_i32 m0, 0x1000\n"
    "v_mfma_f32_16x16x32_bf16 v[220:223], a[92:95], a[104:107], v[220:223]\n"
    "ds_read_b64_tr_b16 a[18:19], v6 offset:10240\n"
    "v_mfma_f32_16x16x32_bf16 v[224:227], a[64:67], a[108:111], v[224:227]\n"
    "v_mfma_f32_16x16x32_bf16 v[228:231], a[68:71], a[108:111], v[228:231]\n"
    "ds_read_b64_tr_b16 a[20:21], v7 offset:8192\n"
    "v_mfma_f32_16x16x32_bf16 v[232:235], a[72:75], a[108:111], v[232:235]\n"
    "buffer_load_dwordx4 v3, s[52:55], s81 offen offset:0 lds\n"
    "v_mfma_f32_16x16x32_bf16 v[236:239], a[76:79], a[108:111], v[236:239]\n"
    "s_add_u32 s81, s81, s68\n"
    "ds_read_b64_tr_b16 a[22:23], v7 offset:10240\n"
    "v_mfma_f32_16x16x32_bf16 v[240:243], a[80:83], a[108:111], v[240:243]\n"
    "v_mfma_f32_16x16x32_bf16 v[244:247], a[84:87], a[108:111], v[244:247]\n"
    "ds_read_b64_tr_b16 a[24:25], v8 offset:8192\n"
    "v_mfma_f32_16x16x32_bf16 v[248:251], a[88:91], a[108:111], v[248:251]\n"
    "v_mfma_f32_16x16x32_bf16 v[252:255], a[92:95], a[108:111], v[252:255]\n"
    "ds_read_b64_tr_b16 a[26:27], v8 offset:10240\n"
    "v_mfma_f32_16x16x32_bf16 v[32:35], a[64:67], a[112:115], v[32:35]\n"
    "buffer_load_dwordx4 v3, s[52:55], s81 offen offset:1024 lds\n"
    "v_mfma_f32_16x16x32_bf16 v[36:39], a[68:71], a[112:115], v[36:39]\n"
    "s_add_u32 s81, s81, s69\n"
    "ds_read_b64_tr_b16 a[28:29], v9 offset:8192\n"
    "v_mfma_f32_16x16x32_bf16 v[40:43], a[72:75], a[112:115], v[40:43]\n"
    "v_mfma_f32_16x16x32_bf16 v[44:47], a[76:79], a[112:115], v[44:47]\n"
    "ds_read_b64_tr_b16 a[30:31], v9 offset:10240\n"
    "v_mfma_f32_16x16x32_bf16 v[48:51], a[80:83], a[112:115], v[48:51]\n"
    "v_mfma_f32_16x16x32_bf16 v[52:55], a[84:87], a[112:115], v[52:55]\n"
    "ds_read_b128 a[40:43], v4 offset:4096\n"
    "v_mfma_f32_16x16x32_bf16 v[56:59], a[88:91], a[112:115], v[56:59]\n"
    "buffer_load_dwordx4 v3, s[52:55], s81 offen offset:2048 lds\n"
    "v_mfma_f32_16x16x32_bf16 v[60:63], a[92:95], a[112:115], v[60:63]\n"
    "s_add_u32 s81, s81, s68\n"
    "ds_read_b128 a[44:47], v4 offset:6144\n"
    "v_mfma_f32_16x16x32_bf16 v[64:67], a[64:67], a[116:119], v[64:67]\n"
    "v_mfma_f32_16x16x32_bf16 v[68:71], a[68:71], a[116:119], v[68:71]\n"
    "ds_read_b128 a[48:51], v4 offset:8192\n"
    "v_mfma_f32_16x16x32_bf16 v[72:75], a[72:75], a[116:119], v[72:75]\n"
    "v_mfma_f32_16x16x32_bf16 v[76:79], a[76:79], a[116:119], v[76:79]\n"
    "ds_read_b128 a[52:55], v4 offset:10240\n"
    "v_mfma_f32_16x16x32_bf16 v[80:83], a[80:83], a[116:119], v[80:83]\n"
    "buffer_load_dwordx4 v3, s[52:55], s81 offen offset:3072 lds\n"
    "v_mfma_f32_16x16x32_bf16 v[84:87], a[84:87], a[116:119], v[84:87]\n"
    "s_add_u32 s81, s81, s70\n"
    "ds_read_b128 a[56:59], v4 offset:12288\n"
    "v_mfma_f32_16x16x32_bf16 v[88:91], a[88:91], a[116:119], v[88:91]\n"
    "v_mfma_f32_16x16x32_bf16 v[92:95], a[92:95], a[116:119], v[92:95]\n"
    "s_waitcnt vmcnt(12)\n"
    "ds_read_b128 a[60:63], v4 offset:14336\n"
    "v_mfma_f32_16x16x32_bf16 v[96:99], a[64:67], a[120:123], v[96:99]\n"
    "v_mfma_f32_16x16x32_bf16 v[100:103], a[68:71], a[120:123], v[100:103]\n"
    "v_cmp_ne_u32 vcc, v1, v13\n"
    "s_nop 1\n"
    "s_cbranch_vccnz LNF0_SPIN_%=\n"
    "LNF0_OK_%=:\n"
    "v_mfma_f32_16x16x32_bf16 v[104:107], a[72:75], a[120:123], v[104:107]\n"
    "v_mfma_f32_16x16x32_bf16 v[108:111], a[76:79], a[120:123], v[108:111]\n"
    "v_mfma_f32_16x16x32_bf16 v[112:115], a[80:83], a[120:123], v[112:115]\n"
    "buffer_load_dwordx4 a[160:163], v0, s[40:43], s71 offen offset:0 sc1\n"
    "v_mfma_f32_16x16x32_bf16 v[116:119], a[84:87], a[120:123], v[116:119]\n"
    "v_mfma_f32_16x16x32_bf16 v[120:123], a[88:91], a[120:123], v[120:123]\n"
    "buffer_load_dwordx4 a[164:167], v0, s[40:43], s72 offen offset:0 sc1\n"
    "v_mfma_f32_16x16x32_bf16 v[124:127], a[92:95], a[120:123], v[124:127]\n"
    "v_mfma_f32_16x16x32_bf16 a[128:131], a[64:67], a[124:127], a[128:131]\n"
    "buffer_load_dwordx4 a[168:171], v0, s[40:43], s73 offen offset:0 sc1\n"
    "v_mfma_f32_16x16x32_bf16 a[132:135], a[68:71], a[124:127], a[132:135]\n"
    "v_mfma_f32_16x16x32_bf16 a[136:139], a[72:75], a[124:127], a[136:139]\n"
    "buffer_load_dwordx4 a[172:175], v0, s[40:43], s74 offen offset:0 sc1\n"
    "v_mfma_f32_16x16x32_bf16 a[140:143], a[76:79], a[124:127], a[140:143]\n"
    "v_mfma_f32_16x16x32_bf16 a[144:147], a[80:83], a[124:127], a[144:147]\n"
    "v_mfma_f32_16x16x32_bf16 a[148:151], a[84:87], a[124:127], a[148:151]\n"
    "s_waitcnt lgkmcnt(0)\n"
    "v_mfma_f32_16x16x32_bf16 a[152:155], a[88:91], a[124:127], a[152:155]\n"
    "v_mfma_f32_16x16x32_bf16 a[156:159], a[92:95], a[124:127], a[156:159]\n"
    "v_mfma_f32_16x16x32_bf16 v[128:131], a[0:3], a[32:35], v[128:131]\n"
    "s_add_u32 m0, s82, 0x10000\n"
    "ds_read_b128 a[96:99], v5 offset:0\n"
    "v_mfma_f32_16x16x32_bf16 v[132:135], a[4:7], a[32:35], v[132:135]\n"
    "v_mfma_f32_16x16x32_bf16 v[136:139], a[8:11], a[32:35], v[136:139]\n"
    "buffer_load_dwordx4 v3, s[52:55], s81 offen offset:0 lds\n"
    "v_mfma_f32_16x16x32_bf16 v[140:143], a[12:15], a[32:35], v[140:143]\n"
    "s_add_u32 s81, s81, s68\n"
    "ds_read_b128 a[100:103], v5 offset:2048\n"
    "v_mfma_f32_16x16x32_bf16 v[144:147], a[16:19], a[32:35], v[144:147]\n"
    "v_mfma_f32_16x16x32_bf16 v[148:151], a[20:23], a[32:35], v[148:151]\n"
    "ds_read_b64_tr_b16 a[64:65], v6 offset:4096\n"
    "v_mfma_f32_16x16x32_bf16 v[152:155], a[24:27], a[32:35], v[152:155]\n"
    "v_mfma_f32_16x16x32_bf16 v[156:159], a[28:31], a[32:35], v[156:159]\n"
    "ds_read_b64_tr_b16 a[66:67], v6 offset:6144\n"
    "v_mfma_f32_16x16x32_bf16 v[160:163], a[0:3], a[36:39], v[160:163]\n"
    "buffer_load_dwordx4 v3, s[52:55], s81 offen offset:1024 lds\n"
    "v_mfma_f32_16x16x32_bf16 v[164:167], a[4:7], a[36:39], v[164:167]\n"
    "s_add_u32 s81, s81, s69\n"
    "ds_read_b64_tr_b16 a[68:69], v7 offset:4096\n"
    "v_mfma_f32_16x16x32_bf16 v[168:171], a[8:11], a[36:39], v[168:171]\n"
    "v_mfma_f32_16x16x32_bf16 v[172:175], a[12:15], a[36:39], v[172:175]\n"
    "ds_read_b64_tr_b16 a[70:71], v7 offset:6144\n"
    "v_mfma_f32_16x16x32_bf16 v[176:179], a[16:19], a[36:39], v[176:179]\n"
    "v_mfma_f32_16x16x32_bf16 v[180:183], a[20:23], a[36:39], v[180:183]\n"
    "ds_read_b64_tr_b16 a[72:73], v8 offset:4096\n"
    "v_mfma_f32_16x16x32_bf16 v[184:187], a[24:27], a[36:39], v[184:187]\n"
    "buffer_load_dwordx4 v3, s[52:55], s81 offen offset:2048 lds\n"
    "v_mfma_f32_16x16x32_bf16 v[188:191], a[28:31], a[36:39], v[188:191]\n"
    "s_add_u32 s81, s81, s68\n"
    "ds_read_b64_tr_b16 a[74:75], v8 offset:6144\n"
    "v_mfma_f32_16x16x32_bf16 v[192:195], a[0:3], a[40:43], v[192:195]\n"
    "v_mfma_f32_16x16x32_bf16 v[196:199], a[4:7], a[40:43], v[196:199]\n"
    "ds_read_b64_tr_b16 a[76:77], v9 offset:4096\n"
    "v_mfma_f32_16x16x32_bf16 v[200:203], a[8:11], a[40:43], v[200:203]\n"
    "v_mfma_f32_16x16x32_bf16 v[204:207], a[12:15], a[40:43], v[204:207]\n"
    "ds_read_b64_tr_b16 a[78:79], v9 offset:6144\n"
    "v_mfma_f32_16x16x32_bf16 v[208:211], a[16:19], a[40:43], v[208:211]\n"
    "buffer_load_dwordx4 v3, s[52:55], s81 offen offset:3072 lds\n"
    "v_mfma_f32_16x16x32_bf16 v[212:215], a[20:23], a[40:43], v[212:215]\n"
    "s_add_u32 s81, s81, s70\n"
    "ds_read_b64_tr_b16 a[80:81], v6 offset:12288\n"
    "v_mfma_f32_16x16x32_bf16 v[216:219], a[24:27], a[40:43], v[216:219]\n"
    "s_addk_i32 m0, 0x1000\n"
    "v_mfma_f32_16x16x32_bf16 v[220:223], a[28:31], a[40:43], v[220:223]\n"
    "ds_read_b64_tr_b16 a[82:83], v6 offset:14336\n"
    "v_mfma_f32_16x16x32_bf16 v[224:227], a[0:3], a[44:47], v[224:227]\n"
    "v_mfma_f32_16x16x32_bf16 v[228:231], a[4:7], a[44:47], v[228:231]\n"
    "ds_read_b64_tr_b16 a[84:85], v7 offset:12288\n"
    "v_mfma_f32_16x16x32_bf16 v[232:235], a[8:11], a[44:47], v[232:235]\n"
    "buffer_load_dwordx4 v3, s[52:55], s81 offen offset:0 lds\n"
    "v_mfma_f32_16x16x32_bf16 v[236:239], a[12:15], a[44:47], v[236:239]\n"
    "s_add_u32 s81, s81, s68\n"
    "ds_read_b64_tr_b16 a[86:87], v7 offset:14336\n"
    "v_mfma_f32_16x16x32_bf16 v[240:243], a[16:19], a[44:47], v[240:243]\n"
    "v_mfma_f32_16x16x32_bf16 v[244:247], a[20:23], a[44:47], v[244:247]\n"
    "ds_read_b64_tr_b16 a[88:89], v8 offset:12288\n"
    "v_mfma_f32_16x16x32_bf16 v[248:251], a[24:27], a[44:47], v[248:251]\n"
    "v_mfma_f32_16x16x32_bf16 v[252:255], a[28:31], a[44:47], v[252:255]\n"
    "ds_read_b64_tr_b16 a[90:91], v8 offset:14336\n"
    "v_mfma_f32_16x16x32_bf16 v[32:35], a[0:3], a[48:51], v[32:35]\n"
    "buffer_load_dwordx4 v3, s[52:55], s81 offen offset:1024 lds\n"
    "v_mfma_f32_16x16x32_bf16 v[36:39], a[4:7], a[48:51], v[36:39]\n"
    "s_add_u32 s81, s81, s69\n"
    "ds_read_b64_tr_b16 a[92:93], v9 offset:12288\n"
    "v_mfma_f32_16x16x32_bf16 v[40:43], a[8:11], a[48:51], v[40:43]\n"
    "v_mfma_f32_16x16x32_bf16 v[44:47], a[12:15], a[48:51], v[44:47]\n"
    "ds_read_b64_tr_b16 a[94:95], v9 offset:14336\n"
    "v_mfma_f32_16x16x32_bf16 v[48:51], a[16:19], a[48:51], v[48:51]\n"
    "v_mfma_f32_16x16x32_bf16 v[52:55], a[20:23], a[48:51], v[52:55]\n"
    "ds_read_b128 a[104:107], v5 offset:4096\n"
    "v_mfma_f32_16x16x32_bf16 v[56:59], a[24:27], a[48:51], v[56:59]\n"
    "buffer_load_dwordx4 v3, s[52:55], s81 offen offset:2048 lds\n"
    "v_mfma_f32_16x16x32_bf16 v[60:63], a[28:31], a[48:51], v[60:63]\n"
    "s_add_u32 s81, s81, s68\n"
    "ds_read_b128 a[108:111], v5 offset:6144\n"
    "v_mfma_f32_16x16x32_bf16 v[64:67], a[0:3], a[52:55], v[64:67]\n"
    "v_mfma_f32_16x16x32_bf16 v[68:71], a[4:7], a[52:55], v[68:71]\n"
    "ds_read_b128 a[112:115], v5 offset:8192\n"
    "v_mfma_f32_16x16x32_bf16 v[72:75], a[8:11], a[52:55], v[72:75]\n"
    "v_mfma_f32_16x16x32_bf16 v[76:79], a[12:15], a[52:55], v[76:79]\n"
    "ds_read_b128 a[116:119], v5 offset:10240\n"
    "v_mfma_f32_16x16x32_bf16 v[80:83], a[16:19], a[52:55], v[80:83]\n"
    "buffer_load_dwordx4 v3, s[52:55], s81 offen offset:3072 lds\n"
    "v_mfma_f32_16x16x32_bf16 v[84:87], a[20:23], a[52:55], v[84:87]\n"
    "s_add_u32 s81, s81, s70\n"
    "ds_read_b128 a[120:123], v5 offset:12288\n"
    "v_mfma_f32_16x16x32_bf16 v[88:91], a[24:27], a[52:55], v[88:91]\n"
    "v_mfma_f32_16x16x32_bf16 v[92:95], a[28:31], a[52:55], v[92:95]\n"
    "ds_read_b128 a[124:127], v5 offset:14336\n"
    "v_mfma_f32_16x16x32_bf16 v[96:99], a[0:3], a[56:59], v[96:99]\n"
    "v_mfma_f32_16x16x32_bf16 v[100:103], a[4:7], a[56:59], v[100:103]\n"
    "v_mfma_f32_16x16x32_bf16 v[104:107], a[8:11], a[56:59], v[104:107]\n"
    "buffer_load_dwordx4 a[176:179], v0, s[40:43], s71 offen offset:1024 sc1\n"
    "v_mfma_f32_16x16x32_bf16 v[108:111], a[12:15], a[56:59], v[108:111]\n"
    "v_mfma_f32_16x16x32_bf16 v[112:115], a[16:19], a[56:59], v[112:115]\n"
    "buffer_load_dwordx4 a[180:183], v0, s[40:43], s72 offen offset:1024 sc1\n"
    "v_mfma_f32_16x16x32_bf16 v[116:119], a[20:23], a[56:59], v[116:119]\n"
    "v_mfma_f32_16x16x32_bf16 v[120:123], a[24:27], a[56:59], v[120:123]\n"
    "buffer_load_dwordx4 a[184:187], v0, s[40:43], s73 offen offset:1024 sc1\n"
    "v_mfma_f32_16x16x32_bf16 v[124:127], a[28:31], a[56:59], v[124:127]\n"
    "v_mfma_f32_16x16x32_bf16 a[128:131], a[0:3], a[60:63], a[128:131]\n"
    "buffer_load_dwordx4 a[188:191], v0, s[40:43], s74 offen offset:1024 sc1\n"
    "v_mfma_f32_16x16x32_bf16 a[132:135], a[4:7], a[60:63], a[132:135]\n"
    "v_mfma_f32_16x16x32_bf16 a[136:139], a[8:11], a[60:63], a[136:139]\n"
    "v_mfma_f32_16x16x32_bf16 a[140:143], a[12:15], a[60:63], a[140:143]\n"
    "v_mfma_f32_16x16x32_bf16 a[144:147], a[16:19], a[60:63], a[144:147]\n"
    "s_waitcnt lgkmcnt(0)\n"
    "v_mfma_f32_16x16x32_bf16 a[148:151], a[20:23], a[60:63], a[148:151]\n"
    "s_waitcnt vmcnt(24)\n"
    "v_mfma_f32_16x16x32_bf16 a[152:155], a[24:27], a[60:63], a[152:155]\n"
    "s_barrier\n"
    "v_mfma_f32_16x16x32_bf16 a[156:159], a[28:31], a[60:63], a[156:159]\n"
    "v_mfma_f32_16x16x32_bf16 v[128:131], a[64:67], a[96:99], v[128:131]\n"
    "v_add_u32 v6, 0xfffe0000, v6\n"
    "v_add_u32 v7, 0xfffe0000, v7\n"
    "v_mfma_f32_16x16x32_bf16 v[132:135], a[68:71], a[96:99], v[132:135]\n"
    "buffer_load_dwordx4 a[192:195], v0, s[40:43], s71 offen offset:2048 sc1\n"
    "v_mfma_f32_16x16x32_bf16 v[136:139], a[72:75], a[96:99], v[136:139]\n"
    "buffer_load_dwordx4 a[196:199], v0, s[40:43], s72 offen offset:2048 sc1\n"
    "v_mfma_f32_16x16x32_bf16 v[140:143], a[76:79], a[96:99], v[140:143]\n"
    "buffer_load_dwordx4 a[200:203], v0, s[40:43], s73 offen offset:2048 sc1\n"
    "v_mfma_f32_16x16x32_bf16 v[144:147], a[80:83], a[96:99], v[144:147]\n"
    "buffer_load_dwordx4 a[204:207], v0, s[40:43], s74 offen offset:2048 sc1\n"
    "v_mfma_f32_16x16x32_bf16 v[148:151], a[84:87], a[96:99], v[148:151]\n"
    "v_add_u32 v8, 0xfffe0000, v8\n"
    "v_add_u32 v9, 0xfffe0000, v9\n"
    "v_mfma_f32_16x16x32_bf16 v[152:155], a[88:91], a[96:99], v[152:155]\n"
    "s_add_u32 m0, s82, 0x18000\n"
    "v_cvt_pk_bf16_f32 v128, v128, v129\n"
    "v_cvt_pk_bf16_f32 v129, v130, v131\n"
    "v_mfma_f32_16x16x32_bf16 v[156:159], a[92:95], a[96:99], v[156:159]\n"
    "v_cvt_pk_bf16_f32 v130, v132, v133\n"
    "v_cvt_pk_bf16_f32 v131, v134, v135\n"
    "v_mfma_f32_16x16x32_bf16 v[160:163], a[64:67], a[100:103], v[160:163]\n"
    "buffer_load_dwordx4 v3, s[52:55], s81 offen offset:0 lds\n"
    "v_mfma_f32_16x16x32_bf16 v[164:167], a[68:71], a[100:103], v[164:167]\n"
    "s_add_u32 s81, s81, s68\n"
    "v_cvt_pk_bf16_f32 v132, v136, v137\n"
    "v_cvt_pk_bf16_f32 v133, v138, v139\n"
    "v_mfma_f32_16x16x32_bf16 v[168:171], a[72:75], a[100:103], v[168:171]\n"
    "v_cvt_pk_bf16_f32 v134, v140, v141\n"
    "v_cvt_pk_bf16_f32 v135, v142, v143\n"
    "v_mfma_f32_16x16x32_bf16 v[172:175], a[76:79], a[100:103], v[172:175]\n"
    "v_cvt_pk_bf16_f32 v136, v144, v145\n"
    "v_cvt_pk_bf16_f32 v137, v146, v147\n"
    "v_mfma_f32_16x16x32_bf16 v[176:179], a[80:83], a[100:103], v[176:179]\n"
    "v_cvt_pk_bf16_f32 v138, v148, v149\n"
    "v_cvt_pk_bf16_f32 v139, v150, v151\n"
    "v_mfma_f32_16x16x32_bf16 v[180:183], a[84:87], a[100:103], v[180:183]\n"
    "buffer_load_dwordx4 v3, s[52:55], s81 offen offset:1024 lds\n"
    "v_mfma_f32_16x16x32_bf16 v[184:187], a[88:91], a[100:103], v[184:187]\n"
    "s_add_u32 s81, s81, s69\n"
    "v_cvt_pk_bf16_f32 v140, v152, v153\n"
    "v_cvt_pk_bf16_f32 v141, v154, v155\n"
    "v_mfma_f32_16x16x32_bf16 v[188:191], a[92:95], a[100:103], v[188:191]\n"
    "v_cvt_pk_bf16_f32 v142, v156, v157\n"
    "v_cvt_pk_bf16_f32 v143, v158, v159\n"
    "v_mfma_f32_16x16x32_bf16 v[192:195], a[64:67], a[104:107], v[192:195]\n"
    "v_cvt_pk_bf16_f32 v144, v160, v161\n"
    "v_cvt_pk_bf16_f32 v145, v162, v163\n"
    "v_mfma_f32_16x16x32_bf16 v[196:199], a[68:71], a[104:107], v[196:199]\n"
    "v_cvt_pk_bf16_f32 v146, v164, v165\n"
    "v_cvt_pk_bf16_f32 v147, v166, v167\n"
    "v_mfma_f32_16x16x32_bf16 v[200:203], a[72:75], a[104:107], v[200:203]\n"
    "buffer_load_dwordx4 v3, s[52:55], s81 offen offset:2048 lds\n"
    "v_mfma_f32_16x16x32_bf16 v[204:207], a[76:79], a[104:107], v[204:207]\n"
    "s_add_u32 s81, s81, s68\n"
    "v_cvt_pk_bf16_f32 v148, v168, v169\n"
    "v_cvt_pk_bf16_f32 v149, v170, v171\n"
    "v_mfma_f32_16x16x32_bf16 v[208:211], a[80:83], a[104:107], v[208:211]\n"
    "v_cvt_pk_bf16_f32 v150, v172, v173\n"
    "v_cvt_pk_bf16_f32 v151, v174, v175\n"
    "v_mfma_f32_16x16x32_bf16 v[212:215], a[84:87], a[104:107], v[212:215]\n"
    "v_cvt_pk_bf16_f32 v152, v176, v177\n"
    "v_cvt_pk_bf16_f32 v153, v178, v179\n"
    "v_mfma_f32_16x16x32_bf16 v[216:219], a[88:91], a[104:107], v[216:219]\n"
    "v_cvt_pk_bf16_f32 v154, v180, v181\n"
    "v_cvt_pk_bf16_f32 v155, v182, v183\n"
    "v_mfma_f32_16x16x32_bf16 v[220:223], a[92:95], a[104:107], v[220:223]\n"
    "buffer_load_dwordx4 v3, s[52:55], s81 offen offset:3072 lds\n"
    "v_mfma_f32_16x16x32_bf16 v[224:227], a[64:67], a[108:111], v[224:227]\n"
    "s_add_u32 s81, s81, s70\n"
    "v_cvt_pk_bf16_f32 v156, v184, v185\n"
    "v_cvt_pk_bf16_f32 v157, v186, v187\n"
    "v_mfma_f32_16x16x32_bf16 v[228:231], a[68:71], a[108:111], v[228:231]\n"
    "s_addk_i32 m0, 0x1000\n"
    "v_cvt_pk_bf16_f32 v158, v188, v189\n"
    "v_cvt_pk_bf16_f32 v159, v190, v191\n"
    "v_mfma_f32_16x16x32_bf16 v[232:235], a[72:75], a[108:111], v[232:235]\n"
    "v_cvt_pk_bf16_f32 v160, v192, v193\n"
    "v_cvt_pk_bf16_f32 v161, v194, v195\n"
    "v_mfma_f32_16x16x32_bf16 v[236:239], a[76:79], a[108:111], v[236:239]\n"
    "v_cvt_pk_bf16_f32 v162, v196, v197\n"
    "v_cvt_pk_bf16_f32 v163, v198, v199\n"
    "v_mfma_f32_16x16x32_bf16 v[240:243], a[80:83], a[108:111], v[240:243]\n"
    "buffer_load_dwordx4 v3, s[52:55], s81 offen offset:0 lds\n"
    "v_mfma_f32_16x16x32_bf16 v[244:247], a[84:87], a[108:111], v[244:247]\n"
    "s_add_u32 s81, s81, s68\n"
    "v_cvt_pk_bf16_f32 v164, v200, v201\n"
    "v_cvt_pk_bf16_f32 v165, v202, v203\n"
    "v_mfma_f32_16x16x32_bf16 v[248:251], a[88:91], a[108:111], v[248:251]\n"
    "v_cvt_pk_bf16_f32 v166, v204, v205\n"
    "v_cvt_pk_bf16_f32 v167, v206, v207\n"
    "v_mfma_f32_16x16x32_bf16 v[252:255], a[92:95], a[108:111], v[252:255]\n"
    "v_cvt_pk_bf16_f32 v168, v208, v209\n"
    "v_cvt_pk_bf16_f32 v169, v210, v211\n"
    "v_mfma_f32_16x16x32_bf16 v[32:35], a[64:67], a[112:115], v[32:35]\n"
    "v_cvt_pk_bf16_f32 v170, v212, v213\n"
    "v_cvt_pk_bf16_f32 v171, v214, v215\n"
    "v_mfma_f32_16x16x32_bf16 v[36:39], a[68:71], a[112:115], v[36:39]\n"
    "buffer_load_dwordx4 v3, s[52:55], s81 offen offset:1024 lds\n"
    "v_mfma_f32_16x16x32_bf16 v[40:43], a[72:75], a[112:115], v[40:43]\n"
    "s_add_u32 s81, s81, s69\n"
    "v_cvt_pk_bf16_f32 v172, v216, v217\n"
    "v_cvt_pk_bf16_f32 v173, v218, v219\n"
    "v_mfma_f32_16x16x32_bf16 v[44:47], a[76:79], a[112:115], v[44:47]\n"
    "v_cvt_pk_bf16_f32 v174, v220, v221\n"
    "v_cvt_pk_bf16_f32 v175, v222, v223\n"
    "v_mfma_f32_16x16x32_bf16 v[48:51], a[80:83], a[112:115], v[48:51]\n"
    "v_cvt_pk_bf16_f32 v176, v224, v225\n"
    "v_cvt_pk_bf16_f32 v177, v226, v227\n"
    "v_mfma_f32_16x16x32_bf16 v[52:55], a[84:87], a[112:115], v[52:55]\n"
    "v_cvt_pk_bf16_f32 v178, v228, v229\n"
    "v_cvt_pk_bf16_f32 v179, v230, v231\n"
    "v_mfma_f32_16x16x32_bf16 v[56:59], a[88:91], a[112:115], v[56:59]\n"
    "buffer_load_dwordx4 v3, s[52:55], s81 offen offset:2048 lds\n"
    "v_mfma_f32_16x16x32_bf16 v[60:63], a[92:95], a[112:115], v[60:63]\n"
    "s_add_u32 s81, s81, s68\n"
    "v_cvt_pk_bf16_f32 v180, v232, v233\n"
    "v_cvt_pk_bf16_f32 v181, v234, v235\n"
    "v_mfma_f32_16x16x32_bf16 v[64:67], a[64:67], a[116:119], v[64:67]\n"
    "v_cvt_pk_bf16_f32 v182, v236, v237\n"
    "v_cvt_pk_bf16_f32 v183, v238, v239\n"
    "v_mfma_f32_16x16x32_bf16 v[68:71], a[68:71], a[116:119], v[68:71]\n"
    "v_cvt_pk_bf16_f32 v184, v240, v241\n"
    "v_cvt_pk_bf16_f32 v185, v242, v243\n"
    "v_mfma_f32_16x16x32_bf16 v[72:75], a[72:75], a[116:119], v[72:75]\n"
    "v_cvt_pk_bf16_f32 v186, v244, v245\n"
    "v_cvt_pk_bf16_f32 v187, v246, v247\n"
    "v_mfma_f32_16x16x32_bf16 v[76:79], a[76:79], a[116:119], v[76:79]\n"
    "buffer_load_dwordx4 v3, s[52:55], s81 offen offset:3072 lds\n"
    "v_mfma_f32_16x16x32_bf16 v[80:83], a[80:83], a[116:119], v[80:83]\n"
    "s_add_u32 s81, s81, s70\n"
    "v_cvt_pk_bf16_f32 v188, v248, v249\n"
    "v_cvt_pk_bf16_f32 v189, v250, v251\n"
    "v_mfma_f32_16x16x32_bf16 v[84:87], a[84:87], a[116:119], v[84:87]\n"
    "v_cvt_pk_bf16_f32 v190, v252, v253\n"
    "v_cvt_pk_bf16_f32 v191, v254, v255\n"
    "v_mfma_f32_16x16x32_bf16 v[88:91], a[88:91], a[116:119], v[88:91]\n"
    "v_cvt_pk_bf16_f32 v192, v32, v33\n"
    "v_cvt_pk_bf16_f32 v193, v34, v35\n"
    "v_mfma_f32_16x16x32_bf16 v[92:95], a[92:95], a[116:119], v[92:95]\n"
    "v_cvt_pk_bf16_f32 v194, v36, v37\n"
    "v_cvt_pk_bf16_f32 v195, v38, v39\n"
    "v_mfma_f32_16x16x32_bf16 v[96:99], a[64:67], a[120:123], v[96:99]\n"
    "ds_read_b64_tr_b16 a[224:225], v6 offset:16384\n"
    "v_cvt_pk_bf16_f32 v196, v40, v41\n"
    "v_mfma_f32_16x16x32_bf16 v[100:103], a[68:71], a[120:123], v[100:103]\n"
    "v_cvt_pk_bf16_f32 v197, v42, v43\n"
    "v_cvt_pk_bf16_f32 v198, v44, v45\n"
    "v_mfma_f32_16x16x32_bf16 v[104:107], a[72:75], a[120:123], v[104:107]\n"
    "ds_read_b64_tr_b16 a[226:227], v6 offset:18432\n"
    "v_cvt_pk_bf16_f32 v199, v46, v47\n"
    "v_mfma_f32_16x16x32_bf16 v[108:111], a[76:79], a[120:123], v[108:111]\n"
    "v_cvt_pk_bf16_f32 v200, v48, v49\n"
    "v_cvt_pk_bf16_f32 v201, v50, v51\n"
    "v_mfma_f32_16x16x32_bf16 v[112:115], a[80:83], a[120:123], v[112:115]\n"
    "ds_read_b64_tr_b16 a[228:229], v6 offset:20480\n"
    "v_cvt_pk_bf16_f32 v202, v52, v53\n"
    "v_mfma_f32_16x16x32_bf16 v[116:119], a[84:87], a[120:123], v[116:119]\n"
    "v_cvt_pk_bf16_f32 v203, v54, v55\n"
    "v_cvt_pk_bf16_f32 v204, v56, v57\n"
    "v_mfma_f32_16x16x32_bf16 v[120:123], a[88:91], a[120:123], v[120:123]\n"
    "ds_read_b64_tr_b16 a[230:231], v6 offset:22528\n"
    "v_cvt_pk_bf16_f32 v205, v58, v59\n"
    "v_mfma_f32_16x16x32_bf16 v[124:127], a[92:95], a[120:123], v[124:127]\n"
    "v_cvt_pk_bf16_f32 v206, v60, v61\n"
    "v_cvt_pk_bf16_f32 v207, v62, v63\n"
    "v_mfma_f32_16x16x32_bf16 a[128:131], a[64:67], a[124:127], a[128:131]\n"
    "ds_read_b64_tr_b16 a[232:233], v7 offset:16384\n"
    "v_cvt_pk_bf16_f32 v208, v64, v65\n"
    "v_mfma_f32_16x16x32_bf16 a[132:135], a[68:71], a[124:127], a[132:135]\n"
    "v_cvt_pk_bf16_f32 v209, v66, v67\n"
    "v_cvt_pk_bf16_f32 v210, v68, v69\n"
    "v_mfma_f32_16x16x32_bf16 a[136:139], a[72:75], a[124:127], a[136:139]\n"
    "ds_read_b64_tr_b16 a[234:235], v7 offset:18432\n"
    "v_cvt_pk_bf16_f32 v211, v70, v71\n"
    "v_mfma_f32_16x16x32_bf16 a[140:143], a[76:79], a[124:127], a[140:143]\n"
    "v_cvt_pk_bf16_f32 v212, v72, v73\n"
    "v_cvt_pk_bf16_f32 v213, v74, v75\n"
    "v_mfma_f32_16x16x32_bf16 a[144:147], a[80:83], a[124:127], a[144:147]\n"
    "ds_read_b64_tr_b16 a[236:237], v7 offset:20480\n"
    "v_cvt_pk_bf16_f32 v214, v76, v77\n"
    "v_mfma_f32_16x16x32_bf16 a[148:151], a[84:87], a[124:127], a[148:151]\n"
    "v_cvt_pk_bf16_f32 v215, v78, v79\n"
    "v_mfma_f32_16x16x32_bf16 a[152:155], a[88:91], a[124:127], a[152:155]\n"
    "s_waitcnt vmcnt(12)\n"
    "ds_read_b64_tr_b16 a[238:239], v7 offset:22528\n"
    "v_mfma_f32_16x16x32_bf16 a[156:159], a[92:95], a[124:127], a[156:159]\n"
    "s_waitcnt lgkmcnt(4)\n"
    "s_branch KV_DONE_%=\n"
    "KV_LOOP1_%=:\n"
    "v_mfma_f32_16x16x32_bf16 v[128:131], a[32:35], a[0:3], v[128:131]\n"
    "s_add_u32 m0, s80, 0\n"
    "ds_read_b128 a[96:99], v5 offset:0\n"
    "v_mfma_f32_16x16x32_bf16 v[132:135], a[36:39], a[0:3], v[132:135]\n"
    "v_mfma_f32_16x16x32_bf16 v[136:139], a[32:35], a[4:7], v[136:139]\n"
    "buffer_load_dwordx4 v2, s[48:51], s65 offen offset:0 nt lds\n"
    "v_mfma_f32_16x16x32_bf16 v[140:143], a[36:39], a[4:7], v[140:143]\n"
    "s_add_u32 s65, s65, 0x1fc00\n"
    "ds_read_b128 a[100:103], v5 offset:2048\n"
    "v_mfma_f32_16x16x32_bf16 v[144:147], a[32:35], a[8:11], v[144:147]\n"
    "v_mfma_f32_16x16x32_bf16 v[148:151], a[36:39], a[8:11], v[148:151]\n"
    "ds_read_b64_tr_b16 a[64:65], v6 offset:4096\n"
    "v_mfma_f32_16x16x32_bf16 v[152:155], a[32:35], a[12:15], v[152:155]\n"
    "v_mfma_f32_16x16x32_bf16 v[156:159], a[36:39], a[12:15], v[156:159]\n"
    "ds_read_b64_tr_b16 a[66:67], v6 offset:6144\n"
    "v_mfma_f32_16x16x32_bf16 v[160:163], a[32:35], a[16:19], v[160:163]\n"
    "buffer_load_dwordx4 v2, s[48:51], s65 offen offset:1024 nt lds\n"
    "v_mfma_f32_16x16x32_bf16 v[164:167], a[36:39], a[16:19], v[164:167]\n"
    "s_add_u32 s65, s65, 0xfffe7c00\n"
    "ds_read_b64_tr_b16 a[68:69], v7 offset:4096\n"
    "v_mfma_f32_16x16x32_bf16 v[168:171], a[32:35], a[20:23], v[168:171]\n"
    "s_sub_u32 s60, s79, s78\n"
    "v_mfma_f32_16x16x32_bf16 v[172:175], a[36:39], a[20:23], v[172:175]\n"
    "ds_read_b64_tr_b16 a[70:71], v7 offset:6144\n"
    "v_mfma_f32_16x16x32_bf16 v[176:179], a[32:35], a[24:27], v[176:179]\n"
    "v_mfma_f32_16x16x32_bf16 v[180:183], a[36:39], a[24:27], v[180:183]\n"
    "ds_read_b64_tr_b16 a[72:73], v8 offset:4096\n"
    "v_mfma_f32_16x16x32_bf16 v[184:187], a[32:35], a[28:31], v[184:187]\n"
    "buffer_load_dwordx4 v2, s[48:51], s65 offen offset:2048 nt lds\n"
    "v_mfma_f32_16x16x32_bf16 v[188:191], a[36:39], a[28:31], v[188:191]\n"
    "s_add_u32 s65, s65, 0x1fc00\n"
    "ds_read_b64_tr_b16 a[74:75], v8 offset:6144\n"
    "v_mfma_f32_16x16x32_bf16 v[192:195], a[40:43], a[0:3], v[192:195]\n"
    "v_mfma_f32_16x16x32_bf16 v[196:199], a[44:47], a[0:3], v[196:199]\n"
    "ds_read_b64_tr_b16 a[76:77], v9 offset:4096\n"
    "v_mfma_f32_16x16x32_bf16 v[200:203], a[40:43], a[4:7], v[200:203]\n"
    "v_mfma_f32_16x16x32_bf16 v[204:207], a[44:47], a[4:7], v[204:207]\n"
    "ds_read_b64_tr_b16 a[78:79], v9 offset:6144\n"
    "v_mfma_f32_16x16x32_bf16 v[208:211], a[40:43], a[8:11], v[208:211]\n"
    "buffer_load_dwordx4 v2, s[48:51], s65 offen offset:3072 nt lds\n"
    "v_mfma_f32_16x16x32_bf16 v[212:215], a[44:47], a[8:11], v[212:215]\n"
    "s_add_u32 s65, s65, 0x18c00\n"
    "ds_read_b64_tr_b16 a[80:81], v6 offset:12288\n"
    "v_mfma_f32_16x16x32_bf16 v[216:219], a[40:43], a[12:15], v[216:219]\n"
    "s_add_u32 m0, s80, 4096\n"
    "v_mfma_f32_16x16x32_bf16 v[220:223], a[44:47], a[12:15], v[220:223]\n"
    "ds_read_b64_tr_b16 a[82:83], v6 offset:14336\n"
    "v_mfma_f32_16x16x32_bf16 v[224:227], a[40:43], a[16:19], v[224:227]\n"
    "v_mfma_f32_16x16x32_bf16 v[228:231], a[44:47], a[16:19], v[228:231]\n"
    "ds_read_b64_tr_b16 a[84:85], v7 offset:12288\n"
    "v_mfma_f32_16x16x32_bf16 v[232:235], a[40:43], a[20:23], v[232:235]\n"
    "buffer_load_dwordx4 v2, s[48:51], s65 offen offset:0 nt lds\n"
    "v_mfma_f32_16x16x32_bf16 v[236:239], a[44:47], a[20:23], v[236:239]\n"
    "s_add_u32 s65, s65, 0x1fc00\n"
    "ds_read_b64_tr_b16 a[86:87], v7 offset:14336\n"
    "v_mfma_f32_16x16x32_bf16 v[240:243], a[40:43], a[24:27], v[240:243]\n"
    "v_mfma_f32_16x16x32_bf16 v[244:247], a[44:47], a[24:27], v[244:247]\n"
    "ds_read_b64_tr_b16 a[88:89], v8 offset:12288\n"
    "v_mfma_f32_16x16x32_bf16 v[248:251], a[40:43], a[28:31], v[248:251]\n"
    "v_mfma_f32_16x16x32_bf16 v[252:255], a[44:47], a[28:31], v[252:255]\n"
    "ds_read_b64_tr_b16 a[90:91], v8 offset:14336\n"
    "v_mfma_f32_16x16x32_bf16 v[32:35], a[48:51], a[0:3], v[32:35]\n"
    "buffer_load_dwordx4 v2, s[48:51], s65 offen offset:1024 nt lds\n"
    "v_mfma_f32_16x16x32_bf16 v[36:39], a[52:55], a[0:3], v[36:39]\n"
    "s_add_u32 s65, s65, 0xfffe7c00\n"
    "ds_read_b64_tr_b16 a[92:93], v9 offset:12288\n"
    "v_mfma_f32_16x16x32_bf16 v[40:43], a[48:51], a[4:7], v[40:43]\n"
    "v_mfma_f32_16x16x32_bf16 v[44:47], a[52:55], a[4:7], v[44:47]\n"
    "ds_read_b64_tr_b16 a[94:95], v9 offset:14336\n"
    "v_mfma_f32_16x16x32_bf16 v[48:51], a[48:51], a[8:11], v[48:51]\n"
    "v_mfma_f32_16x16x32_bf16 v[52:55], a[52:55], a[8:11], v[52:55]\n"
    "ds_read_b128 a[104:107], v5 offset:4096\n"
    "v_mfma_f32_16x16x32_bf16 v[56:59], a[48:51], a[12:15], v[56:59]\n"
    "buffer_load_dwordx4 v2, s[48:51], s65 offen offset:2048 nt lds\n"
    "v_mfma_f32_16x16x32_bf16 v[60:63], a[52:55], a[12:15], v[60:63]\n"
    "s_add_u32 s65, s65, 0x1fc00\n"
    "ds_read_b128 a[108:111], v5 offset:6144\n"
    "v_mfma_f32_16x16x32_bf16 v[64:67], a[48:51], a[16:19], v[64:67]\n"
    "v_mfma_f32_16x16x32_bf16 v[68:71], a[52:55], a[16:19], v[68:71]\n"
    "ds_read_b128 a[112:115], v5 offset:8192\n"
    "v_mfma_f32_16x16x32_bf16 v[72:75], a[48:51], a[20:23], v[72:75]\n"
    "v_mfma_f32_16x16x32_bf16 v[76:79], a[52:55], a[20:23], v[76:79]\n"
    "ds_read_b128 a[116:119], v5 offset:10240\n"
    "v_mfma_f32_16x16x32_bf16 v[80:83], a[48:51], a[24:27], v[80:83]\n"
    "buffer_load_dwordx4 v2, s[48:51], s65 offen offset:3072 nt lds\n"
    "v_mfma_f32_16x16x32_bf16 v[84:87], a[52:55], a[24:27], v[84:87]\n"
    "s_add_u32 s65, s65, 0x18c00\n"
    "ds_read_b128 a[120:123], v5 offset:12288\n"
    "v_mfma_f32_16x16x32_bf16 v[88:91], a[48:51], a[28:31], v[88:91]\n"
    "s_bitset0_b32 s65, 22\n"
    "v_mfma_f32_16x16x32_bf16 v[92:95], a[52:55], a[28:31], v[92:95]\n"
    "s_mov_b32 s59, s78\n"
    "ds_read_b128 a[124:127], v5 offset:14336\n"
    "v_mfma_f32_16x16x32_bf16 v[96:99], a[56:59], a[0:3], v[96:99]\n"
    "s_mov_b32 s78, s79\n"
    "v_mfma_f32_16x16x32_bf16 v[100:103], a[60:63], a[0:3], v[100:103]\n"
    "s_mov_b32 s79, s80\n"
    "v_xor_b32 v4, 0x10000, v4\n"
    "v_xor_b32 v5, 0x10000, v5\n"
    "v_mfma_f32_16x16x32_bf16 v[104:107], a[56:59], a[4:7], v[104:107]\n"
    "s_mov_b32 s80, s59\n"
    "v_add_u32 v6, s60, v6\n"
    "v_add_u32 v7, s60, v7\n"
    "v_mfma_f32_16x16x32_bf16 v[108:111], a[60:63], a[4:7], v[108:111]\n"
    "v_add_u32 v8, s60, v8\n"
    "v_add_u32 v9, s60, v9\n"
    "v_mfma_f32_16x16x32_bf16 v[112:115], a[56:59], a[8:11], v[112:115]\n"
    "v_mfma_f32_16x16x32_bf16 v[116:119], a[60:63], a[8:11], v[116:119]\n"
    "v_mfma_f32_16x16x32_bf16 v[120:123], a[56:59], a[12:15], v[120:123]\n"
    "v_mfma_f32_16x16x32_bf16 v[124:127], a[60:63], a[12:15], v[124:127]\n"
    "v_mfma_f32_16x16x32_bf16 a[128:131], a[56:59], a[16:19], a[128:131]\n"
    "v_mfma_f32_16x16x32_bf16 a[132:135], a[60:63], a[16:19], a[132:135]\n"
    "v_mfma_f32_16x16x32_bf16 a[136:139], a[56:59], a[20:23], a[136:139]\n"
    "v_mfma_f32_16x16x32_bf16 a[140:143], a[60:63], a[20:23], a[140:143]\n"
    "v_mfma_f32_16x16x32_bf16 a[144:147], a[56:59], a[24:27], a[144:147]\n"
    "s_waitcnt lgkmcnt(0)\n"
    "v_mfma_f32_16x16x32_bf16 a[148:151], a[60:63], a[24:27], a[148:151]\n"
    "s_waitcnt vmcnt(9)\n"
    "v_mfma_f32_16x16x32_bf16 a[152:155], a[56:59], a[28:31], a[152:155]\n"
    "s_barrier\n"
    "v_mfma_f32_16x16x32_bf16 a[156:159], a[60:63], a[28:31], a[156:159]\n"
    "KV_S1_1_%=:\n"
    "v_mfma_f32_16x16x32_bf16 v[128:131], a[96:99], a[64:67], v[128:131]\n"
    "ds_read_b128 a[32:35], v4 offset:0\n"
    "v_mfma_f32_16x16x32_bf16 v[132:135], a[100:103], a[64:67], v[132:135]\n"
    "v_mfma_f32_16x16x32_bf16 v[136:139], a[96:99], a[68:71], v[136:139]\n"
    "buffer_store_dword v13, off, s[40:43], s86 offset:0 sc1\n"
    "v_mfma_f32_16x16x32_bf16 v[140:143], a[100:103], a[68:71], v[140:143]\n"
    "s_mov_b32 s86, s85\n"
    "ds_read_b128 a[36:39], v4 offset:2048\n"
    "v_mfma_f32_16x16x32_bf16 v[144:147], a[96:99], a[72:75], v[144:147]\n"
    "s_mov_b32 s85, 0xc0000000\n"
    "v_mfma_f32_16x16x32_bf16 v[148:151], a[100:103], a[72:75], v[148:151]\n"
    "ds_read_b64_tr_b16 a[0:1], v6 offset:0\n"
    "v_mfma_f32_16x16x32_bf16 v[152:155], a[96:99], a[76:79], v[152:155]\n"
    "buffer_store_dwordx4 v[18:21], v30, s[40:43], s87 offen offset:0 sc1\n"
    "v_mfma_f32_16x16x32_bf16 v[156:159], a[100:103], a[76:79], v[156:159]\n"
    "ds_read_b64_tr_b16 a[2:3], v6 offset:2048\n"
    "v_mfma_f32_16x16x32_bf16 v[160:163], a[96:99], a[80:83], v[160:163]\n"
    "v_mfma_f32_16x16x32_bf16 v[164:167], a[100:103], a[80:83], v[164:167]\n"
    "ds_read_b64_tr_b16 a[4:5], v7 offset:0\n"
    "v_mfma_f32_16x16x32_bf16 v[168:171], a[96:99], a[84:87], v[168:171]\n"
    "buffer_store_dwordx4 v[22:25], v30, s[40:43], s87 offen offset:2048 sc1\n"
    "v_mfma_f32_16x16x32_bf16 v[172:175], a[100:103], a[84:87], v[172:175]\n"
    "ds_read_b64_tr_b16 a[6:7], v7 offset:2048\n"
    "v_mfma_f32_16x16x32_bf16 v[176:179], a[96:99], a[88:91], v[176:179]\n"
    "v_mfma_f32_16x16x32_bf16 v[180:183], a[100:103], a[88:91], v[180:183]\n"
    "ds_read_b64_tr_b16 a[8:9], v8 offset:0\n"
    "v_mfma_f32_16x16x32_bf16 v[184:187], a[96:99], a[92:95], v[184:187]\n"
    "buffer_store_dwordx4 v[26:29], v31, s[40:43], s87 offen offset:0 sc1\n"
    "v_mfma_f32_16x16x32_bf16 v[188:191], a[100:103], a[92:95], v[188:191]\n"
    "s_mov_b32 s87, 0xc0000000\n"
    "ds_read_b64_tr_b16 a[10:11], v8 offset:2048\n"
    "v_mfma_f32_16x16x32_bf16 v[192:195], a[104:107], a[64:67], v[192:195]\n"
    "s_mov_b32 m0, s66\n"
    "v_mfma_f32_16x16x32_bf16 v[196:199], a[108:111], a[64:67], v[196:199]\n"
    "ds_read_b64_tr_b16 a[12:13], v9 offset:0\n"
    "v_mfma_f32_16x16x32_bf16 v[200:203], a[104:107], a[68:71], v[200:203]\n"
    "buffer_load_dwordx4 v1, s[44:47], s64 offen offset:0 lds\n"
    "v_mfma_f32_16x16x32_bf16 v[204:207], a[108:111], a[68:71], v[204:207]\n"
    "s_addk_i32 s64, 0x1c00\n"
    "ds_read_b64_tr_b16 a[14:15], v9 offset:2048\n"
    "v_mfma_f32_16x16x32_bf16 v[208:211], a[104:107], a[72:75], v[208:211]\n"
    "v_mfma_f32_16x16x32_bf16 v[212:215], a[108:111], a[72:75], v[212:215]\n"
    "ds_read_b64_tr_b16 a[16:17], v6 offset:8192\n"
    "v_mfma_f32_16x16x32_bf16 v[216:219], a[104:107], a[76:79], v[216:219]\n"
    "buffer_load_dwordx4 v1, s[44:47], s64 offen offset:1024 lds\n"
    "v_mfma_f32_16x16x32_bf16 v[220:223], a[108:111], a[76:79], v[220:223]\n"
    "s_addk_i32 s64, 0x1c00\n"
    "ds_read_b64_tr_b16 a[18:19], v6 offset:10240\n"
    "v_mfma_f32_16x16x32_bf16 v[224:227], a[104:107], a[80:83], v[224:227]\n"
    "v_mfma_f32_16x16x32_bf16 v[228:231], a[108:111], a[80:83], v[228:231]\n"
    "ds_read_b64_tr_b16 a[20:21], v7 offset:8192\n"
    "v_mfma_f32_16x16x32_bf16 v[232:235], a[104:107], a[84:87], v[232:235]\n"
    "buffer_load_dwordx4 v1, s[44:47], s64 offen offset:2048 lds\n"
    "v_mfma_f32_16x16x32_bf16 v[236:239], a[108:111], a[84:87], v[236:239]\n"
    "s_addk_i32 s64, 0x1c00\n"
    "ds_read_b64_tr_b16 a[22:23], v7 offset:10240\n"
    "v_mfma_f32_16x16x32_bf16 v[240:243], a[104:107], a[88:91], v[240:243]\n"
    "v_mfma_f32_16x16x32_bf16 v[244:247], a[108:111], a[88:91], v[244:247]\n"
    "ds_read_b64_tr_b16 a[24:25], v8 offset:8192\n"
    "v_mfma_f32_16x16x32_bf16 v[248:251], a[104:107], a[92:95], v[248:251]\n"
    "buffer_load_dwordx4 v1, s[44:47], s64 offen offset:3072 lds\n"
    "v_mfma_f32_16x16x32_bf16 v[252:255], a[108:111], a[92:95], v[252:255]\n"
    "s_addk_i32 s64, 0x2c00\n"
    "ds_read_b64_tr_b16 a[26:27], v8 offset:10240\n"
    "v_mfma_f32_16x16x32_bf16 v[32:35], a[112:115], a[64:67], v[32:35]\n"
    "s_addk_i32 m0, 0x1000\n"
    "v_mfma_f32_16x16x32_bf16 v[36:39], a[116:119], a[64:67], v[36:39]\n"
    "ds_read_b64_tr_b16 a[28:29], v9 offset:8192\n"
    "v_mfma_f32_16x16x32_bf16 v[40:43], a[112:115], a[68:71], v[40:43]\n"
    "buffer_load_dwordx4 v1, s[44:47], s64 offen offset:0 lds\n"
    "v_mfma_f32_16x16x32_bf16 v[44:47], a[116:119], a[68:71], v[44:47]\n"
    "s_addk_i32 s64, 0x1c00\n"
    "ds_read_b64_tr_b16 a[30:31], v9 offset:10240\n"
    "v_mfma_f32_16x16x32_bf16 v[48:51], a[112:115], a[72:75], v[48:51]\n"
    "v_mfma_f32_16x16x32_bf16 v[52:55], a[116:119], a[72:75], v[52:55]\n"
    "ds_read_b128 a[40:43], v4 offset:4096\n"
    "v_mfma_f32_16x16x32_bf16 v[56:59], a[112:115], a[76:79], v[56:59]\n"
    "buffer_load_dwordx4 v1, s[44:47], s64 offen offset:1024 lds\n"
    "v_mfma_f32_16x16x32_bf16 v[60:63], a[116:119], a[76:79], v[60:63]\n"
    "s_addk_i32 s64, 0x1c00\n"
    "ds_read_b128 a[44:47], v4 offset:6144\n"
    "v_mfma_f32_16x16x32_bf16 v[64:67], a[112:115], a[80:83], v[64:67]\n"
    "v_mfma_f32_16x16x32_bf16 v[68:71], a[116:119], a[80:83], v[68:71]\n"
    "ds_read_b128 a[48:51], v4 offset:8192\n"
    "v_mfma_f32_16x16x32_bf16 v[72:75], a[112:115], a[84:87], v[72:75]\n"
    "buffer_load_dwordx4 v1, s[44:47], s64 offen offset:2048 lds\n"
    "v_mfma_f32_16x16x32_bf16 v[76:79], a[116:119], a[84:87], v[76:79]\n"
    "s_addk_i32 s64, 0x1c00\n"
    "ds_read_b128 a[52:55], v4 offset:10240\n"
    "v_mfma_f32_16x16x32_bf16 v[80:83], a[112:115], a[88:91], v[80:83]\n"
    "v_mfma_f32_16x16x32_bf16 v[84:87], a[116:119], a[88:91], v[84:87]\n"
    "ds_read_b128 a[56:59], v4 offset:12288\n"
    "v_mfma_f32_16x16x32_bf16 v[88:91], a[112:115], a[92:95], v[88:91]\n"
    "buffer_load_dwordx4 v1, s[44:47], s64 offen offset:3072 lds\n"
    "v_mfma_f32_16x16x32_bf16 v[92:95], a[116:119], a[92:95], v[92:95]\n"
    "s_add_u32 s64, s64, 0xffff2c80\n"
    "ds_read_b128 a[60:63], v4 offset:14336\n"
    "v_mfma_f32_16x16x32_bf16 v[96:99], a[120:123], a[64:67], v[96:99]\n"
    "s_xor_b32 s66, s66, 0x10000\n"
    "v_mfma_f32_16x16x32_bf16 v[100:103], a[124:127], a[64:67], v[100:103]\n"
    "s_bitset0_b32 s64, 10\n"
    "v_mfma_f32_16x16x32_bf16 v[104:107], a[120:123], a[68:71], v[104:107]\n"
    "v_mfma_f32_16x16x32_bf16 v[108:111], a[124:127], a[68:71], v[108:111]\n"
    "v_mfma_f32_16x16x32_bf16 v[112:115], a[120:123], a[72:75], v[112:115]\n"
    "v_mfma_f32_16x16x32_bf16 v[116:119], a[124:127], a[72:75], v[116:119]\n"
    "buffer_load_dword v17, v16, s[52:55], s83 offen\n"
    "v_mfma_f32_16x16x32_bf16 v[120:123], a[120:123], a[76:79], v[120:123]\n"
    "s_add_u32 s83, s83, s84\n"
    "v_mfma_f32_16x16x32_bf16 v[124:127], a[124:127], a[76:79], v[124:127]\n"
    "v_mfma_f32_16x16x32_bf16 a[128:131], a[120:123], a[80:83], a[128:131]\n"
    "v_mfma_f32_16x16x32_bf16 a[132:135], a[124:127], a[80:83], a[132:135]\n"
    "v_mfma_f32_16x16x32_bf16 a[136:139], a[120:123], a[84:87], a[136:139]\n"
    "v_mfma_f32_16x16x32_bf16 a[140:143], a[124:127], a[84:87], a[140:143]\n"
    "v_mfma_f32_16x16x32_bf16 a[144:147], a[120:123], a[88:91], a[144:147]\n"
    "v_mfma_f32_16x16x32_bf16 a[148:151], a[124:127], a[88:91], a[148:151]\n"
    "s_waitcnt lgkmcnt(0)\n"
    "v_mfma_f32_16x16x32_bf16 a[152:155], a[120:123], a[92:95], a[152:155]\n"
    "v_mfma_f32_16x16x32_bf16 a[156:159], a[124:127], a[92:95], a[156:159]\n"
    "s_sub_u32 s63, s63, 1\n"
    "s_cbranch_scc0 KV_LOOP1_%=\n"
    "s_getpc_b64 s[38:39]\n"
    "PCE1_%=:\n"
    "s_lshr_b32 s59, %[bid], 3\n"
    "s_lshl2_add_u32 s59, s59, %[wid]\n"
    "s_mulk_i32 s59, 256\n"
    "v_lshrrev_b32 v19, 5, v12\n"
    "v_lshlrev_b32 v19, 7, v19\n"
    "s_add_u32 s59, s59, KV_DONE_%=-PCE1_%=\n"
    "s_min_u32 s59, s59, KVF_OK_%=-PCE1_%=-256\n"
    "s_add_u32 s38, s38, s59\n"
    "s_addc_u32 s39, s39, 0\n"
    "global_load_dword v19, v19, s[38:39]\n"
    "v_mfma_f32_16x16x32_bf16 v[128:131], a[32:35], a[0:3], v[128:131]\n"
    "s_add_u32 m0, s82, 0\n"
    "ds_read_b128 a[96:99], v5 offset:0\n"
    "v_mfma_f32_16x16x32_bf16 v[132:135], a[36:39], a[0:3], v[132:135]\n"
    "v_mfma_f32_16x16x32_bf16 v[136:139], a[32:35], a[4:7], v[136:139]\n"
    "buffer_load_dwordx4 v3, s[52:55], s81 offen offset:0 lds\n"
    "v_mfma_f32_16x16x32_bf16 v[140:143], a[36:39], a[4:7], v[140:143]\n"
    "s_add_u32 s81, s81, s68\n"
    "ds_read_b128 a[100:103], v5 offset:2048\n"
    "v_mfma_f32_16x16x32_bf16 v[144:147], a[32:35], a[8:11], v[144:147]\n"
    "v_mfma_f32_16x16x32_bf16 v[148:151], a[36:39], a[8:11], v[148:151]\n"
    "ds_read_b64_tr_b16 a[64:65], v6 offset:4096\n"
    "v_mfma_f32_16x16x32_bf16 v[152:155], a[32:35], a[12:15], v[152:155]\n"
    "v_mfma_f32_16x16x32_bf16 v[156:159], a[36:39], a[12:15], v[156:159]\n"
    "ds_read_b64_tr_b16 a[66:67], v6 offset:6144\n"
    "v_mfma_f32_16x16x32_bf16 v[160:163], a[32:35], a[16:19], v[160:163]\n"
    "buffer_load_dwordx4 v3, s[52:55], s81 offen offset:1024 lds\n"
    "v_mfma_f32_16x16x32_bf16 v[164:167], a[36:39], a[16:19], v[164:167]\n"
    "s_add_u32 s81, s81, s69\n"
    "ds_read_b64_tr_b16 a[68:69], v7 offset:4096\n"
    "v_mfma_f32_16x16x32_bf16 v[168:171], a[32:35], a[20:23], v[168:171]\n"
    "s_sub_u32 s60, s79, s78\n"
    "v_mfma_f32_16x16x32_bf16 v[172:175], a[36:39], a[20:23], v[172:175]\n"
    "ds_read_b64_tr_b16 a[70:71], v7 offset:6144\n"
    "v_mfma_f32_16x16x32_bf16 v[176:179], a[32:35], a[24:27], v[176:179]\n"
    "s_lshl_b32 s62, s75, 5\n"
    "v_mfma_f32_16x16x32_bf16 v[180:183], a[36:39], a[24:27], v[180:183]\n"
    "s_sub_u32 s61, s76, s75\n"
    "ds_read_b64_tr_b16 a[72:73], v8 offset:4096\n"
    "v_mfma_f32_16x16x32_bf16 v[184:187], a[32:35], a[28:31], v[184:187]\n"
    "buffer_load_dwordx4 v3, s[52:55], s81 offen offset:2048 lds\n"
    "v_mfma_f32_16x16x32_bf16 v[188:191], a[36:39], a[28:31], v[188:191]\n"
    "s_add_u32 s81, s81, s68\n"
    "ds_read_b64_tr_b16 a[74:75], v8 offset:6144\n"
    "v_mfma_f32_16x16x32_bf16 v[192:195], a[40:43], a[0:3], v[192:195]\n"
    "s_lshl_b32 s61, s61, 1\n"
    "v_mfma_f32_16x16x32_bf16 v[196:199], a[44:47], a[0:3], v[196:199]\n"
    "s_addk_i32 s62, 0x1000\n"
    "ds_read_b64_tr_b16 a[76:77], v9 offset:4096\n"
    "v_lshlrev_b32 v11, 7, v12\n"
    "v_mfma_f32_16x16x32_bf16 v[200:203], a[40:43], a[4:7], v[200:203]\n"
    "v_and_b32 v18, 16, v12\n"
    "v_mul_u32_u24 v18, s61, v18\n"
    "v_mfma_f32_16x16x32_bf16 v[204:207], a[44:47], a[4:7], v[204:207]\n"
    "ds_read_b64_tr_b16 a[78:79], v9 offset:6144\n"
    "v_and_b32 v10, 15, v12\n"
    "v_mfma_f32_16x16x32_bf16 v[208:211], a[40:43], a[8:11], v[208:211]\n"
    "buffer_load_dwordx4 v3, s[52:55], s81 offen offset:3072 lds\n"
    "v_mfma_f32_16x16x32_bf16 v[212:215], a[44:47], a[8:11], v[212:215]\n"
    "s_add_u32 s81, s81, s70\n"
    "ds_read_b64_tr_b16 a[80:81], v6 offset:12288\n"
    "v_lshl_add_u32 v10, v10, 2, v18\n"
    "v_mfma_f32_16x16x32_bf16 v[216:219], a[40:43], a[12:15], v[216:219]\n"
    "s_addk_i32 m0, 0x1000\n"
    "v_mfma_f32_16x16x32_bf16 v[220:223], a[44:47], a[12:15], v[220:223]\n"
    "ds_read_b64_tr_b16 a[82:83], v6 offset:14336\n"
    "v_mfma_f32_16x16x32_bf16 v[224:227], a[40:43], a[16:19], v[224:227]\n"
    "buffer_load_dword v1, v10, s[40:43], s62 offen offset:0 sc1\n"
    "v_mfma_f32_16x16x32_bf16 v[228:231], a[44:47], a[16:19], v[228:231]\n"
    "ds_read_b64_tr_b16 a[84:85], v7 offset:12288\n"
    "v_mfma_f32_16x16x32_bf16 v[232:235], a[40:43], a[20:23], v[232:235]\n"
    "buffer_load_dwordx4 v3, s[52:55], s81 offen offset:0 lds\n"
    "v_mfma_f32_16x16x32_bf16 v[236:239], a[44:47], a[20:23], v[236:239]\n"
    "s_add_u32 s81, s81, s68\n"
    "ds_read_b64_tr_b16 a[86:87], v7 offset:14336\n"
    "v_mfma_f32_16x16x32_bf16 v[240:243], a[40:43], a[24:27], v[240:243]\n"
    "v_mfma_f32_16x16x32_bf16 v[244:247], a[44:47], a[24:27], v[244:247]\n"
    "ds_read_b64_tr_b16 a[88:89], v8 offset:12288\n"
    "v_mfma_f32_16x16x32_bf16 v[248:251], a[40:43], a[28:31], v[248:251]\n"
    "v_mfma_f32_16x16x32_bf16 v[252:255], a[44:47], a[28:31], v[252:255]\n"
    "ds_read_b64_tr_b16 a[90:91], v8 offset:14336\n"
    "v_mfma_f32_16x16x32_bf16 v[32:35], a[48:51], a[0:3], v[32:35]\n"
    "buffer_load_dwordx4 v3, s[52:55], s81 offen offset:1024 lds\n"
    "v_mfma_f32_16x16x32_bf16 v[36:39], a[52:55], a[0:3], v[36:39]\n"
    "s_add_u32 s81, s81, s69\n"
    "ds_read_b64_tr_b16 a[92:93], v9 offset:12288\n"
    "v_mfma_f32_16x16x32_bf16 v[40:43], a[48:51], a[4:7], v[40:43]\n"
    "v_mfma_f32_16x16x32_bf16 v[44:47], a[52:55], a[4:7], v[44:47]\n"
    "ds_read_b64_tr_b16 a[94:95], v9 offset:14336\n"
    "v_mfma_f32_16x16x32_bf16 v[48:51], a[48:51], a[8:11], v[48:51]\n"
    "v_mfma_f32_16x16x32_bf16 v[52:55], a[52:55], a[8:11], v[52:55]\n"
    "ds_read_b128 a[104:107], v5 offset:4096\n"
    "v_mfma_f32_16x16x32_bf16 v[56:59], a[48:51], a[12:15], v[56:59]\n"
    "buffer_load_dwordx4 v3, s[52:55], s81 offen offset:2048 lds\n"
    "v_mfma_f32_16x16x32_bf16 v[60:63], a[52:55], a[12:15], v[60:63]\n"
    "s_add_u32 s81, s81, s68\n"
    "ds_read_b128 a[108:111], v5 offset:6144\n"
    "v_mfma_f32_16x16x32_bf16 v[64:67], a[48:51], a[16:19], v[64:67]\n"
    "v_mfma_f32_16x16x32_bf16 v[68:71], a[52:55], a[16:19], v[68:71]\n"
    "ds_read_b128 a[112:115], v5 offset:8192\n"
    "v_mfma_f32_16x16x32_bf16 v[72:75], a[48:51], a[20:23], v[72:75]\n"
    "v_mfma_f32_16x16x32_bf16 v[76:79], a[52:55], a[20:23], v[76:79]\n"
    "ds_read_b128 a[116:119], v5 offset:10240\n"
    "v_mfma_f32_16x16x32_bf16 v[80:83], a[48:51], a[24:27], v[80:83]\n"
    "buffer_load_dwordx4 v3, s[52:55], s81 offen offset:3072 lds\n"
    "v_mfma_f32_16x16x32_bf16 v[84:87], a[52:55], a[24:27], v[84:87]\n"
    "s_add_u32 s81, s81, s70\n"
    "ds_read_b128 a[120:123], v5 offset:12288\n"
    "v_mfma_f32_16x16x32_bf16 v[88:91], a[48:51], a[28:31], v[88:91]\n"
    "s_mov_b32 s59, s78\n"
    "v_mfma_f32_16x16x32_bf16 v[92:95], a[52:55], a[28:31], v[92:95]\n"
    "s_mov_b32 s78, s79\n"
    "ds_read_b128 a[124:127], v5 offset:14336\n"
    "v_mfma_f32_16x16x32_bf16 v[96:99], a[56:59], a[0:3], v[96:99]\n"
    "s_mov_b32 s79, s80\n"
    "v_mfma_f32_16x16x32_bf16 v[100:103], a[60:63], a[0:3], v[100:103]\n"
    "s_mov_b32 s80, s59\n"
    "v_xor_b32 v4, 0x10000, v4\n"
    "v_xor_b32 v5, 0x10000, v5\n"
    "v_mfma_f32_16x16x32_bf16 v[104:107], a[56:59], a[4:7], v[104:107]\n"
    "v_add_u32 v6, s60, v6\n"
    "v_add_u32 v7, s60, v7\n"
    "v_mfma_f32_16x16x32_bf16 v[108:111], a[60:63], a[4:7], v[108:111]\n"
    "v_add_u32 v8, s60, v8\n"
    "v_add_u32 v9, s60, v9\n"
    "v_mfma_f32_16x16x32_bf16 v[112:115], a[56:59], a[8:11], v[112:115]\n"
    "v_mfma_f32_16x16x32_bf16 v[116:119], a[60:63], a[8:11], v[116:119]\n"
    "v_mfma_f32_16x16x32_bf16 v[120:123], a[56:59], a[12:15], v[120:123]\n"
    "v_mfma_f32_16x16x32_bf16 v[124:127], a[60:63], a[12:15], v[124:127]\n"
    "v_mfma_f32_16x16x32_bf16 a[128:131], a[56:59], a[16:19], a[128:131]\n"
    "v_mfma_f32_16x16x32_bf16 a[132:135], a[60:63], a[16:19], a[132:135]\n"
    "v_mfma_f32_16x16x32_bf16 a[136:139], a[56:59], a[20:23], a[136:139]\n"
    "v_mfma_f32_16x16x32_bf16 a[140:143], a[60:63], a[20:23], a[140:143]\n"
    "v_mfma_f32_16x16x32_bf16 a[144:147], a[56:59], a[24:27], a[144:147]\n"
    "s_waitcnt lgkmcnt(0)\n"
    "v_mfma_f32_16x16x32_bf16 a[148:151], a[60:63], a[24:27], a[148:151]\n"
    "s_waitcnt vmcnt(11)\n"
    "v_mfma_f32_16x16x32_bf16 a[152:155], a[56:59], a[28:31], a[152:155]\n"
    "s_barrier\n"
    "v_mfma_f32_16x16x32_bf16 a[156:159], a[60:63], a[28:31], a[156:159]\n"
    "v_mfma_f32_16x16x32_bf16 v[128:131], a[96:99], a[64:67], v[128:131]\n"
    "s_add_u32 m0, s82, 0x8000\n"
    "ds_read_b128 a[32:35], v4 offset:0\n"
    "v_mfma_f32_16x16x32_bf16 v[132:135], a[100:103], a[64:67], v[132:135]\n"
    "v_mfma_f32_16x16x32_bf16 v[136:139], a[96:99], a[68:71], v[136:139]\n"
    "buffer_load_dwordx4 v3, s[52:55], s81 offen offset:0 lds\n"
    "v_mfma_f32_16x16x32_bf16 v[140:143], a[100:103], a[68:71], v[140:143]\n"
    "s_add_u32 s81, s81, s68\n"
    "ds_read_b128 a[36:39], v4 offset:2048\n"
    "v_mfma_f32_16x16x32_bf16 v[144:147], a[96:99], a[72:75], v[144:147]\n"
    "v_mfma_f32_16x16x32_bf16 v[148:151], a[100:103], a[72:75], v[148:151]\n"
    "ds_read_b64_tr_b16 a[0:1], v6 offset:0\n"
    "v_mfma_f32_16x16x32_bf16 v[152:155], a[96:99], a[76:79], v[152:155]\n"
    "v_mfma_f32_16x16x32_bf16 v[156:159], a[100:103], a[76:79], v[156:159]\n"
    "ds_read_b64_tr_b16 a[2:3], v6 offset:2048\n"
    "v_mfma_f32_16x16x32_bf16 v[160:163], a[96:99], a[80:83], v[160:163]\n"
    "buffer_load_dwordx4 v3, s[52:55], s81 offen offset:1024 lds\n"
    "v_mfma_f32_16x16x32_bf16 v[164:167], a[100:103], a[80:83], v[164:167]\n"
    "s_add_u32 s81, s81, s69\n"
    "ds_read_b64_tr_b16 a[4:5], v7 offset:0\n"
    "v_mfma_f32_16x16x32_bf16 v[168:171], a[96:99], a[84:87], v[168:171]\n"
    "v_mfma_f32_16x16x32_bf16 v[172:175], a[100:103], a[84:87], v[172:175]\n"
    "ds_read_b64_tr_b16 a[6:7], v7 offset:2048\n"
    "v_mfma_f32_16x16x32_bf16 v[176:179], a[96:99], a[88:91], v[176:179]\n"
    "v_mfma_f32_16x16x32_bf16 v[180:183], a[100:103], a[88:91], v[180:183]\n"
    "ds_read_b64_tr_b16 a[8:9], v8 offset:0\n"
    "v_mfma_f32_16x16x32_bf16 v[184:187], a[96:99], a[92:95], v[184:187]\n"
    "buffer_load_dwordx4 v3, s[52:55], s81 offen offset:2048 lds\n"
    "v_mfma_f32_16x16x32_bf16 v[188:191], a[100:103], a[92:95], v[188:191]\n"
    "s_add_u32 s81, s81, s68\n"
    "ds_read_b64_tr_b16 a[10:11], v8 offset:2048\n"
    "v_mfma_f32_16x16x32_bf16 v[192:195], a[104:107], a[64:67], v[192:195]\n"
    "v_mfma_f32_16x16x32_bf16 v[196:199], a[108:111], a[64:67], v[196:199]\n"
    "ds_read_b64_tr_b16 a[12:13], v9 offset:0\n"
    "v_mfma_f32_16x16x32_bf16 v[200:203], a[104:107], a[68:71], v[200:203]\n"
    "v_mfma_f32_16x16x32_bf16 v[204:207], a[108:111], a[68:71], v[204:207]\n"
    "ds_read_b64_tr_b16 a[14:15], v9 offset:2048\n"
    "v_mfma_f32_16x16x32_bf16 v[208:211], a[104:107], a[72:75], v[208:211]\n"
    "buffer_load_dwordx4 v3, s[52:55], s81 offen offset:3072 lds\n"
    "v_mfma_f32_16x16x32_bf16 v[212:215], a[108:111], a[72:75], v[212:215]\n"
    "s_add_u32 s81, s81, s70\n"
    "ds_read_b64_tr_b16 a[16:17], v6 offset:8192\n"
    "v_mfma_f32_16x16x32_bf16 v[216:219], a[104:107], a[76:79], v[216:219]\n"
    "s_addk_i32 m0, 0x1000\n"
    "v_mfma_f32_16x16x32_bf16 v[220:223], a[108:111], a[76:79], v[220:223]\n"
    "ds_read_b64_tr_b16 a[18:19], v6 offset:10240\n"
    "v_mfma_f32_16x16x32_bf16 v[224:227], a[104:107], a[80:83], v[224:227]\n"
    "v_mfma_f32_16x16x32_bf16 v[228:231], a[108:111], a[80:83], v[228:231]\n"
    "ds_read_b64_tr_b16 a[20:21], v7 offset:8192\n"
    "v_mfma_f32_16x16x32_bf16 v[232:235], a[104:107], a[84:87], v[232:235]\n"
    "buffer_load_dwordx4 v3, s[52:55], s81 offen offset:0 lds\n"
    "v_mfma_f32_16x16x32_bf16 v[236:239], a[108:111], a[84:87], v[236:239]\n"
    "s_add_u32 s81, s81, s68\n"
    "ds_read_b64_tr_b16 a[22:23], v7 offset:10240\n"
    "v_mfma_f32_16x16x32_bf16 v[240:243], a[104:107], a[88:91], v[240:243]\n"
    "v_mfma_f32_16x16x32_bf16 v[244:247], a[108:111], a[88:91], v[244:247]\n"
    "ds_read_b64_tr_b16 a[24:25], v8 offset:8192\n"
    "v_mfma_f32_16x16x32_bf16 v[248:251], a[104:107], a[92:95], v[248:251]\n"
    "v_mfma_f32_16x16x32_bf16 v[252:255], a[108:111], a[92:95], v[252:255]\n"
    "ds_read_b64_tr_b16 a[26:27], v8 offset:10240\n"
    "v_mfma_f32_16x16x32_bf16 v[32:35], a[112:115], a[64:67], v[32:35]\n"
    "buffer_load_dwordx4 v3, s[52:55], s81 offen offset:1024 lds\n"
    "v_mfma_f32_16x16x32_bf16 v[36:39], a[116:119], a[64:67], v[36:39]\n"
    "s_add_u32 s81, s81, s69\n"
    "ds_read_b64_tr_b16 a[28:29], v9 offset:8192\n"
    "v_mfma_f32_16x16x32_bf16 v[40:43], a[112:115], a[68:71], v[40:43]\n"
    "v_mfma_f32_16x16x32_bf16 v[44:47], a[116:119], a[68:71], v[44:47]\n"
    "ds_read_b64_tr_b16 a[30:31], v9 offset:10240\n"
    "v_mfma_f32_16x16x32_bf16 v[48:51], a[112:115], a[72:75], v[48:51]\n"
    "v_mfma_f32_16x16x32_bf16 v[52:55], a[116:119], a[72:75], v[52:55]\n"
    "ds_read_b128 a[40:43], v4 offset:4096\n"
    "v_mfma_f32_16x16x32_bf16 v[56:59], a[112:115], a[76:79], v[56:59]\n"
    "buffer_load_dwordx4 v3, s[52:55], s81 offen offset:2048 lds\n"
    "v_mfma_f32_16x16x32_bf16 v[60:63], a[116:119], a[76:79], v[60:63]\n"
    "s_add_u32 s81, s81, s68\n"
    "ds_read_b128 a[44:47], v4 offset:6144\n"
    "v_mfma_f32_16x16x32_bf16 v[64:67], a[112:115], a[80:83], v[64:67]\n"
    "v_mfma_f32_16x16x32_bf16 v[68:71], a[116:119], a[80:83], v[68:71]\n"
    "ds_read_b128 a[48:51], v4 offset:8192\n"
    "v_mfma_f32_16x16x32_bf16 v[72:75], a[112:115], a[84:87], v[72:75]\n"
    "v_mfma_f32_16x16x32_bf16 v[76:79], a[116:119], a[84:87], v[76:79]\n"
    "ds_read_b128 a[52:55], v4 offset:10240\n"
    "v_mfma_f32_16x16x32_bf16 v[80:83], a[112:115], a[88:91], v[80:83]\n"
    "buffer_load_dwordx4 v3, s[52:55], s81 offen offset:3072 lds\n"
    "v_mfma_f32_16x16x32_bf16 v[84:87], a[116:119], a[88:91], v[84:87]\n"
    "s_add_u32 s81, s81, s70\n"
    "ds_read_b128 a[56:59], v4 offset:12288\n"
    "v_mfma_f32_16x16x32_bf16 v[88:91], a[112:115], a[92:95], v[88:91]\n"
    "v_mfma_f32_16x16x32_bf16 v[92:95], a[116:119], a[92:95], v[92:95]\n"
    "s_waitcnt vmcnt(12)\n"
    "ds_read_b128 a[60:63], v4 offset:14336\n"
    "v_mfma_f32_16x16x32_bf16 v[96:99], a[120:123], a[64:67], v[96:99]\n"
    "v_mfma_f32_16x16x32_bf16 v[100:103], a[124:127], a[64:67], v[100:103]\n"
    "v_cmp_ne_u32 vcc, v1, v13\n"
    "s_nop 1\n"
    "s_cbranch_vccnz LNF1_SPIN_%=\n"
    "LNF1_OK_%=:\n"
    "v_mfma_f32_16x16x32_bf16 v[104:107], a[120:123], a[68:71], v[104:107]\n"
    "v_mfma_f32_16x16x32_bf16 v[108:111], a[124:127], a[68:71], v[108:111]\n"
    "v_mfma_f32_16x16x32_bf16 v[112:115], a[120:123], a[72:75], v[112:115]\n"
    "buffer_load_dwordx4 a[160:163], v0, s[40:43], s71 offen offset:0 sc1\n"
    "v_mfma_f32_16x16x32_bf16 v[116:119], a[124:127], a[72:75], v[116:119]\n"
    "v_mfma_f32_16x16x32_bf16 v[120:123], a[120:123], a[76:79], v[120:123]\n"
    "buffer_load_dwordx4 a[164:167], v0, s[40:43], s72 offen offset:0 sc1\n"
    "v_mfma_f32_16x16x32_bf16 v[124:127], a[124:127], a[76:79], v[124:127]\n"
    "v_mfma_f32_16x16x32_bf16 a[128:131], a[120:123], a[80:83], a[128:131]\n"
    "buffer_load_dwordx4 a[168:171], v0, s[40:43], s73 offen offset:0 sc1\n"
    "v_mfma_f32_16x16x32_bf16 a[132:135], a[124:127], a[80:83], a[132:135]\n"
    "v_mfma_f32_16x16x32_bf16 a[136:139], a[120:123], a[84:87], a[136:139]\n"
    "buffer_load_dwordx4 a[172:175], v0, s[40:43], s74 offen offset:0 sc1\n"
    "v_mfma_f32_16x16x32_bf16 a[140:143], a[124:127], a[84:87], a[140:143]\n"
    "v_mfma_f32_16x16x32_bf16 a[144:147], a[120:123], a[88:91], a[144:147]\n"
    "v_mfma_f32_16x16x32_bf16 a[148:151], a[124:127], a[88:91], a[148:151]\n"
    "s_waitcnt lgkmcnt(0)\n"
    "v_mfma_f32_16x16x32_bf16 a[152:155], a[120:123], a[92:95], a[152:155]\n"
    "v_mfma_f32_16x16x32_bf16 a[156:159], a[124:127], a[92:95], a[156:159]\n"
    "v_mfma_f32_16x16x32_bf16 v[128:131], a[32:35], a[0:3], v[128:131]\n"
    "s_add_u32 m0, s82, 0x10000\n"
    "ds_read_b128 a[96:99], v5 offset:0\n"
    "v_mfma_f32_16x16x32_bf16 v[132:135], a[36:39], a[0:3], v[132:135]\n"
    "v_mfma_f32_16x16x32_bf16 v[136:139], a[32:35], a[4:7], v[136:139]\n"
    "buffer_load_dwordx4 v3, s[52:55], s81 offen offset:0 lds\n"
    "v_mfma_f32_16x16x32_bf16 v[140:143], a[36:39], a[4:7], v[140:143]\n"
    "s_add_u32 s81, s81, s68\n"
    "ds_read_b128 a[100:103], v5 offset:2048\n"
    "v_mfma_f32_16x16x32_bf16 v[144:147], a[32:35], a[8:11], v[144:147]\n"
    "v_mfma_f32_16x16x32_bf16 v[148:151], a[36:39], a[8:11], v[148:151]\n"
    "ds_read_b64_tr_b16 a[64:65], v6 offset:4096\n"
    "v_mfma_f32_16x16x32_bf16 v[152:155], a[32:35], a[12:15], v[152:155]\n"
    "v_mfma_f32_16x16x32_bf16 v[156:159], a[36:39], a[12:15], v[156:159]\n"
    "ds_read_b64_tr_b16 a[66:67], v6 offset:6144\n"
    "v_mfma_f32_16x16x32_bf16 v[160:163], a[32:35], a[16:19], v[160:163]\n"
    "buffer_load_dwordx4 v3, s[52:55], s81 offen offset:1024 lds\n"
    "v_mfma_f32_16x16x32_bf16 v[164:167], a[36:39], a[16:19], v[164:167]\n"
    "s_add_u32 s81, s81, s69\n"
    "ds_read_b64_tr_b16 a[68:69], v7 offset:4096\n"
    "v_mfma_f32_16x16x32_bf16 v[168:171], a[32:35], a[20:23], v[168:171]\n"
    "v_mfma_f32_16x16x32_bf16 v[172:175], a[36:39], a[20:23], v[172:175]\n"
    "ds_read_b64_tr_b16 a[70:71], v7 offset:6144\n"
    "v_mfma_f32_16x16x32_bf16 v[176:179], a[32:35], a[24:27], v[176:179]\n"
    "v_mfma_f32_16x16x32_bf16 v[180:183], a[36:39], a[24:27], v[180:183]\n"
    "ds_read_b64_tr_b16 a[72:73], v8 offset:4096\n"
    "v_mfma_f32_16x16x32_bf16 v[184:187], a[32:35], a[28:31], v[184:187]\n"
    "buffer_load_dwordx4 v3, s[52:55], s81 offen offset:2048 lds\n"
    "v_mfma_f32_16x16x32_bf16 v[188:191], a[36:39], a[28:31], v[188:191]\n"
    "s_add_u32 s81, s81, s68\n"
    "ds_read_b64_tr_b16 a[74:75], v8 offset:6144\n"
    "v_mfma_f32_16x16x32_bf16 v[192:195], a[40:43], a[0:3], v[192:195]\n"
    "v_mfma_f32_16x16x32_bf16 v[196:199], a[44:47], a[0:3], v[196:199]\n"
    "ds_read_b64_tr_b16 a[76:77], v9 offset:4096\n"
    "v_mfma_f32_16x16x32_bf16 v[200:203], a[40:43], a[4:7], v[200:203]\n"
    "v_mfma_f32_16x16x32_bf16 v[204:207], a[44:47], a[4:7], v[204:207]\n"
    "ds_read_b64_tr_b16 a[78:79], v9 offset:6144\n"
    "v_mfma_f32_16x16x32_bf16 v[208:211], a[40:43], a[8:11], v[208:211]\n"
    "buffer_load_dwordx4 v3, s[52:55], s81 offen offset:3072 lds\n"
    "v_mfma_f32_16x16x32_bf16 v[212:215], a[44:47], a[8:11], v[212:215]\n"
    "s_add_u32 s81, s81, s70\n"
    "ds_read_b64_tr_b16 a[80:81], v6 offset:12288\n"
    "v_mfma_f32_16x16x32_bf16 v[216:219], a[40:43], a[12:15], v[216:219]\n"
    "s_addk_i32 m0, 0x1000\n"
    "v_mfma_f32_16x16x32_bf16 v[220:223], a[44:47], a[12:15], v[220:223]\n"
    "ds_read_b64_tr_b16 a[82:83], v6 offset:14336\n"
    "v_mfma_f32_16x16x32_bf16 v[224:227], a[40:43], a[16:19], v[224:227]\n"
    "v_mfma_f32_16x16x32_bf16 v[228:231], a[44:47], a[16:19], v[228:231]\n"
    "ds_read_b64_tr_b16 a[84:85], v7 offset:12288\n"
    "v_mfma_f32_16x16x32_bf16 v[232:235], a[40:43], a[20:23], v[232:235]\n"
    "buffer_load_dwordx4 v3, s[52:55], s81 offen offset:0 lds\n"
    "v_mfma_f32_16x16x32_bf16 v[236:239], a[44:47], a[20:23], v[236:239]\n"
    "s_add_u32 s81, s81, s68\n"
    "ds_read_b64_tr_b16 a[86:87], v7 offset:14336\n"
    "v_mfma_f32_16x16x32_bf16 v[240:243], a[40:43], a[24:27], v[240:243]\n"
    "v_mfma_f32_16x16x32_bf16 v[244:247], a[44:47], a[24:27], v[244:247]\n"
    "ds_read_b64_tr_b16 a[88:89], v8 offset:12288\n"
    "v_mfma_f32_16x16x32_bf16 v[248:251], a[40:43], a[28:31], v[248:251]\n"
    "v_mfma_f32_16x16x32_bf16 v[252:255], a[44:47], a[28:31], v[252:255]\n"
    "ds_read_b64_tr_b16 a[90:91], v8 offset:14336\n"
    "v_mfma_f32_16x16x32_bf16 v[32:35], a[48:51], a[0:3], v[32:35]\n"
    "buffer_load_dwordx4 v3, s[52:55], s81 offen offset:1024 lds\n"
    "v_mfma_f32_16x16x32_bf16 v[36:39], a[52:55], a[0:3], v[36:39]\n"
    "s_add_u32 s81, s81, s69\n"
    "ds_read_b64_tr_b16 a[92:93], v9 offset:12288\n"
    "v_mfma_f32_16x16x32_bf16 v[40:43], a[48:51], a[4:7], v[40:43]\n"
    "v_mfma_f32_16x16x32_bf16 v[44:47], a[52:55], a[4:7], v[44:47]\n"
    "ds_read_b64_tr_b16 a[94:95], v9 offset:14336\n"
    "v_mfma_f32_16x16x32_bf16 v[48:51], a[48:51], a[8:11], v[48:51]\n"
    "v_mfma_f32_16x16x32_bf16 v[52:55], a[52:55], a[8:11], v[52:55]\n"
    "ds_read_b128 a[104:107], v5 offset:4096\n"
    "v_mfma_f32_16x16x32_bf16 v[56:59], a[48:51], a[12:15], v[56:59]\n"
    "buffer_load_dwordx4 v3, s[52:55], s81 offen offset:2048 lds\n"
    "v_mfma_f32_16x16x32_bf16 v[60:63], a[52:55], a[12:15], v[60:63]\n"
    "s_add_u32 s81, s81, s68\n"
    "ds_read_b128 a[108:111], v5 offset:6144\n"
    "v_mfma_f32_16x16x32_bf16 v[64:67], a[48:51], a[16:19], v[64:67]\n"
    "v_mfma_f32_16x16x32_bf16 v[68:71], a[52:55], a[16:19], v[68:71]\n"
    "ds_read_b128 a[112:115], v5 offset:8192\n"
    "v_mfma_f32_16x16x32_bf16 v[72:75], a[48:51], a[20:23], v[72:75]\n"
    "v_mfma_f32_16x16x32_bf16 v[76:79], a[52:55], a[20:23], v[76:79]\n"
    "ds_read_b128 a[116:119], v5 offset:10240\n"
    "v_mfma_f32_16x16x32_bf16 v[80:83], a[48:51], a[24:27], v[80:83]\n"
    "buffer_load_dwordx4 v3, s[52:55], s81 offen offset:3072 lds\n"
    "v_mfma_f32_16x16x32_bf16 v[84:87], a[52:55], a[24:27], v[84:87]\n"
    "s_add_u32 s81, s81, s70\n"
    "ds_read_b128 a[120:123], v5 offset:12288\n"
    "v_mfma_f32_16x16x32_bf16 v[88:91], a[48:51], a[28:31], v[88:91]\n"
    "v_mfma_f32_16x16x32_bf16 v[92:95], a[52:55], a[28:31], v[92:95]\n"
    "ds_read_b128 a[124:127], v5 offset:14336\n"
    "v_mfma_f32_16x16x32_bf16 v[96:99], a[56:59], a[0:3], v[96:99]\n"
    "v_mfma_f32_16x16x32_bf16 v[100:103], a[60:63], a[0:3], v[100:103]\n"
    "v_mfma_f32_16x16x32_bf16 v[104:107], a[56:59], a[4:7], v[104:107]\n"
    "buffer_load_dwordx4 a[176:179], v0, s[40:43], s71 offen offset:1024 sc1\n"
    "v_mfma_f32_16x16x32_bf16 v[108:111], a[60:63], a[4:7], v[108:111]\n"
    "v_mfma_f32_16x16x32_bf16 v[112:115], a[56:59], a[8:11], v[112:115]\n"
    "buffer_load_dwordx4 a[180:183], v0, s[40:43], s72 offen offset:1024 sc1\n"
    "v_mfma_f32_16x16x32_bf16 v[116:119], a[60:63], a[8:11], v[116:119]\n"
    "v_mfma_f32_16x16x32_bf16 v[120:123], a[56:59], a[12:15], v[120:123]\n"
    "buffer_load_dwordx4 a[184:187], v0, s[40:43], s73 offen offset:1024 sc1\n"
    "v_mfma_f32_16x16x32_bf16 v[124:127], a[60:63], a[12:15], v[124:127]\n"
    "v_mfma_f32_16x16x32_bf16 a[128:131], a[56:59], a[16:19], a[128:131]\n"
    "buffer_load_dwordx4 a[188:191], v0, s[40:43], s74 offen offset:1024 sc1\n"
    "v_mfma_f32_16x16x32_bf16 a[132:135], a[60:63], a[16:19], a[132:135]\n"
    "v_mfma_f32_16x16x32_bf16 a[136:139], a[56:59], a[20:23], a[136:139]\n"
    "v_mfma_f32_16x16x32_bf16 a[140:143], a[60:63], a[20:23], a[140:143]\n"
    "v_mfma_f32_16x16x32_bf16 a[144:147], a[56:59], a[24:27], a[144:147]\n"
    "s_waitcnt lgkmcnt(0)\n"
    "v_mfma_f32_16x16x32_bf16 a[148:151], a[60:63], a[24:27], a[148:151]\n"
    "s_waitcnt vmcnt(24)\n"
    "v_mfma_f32_16x16x32_bf16 a[152:155], a[56:59], a[28:31], a[152:155]\n"
    "s_barrier\n"
    "v_mfma_f32_16x16x32_bf16 a[156:159], a[60:63], a[28:31], a[156:159]\n"
    "v_mfma_f32_16x16x32_bf16 v[128:131], a[96:99], a[64:67], v[128:131]\n"
    "v_add_u32 v6, 0xfffdc000, v6\n"
    "v_add_u32 v7, 0xfffdc000, v7\n"
    "v_mfma_f32_16x16x32_bf16 v[132:135], a[100:103], a[64:67], v[132:135]\n"
    "buffer_load_dwordx4 a[192:195], v0, s[40:43], s71 offen offset:2048 sc1\n"
    "v_mfma_f32_16x16x32_bf16 v[136:139], a[96:99], a[68:71], v[136:139]\n"
    "buffer_load_dwordx4 a[196:199], v0, s[40:43], s72 offen offset:2048 sc1\n"
    "v_mfma_f32_16x16x32_bf16 v[140:143], a[100:103], a[68:71], v[140:143]\n"
    "buffer_load_dwordx4 a[200:203], v0, s[40:43], s73 offen offset:2048 sc1\n"
    "v_mfma_f32_16x16x32_bf16 v[144:147], a[96:99], a[72:75], v[144:147]\n"
    "buffer_load_dwordx4 a[204:207], v0, s[40:43], s74 offen offset:2048 sc1\n"
    "v_mfma_f32_16x16x32_bf16 v[148:151], a[100:103], a[72:75], v[148:151]\n"
    "v_add_u32 v8, 0xfffdc000, v8\n"
    "v_add_u32 v9, 0xfffdc000, v9\n"
    "v_mfma_f32_16x16x32_bf16 v[152:155], a[96:99], a[76:79], v[152:155]\n"
    "s_add_u32 m0, s82, 0x18000\n"
    "v_cvt_pk_bf16_f32 v128, v128, v129\n"
    "v_cvt_pk_bf16_f32 v129, v130, v131\n"
    "v_mfma_f32_16x16x32_bf16 v[156:159], a[100:103], a[76:79], v[156:159]\n"
    "v_cvt_pk_bf16_f32 v130, v132, v133\n"
    "v_cvt_pk_bf16_f32 v131, v134, v135\n"
    "v_mfma_f32_16x16x32_bf16 v[160:163], a[96:99], a[80:83], v[160:163]\n"
    "buffer_load_dwordx4 v3, s[52:55], s81 offen offset:0 lds\n"
    "v_mfma_f32_16x16x32_bf16 v[164:167], a[100:103], a[80:83], v[164:167]\n"
    "s_add_u32 s81, s81, s68\n"
    "v_cvt_pk_bf16_f32 v132, v136, v137\n"
    "v_cvt_pk_bf16_f32 v133, v138, v139\n"
    "v_mfma_f32_16x16x32_bf16 v[168:171], a[96:99], a[84:87], v[168:171]\n"
    "v_cvt_pk_bf16_f32 v134, v140, v141\n"
    "v_cvt_pk_bf16_f32 v135, v142, v143\n"
    "v_mfma_f32_16x16x32_bf16 v[172:175], a[100:103], a[84:87], v[172:175]\n"
    "v_cvt_pk_bf16_f32 v136, v144, v145\n"
    "v_cvt_pk_bf16_f32 v137, v146, v147\n"
    "v_mfma_f32_16x16x32_bf16 v[176:179], a[96:99], a[88:91], v[176:179]\n"
    "v_cvt_pk_bf16_f32 v138, v148, v149\n"
    "v_cvt_pk_bf16_f32 v139, v150, v151\n"
    "v_mfma_f32_16x16x32_bf16 v[180:183], a[100:103], a[88:91], v[180:183]\n"
    "buffer_load_dwordx4 v3, s[52:55], s81 offen offset:1024 lds\n"
    "v_mfma_f32_16x16x32_bf16 v[184:187], a[96:99], a[92:95], v[184:187]\n"
    "s_add_u32 s81, s81, s69\n"
    "v_cvt_pk_bf16_f32 v140, v152, v153\n"
    "v_cvt_pk_bf16_f32 v141, v154, v155\n"
    "v_mfma_f32_16x16x32_bf16 v[188:191], a[100:103], a[92:95], v[188:191]\n"
    "v_cvt_pk_bf16_f32 v142, v156, v157\n"
    "v_cvt_pk_bf16_f32 v143, v158, v159\n"
    "v_mfma_f32_16x16x32_bf16 v[192:195], a[104:107], a[64:67], v[192:195]\n"
    "v_cvt_pk_bf16_f32 v144, v160, v161\n"
    "v_cvt_pk_bf16_f32 v145, v162, v163\n"
    "v_mfma_f32_16x16x32_bf16 v[196:199], a[108:111], a[64:67], v[196:199]\n"
    "v_cvt_pk_bf16_f32 v146, v164, v165\n"
    "v_cvt_pk_bf16_f32 v147, v166, v167\n"
    "v_mfma_f32_16x16x32_bf16 v[200:203], a[104:107], a[68:71], v[200:203]\n"
    "buffer_load_dwordx4 v3, s[52:55], s81 offen offset:2048 lds\n"
    "v_mfma_f32_16x16x32_bf16 v[204:207], a[108:111], a[68:71], v[204:207]\n"
    "s_add_u32 s81, s81, s68\n"
    "v_cvt_pk_bf16_f32 v148, v168, v169\n"
    "v_cvt_pk_bf16_f32 v149, v170, v171\n"
    "v_mfma_f32_16x16x32_bf16 v[208:211], a[104:107], a[72:75], v[208:211]\n"
    "v_cvt_pk_bf16_f32 v150, v172, v173\n"
    "v_cvt_pk_bf16_f32 v151, v174, v175\n"
    "v_mfma_f32_16x16x32_bf16 v[212:215], a[108:111], a[72:75], v[212:215]\n"
    "v_cvt_pk_bf16_f32 v152, v176, v177\n"
    "v_cvt_pk_bf16_f32 v153, v178, v179\n"
    "v_mfma_f32_16x16x32_bf16 v[216:219], a[104:107], a[76:79], v[216:219]\n"
    "v_cvt_pk_bf16_f32 v154, v180, v181\n"
    "v_cvt_pk_bf16_f32 v155, v182, v183\n"
    "v_mfma_f32_16x16x32_bf16 v[220:223], a[108:111], a[76:79], v[220:223]\n"
    "buffer_load_dwordx4 v3, s[52:55], s81 offen offset:3072 lds\n"
    "v_mfma_f32_16x16x32_bf16 v[224:227], a[104:107], a[80:83], v[224:227]\n"
    "s_add_u32 s81, s81, s70\n"
    "v_cvt_pk_bf16_f32 v156, v184, v185\n"
    "v_cvt_pk_bf16_f32 v157, v186, v187\n"
    "v_mfma_f32_16x16x32_bf16 v[228:231], a[108:111], a[80:83], v[228:231]\n"
    "s_addk_i32 m0, 0x1000\n"
    "v_cvt_pk_bf16_f32 v158, v188, v189\n"
    "v_cvt_pk_bf16_f32 v159, v190, v191\n"
    "v_mfma_f32_16x16x32_bf16 v[232:235], a[104:107], a[84:87], v[232:235]\n"
    "v_cvt_pk_bf16_f32 v160, v192, v193\n"
    "v_cvt_pk_bf16_f32 v161, v194, v195\n"
    "v_mfma_f32_16x16x32_bf16 v[236:239], a[108:111], a[84:87], v[236:239]\n"
    "v_cvt_pk_bf16_f32 v162, v196, v197\n"
    "v_cvt_pk_bf16_f32 v163, v198, v199\n"
    "v_mfma_f32_16x16x32_bf16 v[240:243], a[104:107], a[88:91], v[240:243]\n"
    "buffer_load_dwordx4 v3, s[52:55], s81 offen offset:0 lds\n"
    "v_mfma_f32_16x16x32_bf16 v[244:247], a[108:111], a[88:91], v[244:247]\n"
    "s_add_u32 s81, s81, s68\n"
    "v_cvt_pk_bf16_f32 v164, v200, v201\n"
    "v_cvt_pk_bf16_f32 v165, v202, v203\n"
    "v_mfma_f32_16x16x32_bf16 v[248:251], a[104:107], a[92:95], v[248:251]\n"
    "v_cvt_pk_bf16_f32 v166, v204, v205\n"
    "v_cvt_pk_bf16_f32 v167, v206, v207\n"
    "v_mfma_f32_16x16x32_bf16 v[252:255], a[108:111], a[92:95], v[252:255]\n"
    "v_cvt_pk_bf16_f32 v168, v208, v209\n"
    "v_cvt_pk_bf16_f32 v169, v210, v211\n"
    "v_mfma_f32_16x16x32_bf16 v[32:35], a[112:115], a[64:67], v[32:35]\n"
    "v_cvt_pk_bf16_f32 v170, v212, v213\n"
    "v_cvt_pk_bf16_f32 v171, v214, v215\n"
    "v_mfma_f32_16x16x32_bf16 v[36:39], a[116:119], a[64:67], v[36:39]\n"
    "buffer_load_dwordx4 v3, s[52:55], s81 offen offset:1024 lds\n"
    "v_mfma_f32_16x16x32_bf16 v[40:43], a[112:115], a[68:71], v[40:43]\n"
    "s_add_u32 s81, s81, s69\n"
    "v_cvt_pk_bf16_f32 v172, v216, v217\n"
    "v_cvt_pk_bf16_f32 v173, v218, v219\n"
    "v_mfma_f32_16x16x32_bf16 v[44:47], a[116:119], a[68:71], v[44:47]\n"
    "v_cvt_pk_bf16_f32 v174, v220, v221\n"
    "v_cvt_pk_bf16_f32 v175, v222, v223\n"
    "v_mfma_f32_16x16x32_bf16 v[48:51], a[112:115], a[72:75], v[48:51]\n"
    "v_cvt_pk_bf16_f32 v176, v224, v225\n"
    "v_cvt_pk_bf16_f32 v177, v226, v227\n"
    "v_mfma_f32_16x16x32_bf16 v[52:55], a[116:119], a[72:75], v[52:55]\n"
    "v_cvt_pk_bf16_f32 v178, v228, v229\n"
    "v_cvt_pk_bf16_f32 v179, v230, v231\n"
    "v_mfma_f32_16x16x32_bf16 v[56:59], a[112:115], a[76:79], v[56:59]\n"
    "buffer_load_dwordx4 v3, s[52:55], s81 offen offset:2048 lds\n"
    "v_mfma_f32_16x16x32_bf16 v[60:63], a[116:119], a[76:79], v[60:63]\n"
    "s_add_u32 s81, s81, s68\n"
    "v_cvt_pk_bf16_f32 v180, v232, v233\n"
    "v_cvt_pk_bf16_f32 v181, v234, v235\n"
    "v_mfma_f32_16x16x32_bf16 v[64:67], a[112:115], a[80:83], v[64:67]\n"
    "v_cvt_pk_bf16_f32 v182, v236, v237\n"
    "v_cvt_pk_bf16_f32 v183, v238, v239\n"
    "v_mfma_f32_16x16x32_bf16 v[68:71], a[116:119], a[80:83], v[68:71]\n"
    "v_cvt_pk_bf16_f32 v184, v240, v241\n"
    "v_cvt_pk_bf16_f32 v185, v242, v243\n"
    "v_mfma_f32_16x16x32_bf16 v[72:75], a[112:115], a[84:87], v[72:75]\n"
    "v_cvt_pk_bf16_f32 v186, v244, v245\n"
    "v_cvt_pk_bf16_f32 v187, v246, v247\n"
    "v_mfma_f32_16x16x32_bf16 v[76:79], a[116:119], a[84:87], v[76:79]\n"
    "buffer_load_dwordx4 v3, s[52:55], s81 offen offset:3072 lds\n"
    "v_mfma_f32_16x16x32_bf16 v[80:83], a[112:115], a[88:91], v[80:83]\n"
    "s_add_u32 s81, s81, s70\n"
    "v_cvt_pk_bf16_f32 v188, v248, v249\n"
    "v_cvt_pk_bf16_f32 v189, v250, v251\n"
    "v_mfma_f32_16x16x32_bf16 v[84:87], a[116:119], a[88:91], v[84:87]\n"
    "v_cvt_pk_bf16_f32 v190, v252, v253\n"
    "v_cvt_pk_bf16_f32 v191, v254, v255\n"
    "v_mfma_f32_16x16x32_bf16 v[88:91], a[112:115], a[92:95], v[88:91]\n"
    "v_cvt_pk_bf16_f32 v192, v32, v33\n"
    "v_cvt_pk_bf16_f32 v193, v34, v35\n"
    "v_mfma_f32_16x16x32_bf16 v[92:95], a[116:119], a[92:95], v[92:95]\n"
    "v_cvt_pk_bf16_f32 v194, v36, v37\n"
    "v_cvt_pk_bf16_f32 v195, v38, v39\n"
    "v_mfma_f32_16x16x32_bf16 v[96:99], a[120:123], a[64:67], v[96:99]\n"
    "ds_read_b64_tr_b16 a[224:225], v6 offset:16384\n"
    "v_cvt_pk_bf16_f32 v196, v40, v41\n"
    "v_mfma_f32_16x16x32_bf16 v[100:103], a[124:127], a[64:67], v[100:103]\n"
    "v_cvt_pk_bf16_f32 v197, v42, v43\n"
    "v_cvt_pk_bf16_f32 v198, v44, v45\n"
    "v_mfma_f32_16x16x32_bf16 v[104:107], a[120:123], a[68:71], v[104:107]\n"
    "ds_read_b64_tr_b16 a[226:227], v6 offset:18432\n"
    "v_cvt_pk_bf16_f32 v199, v46, v47\n"
    "v_mfma_f32_16x16x32_bf16 v[108:111], a[124:127], a[68:71], v[108:111]\n"
    "v_cvt_pk_bf16_f32 v200, v48, v49\n"
    "v_cvt_pk_bf16_f32 v201, v50, v51\n"
    "v_mfma_f32_16x16x32_bf16 v[112:115], a[120:123], a[72:75], v[112:115]\n"
    "ds_read_b64_tr_b16 a[228:229], v6 offset:20480\n"
    "v_cvt_pk_bf16_f32 v202, v52, v53\n"
    "v_mfma_f32_16x16x32_bf16 v[116:119], a[124:127], a[72:75], v[116:119]\n"
    "v_cvt_pk_bf16_f32 v203, v54, v55\n"
    "v_cvt_pk_bf16_f32 v204, v56, v57\n"
    "v_mfma_f32_16x16x32_bf16 v[120:123], a[120:123], a[76:79], v[120:123]\n"
    "ds_read_b64_tr_b16 a[230:231], v6 offset:22528\n"
    "v_cvt_pk_bf16_f32 v205, v58, v59\n"
    "v_mfma_f32_16x16x32_bf16 v[124:127], a[124:127], a[76:79], v[124:127]\n"
    "v_cvt_pk_bf16_f32 v206, v60, v61\n"
    "v_cvt_pk_bf16_f32 v207, v62, v63\n"
    "v_mfma_f32_16x16x32_bf16 a[128:131], a[120:123], a[80:83], a[128:131]\n"
    "ds_read_b64_tr_b16 a[232:233], v7 offset:16384\n"
    "v_cvt_pk_bf16_f32 v208, v64, v65\n"
    "v_mfma_f32_16x16x32_bf16 a[132:135], a[124:127], a[80:83], a[132:135]\n"
    "v_cvt_pk_bf16_f32 v209, v66, v67\n"
    "v_cvt_pk_bf16_f32 v210, v68, v69\n"
    "v_mfma_f32_16x16x32_bf16 a[136:139], a[120:123], a[84:87], a[136:139]\n"
    "ds_read_b64_tr_b16 a[234:235], v7 offset:18432\n"
    "v_cvt_pk_bf16_f32 v211, v70, v71\n"
    "v_mfma_f32_16x16x32_bf16 a[140:143], a[124:127], a[84:87], a[140:143]\n"
    "v_cvt_pk_bf16_f32 v212, v72, v73\n"
    "v_cvt_pk_bf16_f32 v213, v74, v75\n"
    "v_mfma_f32_16x16x32_bf16 a[144:147], a[120:123], a[88:91], a[144:147]\n"
    "ds_read_b64_tr_b16 a[236:237], v7 offset:20480\n"
    "v_cvt_pk_bf16_f32 v214, v76, v77\n"
    "v_mfma_f32_16x16x32_bf16 a[148:151], a[124:127], a[88:91], a[148:151]\n"
    "v_cvt_pk_bf16_f32 v215, v78, v79\n"
    "v_mfma_f32_16x16x32_bf16 a[152:155], a[120:123], a[92:95], a[152:155]\n"
    "s_waitcnt vmcnt(12)\n"
    "ds_read_b64_tr_b16 a[238:239], v7 offset:22528\n"
    "v_mfma_f32_16x16x32_bf16 a[156:159], a[124:127], a[92:95], a[156:159]\n"
    "s_waitcnt lgkmcnt(4)\n"
    "KV_DONE_%=:\n"
    "v_mfma_f32_16x16x32_bf16 a[0:3], a[224:227], a[160:163], 0\n"
    "ds_read_b64_tr_b16 a[240:241], v8 offset:16384\n"
    "v_mfma_f32_16x16x32_bf16 a[0:3], a[228:231], a[176:179], a[0:3]\n"
    "v_mfma_f32_16x16x32_bf16 a[4:7], a[224:227], a[164:167], 0\n"
    "ds_read_b64_tr_b16 a[242:243], v8 offset:18432\n"
    "v_cvt_pk_bf16_f32 v216, v80, v81\n"
    "v_mfma_f32_16x16x32_bf16 a[4:7], a[228:231], a[180:183], a[4:7]\n"
    "v_cvt_pk_bf16_f32 v217, v82, v83\n"
    "v_cvt_pk_bf16_f32 v218, v84, v85\n"
    "v_mfma_f32_16x16x32_bf16 a[8:11], a[224:227], a[168:171], 0\n"
    "ds_read_b64_tr_b16 a[244:245], v8 offset:20480\n"
    "v_cvt_pk_bf16_f32 v219, v86, v87\n"
    "v_mfma_f32_16x16x32_bf16 a[8:11], a[228:231], a[184:187], a[8:11]\n"
    "v_cvt_pk_bf16_f32 v220, v88, v89\n"
    "v_cvt_pk_bf16_f32 v221, v90, v91\n"
    "v_mfma_f32_16x16x32_bf16 a[12:15], a[224:227], a[172:175], 0\n"
    "ds_read_b64_tr_b16 a[246:247], v8 offset:22528\n"
    "v_cvt_pk_bf16_f32 v222, v92, v93\n"
    "v_mfma_f32_16x16x32_bf16 a[12:15], a[228:231], a[188:191], a[12:15]\n"
    "s_waitcnt lgkmcnt(4)\n"
    "v_cvt_pk_bf16_f32 v223, v94, v95\n"
    "v_cvt_pk_bf16_f32 v224, v96, v97\n"
    "v_mfma_f32_16x16x32_bf16 a[16:19], a[232:235], a[160:163], 0\n"
    "ds_read_b64_tr_b16 a[248:249], v9 offset:16384\n"
    "v_cvt_pk_bf16_f32 v225, v98, v99\n"
    "v_mfma_f32_16x16x32_bf16 a[16:19], a[236:239], a[176:179], a[16:19]\n"
    "v_cvt_pk_bf16_f32 v226, v100, v101\n"
    "v_cvt_pk_bf16_f32 v227, v102, v103\n"
    "v_mfma_f32_16x16x32_bf16 a[20:23], a[232:235], a[164:167], 0\n"
    "ds_read_b64_tr_b16 a[250:251], v9 offset:18432\n"
    "v_cvt_pk_bf16_f32 v228, v104, v105\n"
    "v_mfma_f32_16x16x32_bf16 a[20:23], a[236:239], a[180:183], a[20:23]\n"
    "v_cvt_pk_bf16_f32 v229, v106, v107\n"
    "v_cvt_pk_bf16_f32 v230, v108, v109\n"
    "v_mfma_f32_16x16x32_bf16 a[24:27], a[232:235], a[168:171], 0\n"
    "ds_read_b64_tr_b16 a[252:253], v9 offset:20480\n"
    "v_cvt_pk_bf16_f32 v231, v110, v111\n"
    "v_mfma_f32_16x16x32_bf16 a[24:27], a[236:239], a[184:187], a[24:27]\n"
    "v_cvt_pk_bf16_f32 v232, v112, v113\n"
    "v_cvt_pk_bf16_f32 v233, v114, v115\n"
    "v_mfma_f32_16x16x32_bf16 a[28:31], a[232:235], a[172:175], 0\n"
    "ds_read_b64_tr_b16 a[254:255], v9 offset:22528\n"
    "v_cvt_pk_bf16_f32 v234, v116, v117\n"
    "v_mfma_f32_16x16x32_bf16 a[28:31], a[236:239], a[188:191], a[28:31]\n"
    "s_waitcnt lgkmcnt(4)\n"
    "v_cvt_pk_bf16_f32 v235, v118, v119\n"
    "v_cvt_pk_bf16_f32 v236, v120, v121\n"
    "v_mfma_f32_16x16x32_bf16 a[32:35], a[240:243], a[160:163], 0\n"
    "ds_read_b64_tr_b16 a[224:225], v6 offset:24576\n"
    "v_cvt_pk_bf16_f32 v237, v122, v123\n"
    "v_mfma_f32_16x16x32_bf16 a[32:35], a[244:247], a[176:179], a[32:35]\n"
    "v_cvt_pk_bf16_f32 v238, v124, v125\n"
    "v_cvt_pk_bf16_f32 v239, v126, v127\n"
    "v_mfma_f32_16x16x32_bf16 a[36:39], a[240:243], a[164:167], 0\n"
    "ds_read_b64_tr_b16 a[226:227], v6 offset:26624\n"
    "v_accvgpr_read_b32 v16, a128\n"
    "v_mfma_f32_16x16x32_bf16 a[36:39], a[244:247], a[180:183], a[36:39]\n"
    "v_accvgpr_read_b32 v17, a129\n"
    "v_accvgpr_read_b32 v18, a130\n"
    "v_mfma_f32_16x16x32_bf16 a[40:43], a[240:243], a[168:171], 0\n"
    "ds_read_b64_tr_b16 a[228:229], v6 offset:28672\n"
    "v_accvgpr_read_b32 v19, a131\n"
    "v_mfma_f32_16x16x32_bf16 a[40:43], a[244:247], a[184:187], a[40:43]\n"
    "v_accvgpr_read_b32 v20, a132\n"
    "v_accvgpr_read_b32 v21, a133\n"
    "v_mfma_f32_16x16x32_bf16 a[44:47], a[240:243], a[172:175], 0\n"
    "ds_read_b64_tr_b16 a[230:231], v6 offset:30720\n"
    "v_accvgpr_read_b32 v22, a134\n"
    "v_mfma_f32_16x16x32_bf16 a[44:47], a[244:247], a[188:191], a[44:47]\n"
    "s_waitcnt lgkmcnt(4)\n"
    "v_accvgpr_read_b32 v23, a135\n"
    "v_cvt_pk_bf16_f32 v240, v16, v17\n"
    "v_mfma_f32_16x16x32_bf16 a[48:51], a[248:251], a[160:163], 0\n"
    "ds_read_b64_tr_b16 a[232:233], v7 offset:24576\n"
    "v_cvt_pk_bf16_f32 v241, v18, v19\n"
    "v_mfma_f32_16x16x32_bf16 a[48:51], a[252:255], a[176:179], a[48:51]\n"
    "v_cvt_pk_bf16_f32 v242, v20, v21\n"
    "v_cvt_pk_bf16_f32 v243, v22, v23\n"
    "v_mfma_f32_16x16x32_bf16 a[52:55], a[248:251], a[164:167], 0\n"
    "ds_read_b64_tr_b16 a[234:235], v7 offset:26624\n"
    "v_accvgpr_read_b32 v24, a136\n"
    "v_mfma_f32_16x16x32_bf16 a[52:55], a[252:255], a[180:183], a[52:55]\n"
    "v_accvgpr_read_b32 v25, a137\n"
    "v_accvgpr_read_b32 v26, a138\n"
    "v_mfma_f32_16x16x32_bf16 a[56:59], a[248:251], a[168:171], 0\n"
    "ds_read_b64_tr_b16 a[236:237], v7 offset:28672\n"
    "v_accvgpr_read_b32 v27, a139\n"
    "v_mfma_f32_16x16x32_bf16 a[56:59], a[252:255], a[184:187], a[56:59]\n"
    "v_accvgpr_read_b32 v28, a140\n"
    "v_accvgpr_read_b32 v29, a141\n"
    "v_mfma_f32_16x16x32_bf16 a[60:63], a[248:251], a[172:175], 0\n"
    "ds_read_b64_tr_b16 a[238:239], v7 offset:30720\n"
    "v_accvgpr_read_b32 v30, a142\n"
    "v_mfma_f32_16x16x32_bf16 a[60:63], a[252:255], a[188:191], a[60:63]\n"
    "s_waitcnt lgkmcnt(4)\n"
    "v_accvgpr_read_b32 v31, a143\n"
    "v_cvt_pk_bf16_f32 v244, v24, v25\n"
    "v_mfma_f32_16x16x32_bf16 a[64:67], a[224:227], a[160:163], 0\n"
    "ds_read_b64_tr_b16 a[240:241], v8 offset:24576\n"
    "v_cvt_pk_bf16_f32 v245, v26, v27\n"
    "v_mfma_f32_16x16x32_bf16 a[64:67], a[228:231], a[176:179], a[64:67]\n"
    "v_cvt_pk_bf16_f32 v246, v28, v29\n"
    "v_cvt_pk_bf16_f32 v247, v30, v31\n"
    "v_mfma_f32_16x16x32_bf16 a[68:71], a[224:227], a[164:167], 0\n"
    "ds_read_b64_tr_b16 a[242:243], v8 offset:26624\n"
    "v_accvgpr_read_b32 v16, a144\n"
    "v_mfma_f32_16x16x32_bf16 a[68:71], a[228:231], a[180:183], a[68:71]\n"
    "v_accvgpr_read_b32 v17, a145\n"
    "v_accvgpr_read_b32 v18, a146\n"
    "v_mfma_f32_16x16x32_bf16 a[72:75], a[224:227], a[168:171], 0\n"
    "ds_read_b64_tr_b16 a[244:245], v8 offset:28672\n"
    "v_accvgpr_read_b32 v19, a147\n"
    "v_mfma_f32_16x16x32_bf16 a[72:75], a[228:231], a[184:187], a[72:75]\n"
    "v_accvgpr_read_b32 v20, a148\n"
    "v_accvgpr_read_b32 v21, a149\n"
    "v_mfma_f32_16x16x32_bf16 a[76:79], a[224:227], a[172:175], 0\n"
    "ds_read_b64_tr_b16 a[246:247], v8 offset:30720\n"
    "v_accvgpr_read_b32 v22, a150\n"
    "v_mfma_f32_16x16x32_bf16 a[76:79], a[228:231], a[188:191], a[76:79]\n"
    "s_waitcnt lgkmcnt(4)\n"
    "v_accvgpr_read_b32 v23, a151\n"
    "v_cvt_pk_bf16_f32 v248, v16, v17\n"
    "v_mfma_f32_16x16x32_bf16 a[80:83], a[232:235], a[160:163], 0\n"
    "ds_read_b64_tr_b16 a[248:249], v9 offset:24576\n"
    "v_cvt_pk_bf16_f32 v249, v18, v19\n"
    "v_mfma_f32_16x16x32_bf16 a[80:83], a[236:239], a[176:179], a[80:83]\n"
    "v_cvt_pk_bf16_f32 v250, v20, v21\n"
    "v_cvt_pk_bf16_f32 v251, v22, v23\n"
    "v_mfma_f32_16x16x32_bf16 a[84:87], a[232:235], a[164:167], 0\n"
    "ds_read_b64_tr_b16 a[250:251], v9 offset:26624\n"
    "v_accvgpr_read_b32 v24, a152\n"
    "v_mfma_f32_16x16x32_bf16 a[84:87], a[236:239], a[180:183], a[84:87]\n"
    "v_accvgpr_read_b32 v25, a153\n"
    "v_accvgpr_read_b32 v26, a154\n"
    "v_mfma_f32_16x16x32_bf16 a[88:91], a[232:235], a[168:171], 0\n"
    "ds_read_b64_tr_b16 a[252:253], v9 offset:28672\n"
    "v_accvgpr_read_b32 v27, a155\n"
    "v_mfma_f32_16x16x32_bf16 a[88:91], a[236:239], a[184:187], a[88:91]\n"
    "v_accvgpr_read_b32 v28, a156\n"
    "v_accvgpr_read_b32 v29, a157\n"
    "v_mfma_f32_16x16x32_bf16 a[92:95], a[232:235], a[172:175], 0\n"
    "ds_read_b64_tr_b16 a[254:255], v9 offset:30720\n"
    "v_accvgpr_read_b32 v30, a158\n"
    "v_mfma_f32_16x16x32_bf16 a[92:95], a[236:239], a[188:191], a[92:95]\n"
    "s_waitcnt lgkmcnt(4)\n"
    "v_accvgpr_read_b32 v31, a159\n"
    "v_cvt_pk_bf16_f32 v252, v24, v25\n"
    "v_mfma_f32_16x16x32_bf16 a[96:99], a[240:243], a[160:163], 0\n"
    "ds_read_b64_tr_b16 a[224:225], v8 offset:0\n"
    "v_cvt_pk_bf16_f32 v253, v26, v27\n"
    "v_mfma_f32_16x16x32_bf16 a[96:99], a[244:247], a[176:179], a[96:99]\n"
    "v_cvt_pk_bf16_f32 v254, v28, v29\n"
    "v_cvt_pk_bf16_f32 v255, v30, v31\n"
    "v_mfma_f32_16x16x32_bf16 a[100:103], a[240:243], a[164:167], 0\n"
    "ds_read_b64_tr_b16 a[226:227], v8 offset:2048\n"
    "v_mfma_f32_16x16x32_bf16 a[100:103], a[244:247], a[180:183], a[100:103]\n"
    "buffer_load_dwordx4 a[208:211], v0, s[40:43], s71 offen offset:3072 sc1\n"
    "v_mfma_f32_16x16x32_bf16 a[104:107], a[240:243], a[168:171], 0\n"
    "s_addk_i32 s71, 0x1000\n"
    "ds_read_b64_tr_b16 a[228:229], v8 offset:4096\n"
    "v_mfma_f32_16x16x32_bf16 a[104:107], a[244:247], a[184:187], a[104:107]\n"
    "buffer_load_dwordx4 a[212:215], v0, s[40:43], s72 offen offset:3072 sc1\n"
    "v_mfma_f32_16x16x32_bf16 a[108:111], a[240:243], a[172:175], 0\n"
    "s_addk_i32 s72, 0x1000\n"
    "ds_read_b64_tr_b16 a[230:231], v8 offset:6144\n"
    "v_mfma_f32_16x16x32_bf16 a[108:111], a[244:247], a[188:191], a[108:111]\n"
    "s_waitcnt lgkmcnt(4)\n"
    "v_mfma_f32_16x16x32_bf16 a[112:115], a[248:251], a[160:163], 0\n"
    "ds_read_b64_tr_b16 a[232:233], v9 offset:0\n"
    "v_mfma_f32_16x16x32_bf16 a[112:115], a[252:255], a[176:179], a[112:115]\n"
    "buffer_load_dwordx4 a[216:219], v0, s[40:43], s73 offen offset:3072 sc1\n"
    "v_mfma_f32_16x16x32_bf16 a[116:119], a[248:251], a[164:167], 0\n"
    "s_addk_i32 s73, 0x1000\n"
    "ds_read_b64_tr_b16 a[234:235], v9 offset:2048\n"
    "v_mfma_f32_16x16x32_bf16 a[116:119], a[252:255], a[180:183], a[116:119]\n"
    "buffer_load_dwordx4 a[220:223], v0, s[40:43], s74 offen offset:3072 sc1\n"
    "v_mfma_f32_16x16x32_bf16 a[120:123], a[248:251], a[168:171], 0\n"
    "s_addk_i32 s74, 0x1000\n"
    "ds_read_b64_tr_b16 a[236:237], v9 offset:4096\n"
    "v_mfma_f32_16x16x32_bf16 a[120:123], a[252:255], a[184:187], a[120:123]\n"
    "buffer_store_dwordx4 v[128:131], v0, s[40:43], s67 offen offset:0 sc1\n"
    "v_mfma_f32_16x16x32_bf16 a[124:127], a[248:251], a[172:175], 0\n"
    "ds_read_b64_tr_b16 a[238:239], v9 offset:6144\n"
    "v_mfma_f32_16x16x32_bf16 a[124:127], a[252:255], a[188:191], a[124:127]\n"
    "s_waitcnt lgkmcnt(4)\n"
    "v_mfma_f32_16x16x32_bf16 v[32:35], a[224:227], a[160:163], 0\n"
    "ds_read_b64_tr_b16 a[240:241], v6 offset:8192\n"
    "v_mfma_f32_16x16x32_bf16 v[32:35], a[228:231], a[176:179], v[32:35]\n"
    "buffer_store_dwordx4 v[132:135], v0, s[40:43], s67 offen offset:1024 sc1\n"
    "v_mfma_f32_16x16x32_bf16 v[36:39], a[224:227], a[164:167], 0\n"
    "ds_read_b64_tr_b16 a[242:243], v6 offset:10240\n"
    "v_mfma_f32_16x16x32_bf16 v[36:39], a[228:231], a[180:183], v[36:39]\n"
    "v_mfma_f32_16x16x32_bf16 v[40:43], a[224:227], a[168:171], 0\n"
    "ds_read_b64_tr_b16 a[244:245], v6 offset:12288\n"
    "v_mfma_f32_16x16x32_bf16 v[40:43], a[228:231], a[184:187], v[40:43]\n"
    "buffer_store_dwordx4 v[136:139], v0, s[40:43], s67 offen offset:2048 sc1\n"
    "v_mfma_f32_16x16x32_bf16 v[44:47], a[224:227], a[172:175], 0\n"
    "ds_read_b64_tr_b16 a[246:247], v6 offset:14336\n"
    "v_mfma_f32_16x16x32_bf16 v[44:47], a[228:231], a[188:191], v[44:47]\n"
    "s_waitcnt lgkmcnt(4)\n"
    "v_mfma_f32_16x16x32_bf16 v[48:51], a[232:235], a[160:163], 0\n"
    "ds_read_b64_tr_b16 a[248:249], v7 offset:8192\n"
    "v_mfma_f32_16x16x32_bf16 v[48:51], a[236:239], a[176:179], v[48:51]\n"
    "v_mfma_f32_16x16x32_bf16 v[52:55], a[232:235], a[164:167], 0\n"
    "ds_read_b64_tr_b16 a[250:251], v7 offset:10240\n"
    "v_mfma_f32_16x16x32_bf16 v[52:55], a[236:239], a[180:183], v[52:55]\n"
    "v_mfma_f32_16x16x32_bf16 v[56:59], a[232:235], a[168:171], 0\n"
    "ds_read_b64_tr_b16 a[252:253], v7 offset:12288\n"
    "v_mfma_f32_16x16x32_bf16 v[56:59], a[236:239], a[184:187], v[56:59]\n"
    "v_mfma_f32_16x16x32_bf16 v[60:63], a[232:235], a[172:175], 0\n"
    "ds_read_b64_tr_b16 a[254:255], v7 offset:14336\n"
    "v_mfma_f32_16x16x32_bf16 v[60:63], a[236:239], a[188:191], v[60:63]\n"
    "s_waitcnt lgkmcnt(4)\n"
    "v_mfma_f32_16x16x32_bf16 v[64:67], a[240:243], a[160:163], 0\n"
    "ds_read_b64_tr_b16 a[224:225], v8 offset:8192\n"
    "v_mfma_f32_16x16x32_bf16 v[64:67], a[244:247], a[176:179], v[64:67]\n"
    "v_mfma_f32_16x16x32_bf16 v[68:71], a[240:243], a[164:167], 0\n"
    "ds_read_b64_tr_b16 a[226:227], v8 offset:10240\n"
    "v_mfma_f32_16x16x32_bf16 v[68:71], a[244:247], a[180:183], v[68:71]\n"
    "v_mfma_f32_16x16x32_bf16 v[72:75], a[240:243], a[168:171], 0\n"
    "ds_read_b64_tr_b16 a[228:229], v8 offset:12288\n"
    "v_mfma_f32_16x16x32_bf16 v[72:75], a[244:247], a[184:187], v[72:75]\n"
    "v_mfma_f32_16x16x32_bf16 v[76:79], a[240:243], a[172:175], 0\n"
    "ds_read_b64_tr_b16 a[230:231], v8 offset:14336\n"
    "v_mfma_f32_16x16x32_bf16 v[76:79], a[244:247], a[188:191], v[76:79]\n"
    "s_waitcnt lgkmcnt(4)\n"
    "v_add_u32 v8, 0x8000, v8\n"
    "v_mfma_f32_16x16x32_bf16 v[80:83], a[248:251], a[160:163], 0\n"
    "ds_read_b64_tr_b16 a[232:233], v9 offset:8192\n"
    "v_mfma_f32_16x16x32_bf16 v[80:83], a[252:255], a[176:179], v[80:83]\n"
    "v_mfma_f32_16x16x32_bf16 v[84:87], a[248:251], a[164:167], 0\n"
    "ds_read_b64_tr_b16 a[234:235], v9 offset:10240\n"
    "v_mfma_f32_16x16x32_bf16 v[84:87], a[252:255], a[180:183], v[84:87]\n"
    "v_mfma_f32_16x16x32_bf16 v[88:91], a[248:251], a[168:171], 0\n"
    "ds_read_b64_tr_b16 a[236:237], v9 offset:12288\n"
    "v_mfma_f32_16x16x32_bf16 v[88:91], a[252:255], a[184:187], v[88:91]\n"
    "v_mfma_f32_16x16x32_bf16 v[92:95], a[248:251], a[172:175], 0\n"
    "ds_read_b64_tr_b16 a[238:239], v9 offset:14336\n"
    "v_mfma_f32_16x16x32_bf16 v[92:95], a[252:255], a[188:191], v[92:95]\n"
    "s_waitcnt lgkmcnt(4)\n"
    "v_add_u32 v9, 0x8000, v9\n"
    "v_mfma_f32_16x16x32_bf16 v[96:99], a[224:227], a[160:163], 0\n"
    "ds_read_b64_tr_b16 a[240:241], v6 offset:0\n"
    "v_mfma_f32_16x16x32_bf16 v[96:99], a[228:231], a[176:179], v[96:99]\n"
    "buffer_store_dwordx4 v[140:143], v0, s[40:43], s67 offen offset:3072 sc1\n"
    "v_mfma_f32_16x16x32_bf16 v[100:103], a[224:227], a[164:167], 0\n"
    "ds_read_b64_tr_b16 a[242:243], v6 offset:2048\n"
    "v_mfma_f32_16x16x32_bf16 v[100:103], a[228:231], a[180:183], v[100:103]\n"
    "v_accvgpr_read_b32 v128, a0\n"
    "v_accvgpr_read_b32 v129, a1\n"
    "v_mfma_f32_16x16x32_bf16 v[104:107], a[224:227], a[168:171], 0\n"
    "ds_read_b64_tr_b16 a[244:245], v6 offset:4096\n"
    "v_accvgpr_read_b32 v130, a2\n"
    "v_mfma_f32_16x16x32_bf16 v[104:107], a[228:231], a[184:187], v[104:107]\n"
    "v_accvgpr_read_b32 v131, a3\n"
    "v_accvgpr_read_b32 v132, a4\n"
    "v_mfma_f32_16x16x32_bf16 v[108:111], a[224:227], a[172:175], 0\n"
    "ds_read_b64_tr_b16 a[246:247], v6 offset:6144\n"
    "v_accvgpr_read_b32 v133, a5\n"
    "v_mfma_f32_16x16x32_bf16 v[108:111], a[228:231], a[188:191], v[108:111]\n"
    "s_waitcnt lgkmcnt(4)\n"
    "v_add_u32 v6, 0x8000, v6\n"
    "v_accvgpr_read_b32 v134, a6\n"
    "v_mfma_f32_16x16x32_bf16 v[112:115], a[232:235], a[160:163], 0\n"
    "ds_read_b64_tr_b16 a[248:249], v7 offset:0\n"
    "v_accvgpr_read_b32 v135, a7\n"
    "v_mfma_f32_16x16x32_bf16 v[112:115], a[236:239], a[176:179], v[112:115]\n"
    "v_accvgpr_read_b32 v136, a8\n"
    "v_accvgpr_read_b32 v137, a9\n"
    "v_mfma_f32_16x16x32_bf16 v[116:119], a[232:235], a[164:167], 0\n"
    "ds_read_b64_tr_b16 a[250:251], v7 offset:2048\n"
    "v_accvgpr_read_b32 v138, a10\n"
    "v_mfma_f32_16x16x32_bf16 v[116:119], a[236:239], a[180:183], v[116:119]\n"
    "v_accvgpr_read_b32 v139, a11\n"
    "v_accvgpr_read_b32 v140, a12\n"
    "v_mfma_f32_16x16x32_bf16 v[120:123], a[232:235], a[168:171], 0\n"
    "s_waitcnt vmcnt(36)\n"
    "ds_read_b64_tr_b16 a[252:253], v7 offset:4096\n"
    "v_accvgpr_read_b32 v141, a13\n"
    "v_mfma_f32_16x16x32_bf16 v[120:123], a[236:239], a[184:187], v[120:123]\n"
    "s_barrier\n"
    "v_accvgpr_read_b32 v142, a14\n"
    "v_accvgpr_read_b32 v143, a15\n"
    "v_mfma_f32_16x16x32_bf16 v[124:127], a[232:235], a[172:175], 0\n"
    "ds_read_b64_tr_b16 a[254:255], v7 offset:6144\n"
    "v_accvgpr_write_b32 a0, 0\n"
    "v_mfma_f32_16x16x32_bf16 v[124:127], a[236:239], a[188:191], v[124:127]\n"
    "s_waitcnt lgkmcnt(4)\n"
    "v_add_u32 v7, 0x8000, v7\n"
    "v_accvgpr_write_b32 a1, 0\n"
    "v_mfma_f32_16x16x32_bf16 a[128:131], a[240:243], a[160:163], 0\n"
    "ds_read_b64_tr_b16 a[224:225], v6 offset:0\n"
    "v_accvgpr_write_b32 a2, 0\n"
    "v_mfma_f32_16x16x32_bf16 a[128:131], a[244:247], a[176:179], a[128:131]\n"
    "v_accvgpr_write_b32 a3, 0\n"
    "v_accvgpr_write_b32 a4, 0\n"
    "v_mfma_f32_16x16x32_bf16 a[132:135], a[240:243], a[164:167], 0\n"
    "ds_read_b64_tr_b16 a[226:227], v6 offset:2048\n"
    "v_accvgpr_write_b32 a5, 0\n"
    "v_mfma_f32_16x16x32_bf16 a[132:135], a[244:247], a[180:183], a[132:135]\n"
    "v_accvgpr_write_b32 a6, 0\n"
    "v_accvgpr_write_b32 a7, 0\n"
    "v_mfma_f32_16x16x32_bf16 a[136:139], a[240:243], a[168:171], 0\n"
    "ds_read_b64_tr_b16 a[228:229], v6 offset:4096\n"
    "v_accvgpr_write_b32 a8, 0\n"
    "v_mfma_f32_16x16x32_bf16 a[136:139], a[244:247], a[184:187], a[136:139]\n"
    "v_accvgpr_write_b32 a9, 0\n"
    "v_accvgpr_write_b32 a10, 0\n"
    "v_mfma_f32_16x16x32_bf16 a[140:143], a[240:243], a[172:175], 0\n"
    "ds_read_b64_tr_b16 a[230:231], v6 offset:6144\n"
    "v_accvgpr_write_b32 a11, 0\n"
    "v_mfma_f32_16x16x32_bf16 a[140:143], a[244:247], a[188:191], a[140:143]\n"
    "s_waitcnt lgkmcnt(4)\n"
    "v_accvgpr_write_b32 a12, 0\n"
    "v_accvgpr_write_b32 a13, 0\n"
    "v_mfma_f32_16x16x32_bf16 a[144:147], a[248:251], a[160:163], 0\n"
    "ds_read_b64_tr_b16 a[232:233], v7 offset:0\n"
    "v_accvgpr_write_b32 a14, 0\n"
    "v_mfma_f32_16x16x32_bf16 a[144:147], a[252:255], a[176:179], a[144:147]\n"
    "v_accvgpr_write_b32 a15, 0\n"
    "v_mfma_f32_16x16x32_bf16 a[148:151], a[248:251], a[164:167], 0\n"
    "ds_read_b64_tr_b16 a[234:235], v7 offset:2048\n"
    "v_mfma_f32_16x16x32_bf16 a[148:151], a[252:255], a[180:183], a[148:151]\n"
    "v_mfma_f32_16x16x32_bf16 a[152:155], a[248:251], a[168:171], 0\n"
    "ds_read_b64_tr_b16 a[236:237], v7 offset:4096\n"
    "v_mfma_f32_16x16x32_bf16 a[152:155], a[252:255], a[184:187], a[152:155]\n"
    "v_mfma_f32_16x16x32_bf16 a[156:159], a[248:251], a[172:175], 0\n"
    "s_waitcnt vmcnt(4)\n"
    "ds_read_b64_tr_b16 a[238:239], v7 offset:6144\n"
    "v_mfma_f32_16x16x32_bf16 a[156:159], a[252:255], a[188:191], a[156:159]\n"
    "s_waitcnt lgkmcnt(4)\n"
    "v_mfma_f32_16x16x32_bf16 a[128:131], a[224:227], a[192:195], a[128:131]\n"
    "ds_read_b64_tr_b16 a[240:241], v8 offset:0\n"
    "v_and_b32 v10, 31, v12\n"
    "v_mfma_f32_16x16x32_bf16 a[128:131], a[228:231], a[208:211], a[128:131]\n"
    "v_lshlrev_b32 v10, 2, v10\n"
    "v_mfma_f32_16x16x32_bf16 a[132:135], a[224:227], a[196:199], a[132:135]\n"
    "ds_read_b64_tr_b16 a[242:243], v8 offset:2048\n"
    "v_mfma_f32_16x16x32_bf16 a[132:135], a[228:231], a[212:215], a[132:135]\n"
    "v_mfma_f32_16x16x32_bf16 a[136:139], a[224:227], a[200:203], a[136:139]\n"
    "ds_read_b64_tr_b16 a[244:245], v8 offset:4096\n"
    "v_mfma_f32_16x16x32_bf16 a[136:139], a[228:231], a[216:219], a[136:139]\n"
    "buffer_load_dwordx4 a[160:163], v0, s[40:43], s71 offen offset:0 sc1\n"
    "v_mfma_f32_16x16x32_bf16 a[140:143], a[224:227], a[204:207], a[140:143]\n"
    "ds_read_b64_tr_b16 a[246:247], v8 offset:6144\n"
    "v_mfma_f32_16x16x32_bf16 a[140:143], a[228:231], a[220:223], a[140:143]\n"
    "s_waitcnt lgkmcnt(4)\n"
    "v_mfma_f32_16x16x32_bf16 a[144:147], a[232:235], a[192:195], a[144:147]\n"
    "ds_read_b64_tr_b16 a[248:249], v9 offset:0\n"
    "v_mfma_f32_16x16x32_bf16 a[144:147], a[236:239], a[208:211], a[144:147]\n"
    "buffer_load_dwordx4 a[164:167], v0, s[40:43], s72 offen offset:0 sc1\n"
    "v_mfma_f32_16x16x32_bf16 a[148:151], a[232:235], a[196:199], a[148:151]\n"
    "ds_read_b64_tr_b16 a[250:251], v9 offset:2048\n"
    "v_mfma_f32_16x16x32_bf16 a[148:151], a[236:239], a[212:215], a[148:151]\n"
    "v_mfma_f32_16x16x32_bf16 a[152:155], a[232:235], a[200:203], a[152:155]\n"
    "ds_read_b64_tr_b16 a[252:253], v9 offset:4096\n"
    "v_mfma_f32_16x16x32_bf16 a[152:155], a[236:239], a[216:219], a[152:155]\n"
    "v_mfma_f32_16x16x32_bf16 a[156:159], a[232:235], a[204:207], a[156:159]\n"
    "ds_read_b64_tr_b16 a[254:255], v9 offset:6144\n"
    "v_mfma_f32_16x16x32_bf16 a[156:159], a[236:239], a[220:223], a[156:159]\n"
    "s_waitcnt lgkmcnt(4)\n"
    "v_mfma_f32_16x16x32_bf16 v[32:35], a[240:243], a[192:195], v[32:35]\n"
    "ds_read_b64_tr_b16 a[224:225], v6 offset:8192\n"
    "v_mfma_f32_16x16x32_bf16 v[32:35], a[244:247], a[208:211], v[32:35]\n"
    "buffer_load_dwordx4 a[168:171], v0, s[40:43], s73 offen offset:0 sc1\n"
    "v_mfma_f32_16x16x32_bf16 v[36:39], a[240:243], a[196:199], v[36:39]\n"
    "ds_read_b64_tr_b16 a[226:227], v6 offset:10240\n"
    "v_mfma_f32_16x16x32_bf16 v[36:39], a[244:247], a[212:215], v[36:39]\n"
    "v_mfma_f32_16x16x32_bf16 v[40:43], a[240:243], a[200:203], v[40:43]\n"
    "ds_read_b64_tr_b16 a[228:229], v6 offset:12288\n"
    "v_mfma_f32_16x16x32_bf16 v[40:43], a[244:247], a[216:219], v[40:43]\n"
    "buffer_load_dwordx4 a[172:175], v0, s[40:43], s74 offen offset:0 sc1\n"
    "v_mfma_f32_16x16x32_bf16 v[44:47], a[240:243], a[204:207], v[44:47]\n"
    "ds_read_b64_tr_b16 a[230:231], v6 offset:14336\n"
    "v_mfma_f32_16x16x32_bf16 v[44:47], a[244:247], a[220:223], v[44:47]\n"
    "s_waitcnt lgkmcnt(4)\n"
    "v_mfma_f32_16x16x32_bf16 v[48:51], a[248:251], a[192:195], v[48:51]\n"
    "ds_read_b64_tr_b16 a[232:233], v7 offset:8192\n"
    "v_mfma_f32_16x16x32_bf16 v[48:51], a[252:255], a[208:211], v[48:51]\n"
    "v_mfma_f32_16x16x32_bf16 v[52:55], a[248:251], a[196:199], v[52:55]\n"
    "ds_read_b64_tr_b16 a[234:235], v7 offset:10240\n"
    "v_mfma_f32_16x16x32_bf16 v[52:55], a[252:255], a[212:215], v[52:55]\n"
    "buffer_load_dwordx4 a[176:179], v0, s[40:43], s71 offen offset:1024 sc1\n"
    "v_mfma_f32_16x16x32_bf16 v[56:59], a[248:251], a[200:203], v[56:59]\n"
    "ds_read_b64_tr_b16 a[236:237], v7 offset:12288\n"
    "v_mfma_f32_16x16x32_bf16 v[56:59], a[252:255], a[216:219], v[56:59]\n"
    "v_mfma_f32_16x16x32_bf16 v[60:63], a[248:251], a[204:207], v[60:63]\n"
    "s_mov_b64 s[44:45], %[x]\n"
    "ds_read_b64_tr_b16 a[238:239], v7 offset:14336\n"
    "v_mfma_f32_16x16x32_bf16 v[60:63], a[252:255], a[220:223], v[60:63]\n"
    "s_waitcnt lgkmcnt(4)\n"
    "v_mfma_f32_16x16x32_bf16 v[64:67], a[224:227], a[192:195], v[64:67]\n"
    "s_mov_b32 s46, 0x280000\n"
    "ds_read_b64_tr_b16 a[240:241], v8 offset:8192\n"
    "v_mfma_f32_16x16x32_bf16 v[64:67], a[228:231], a[208:211], v[64:67]\n"
    "buffer_load_dwordx4 a[180:183], v0, s[40:43], s72 offen offset:1024 sc1\n"
    "v_mfma_f32_16x16x32_bf16 v[68:71], a[224:227], a[196:199], v[68:71]\n"
    "ds_read_b64_tr_b16 a[242:243], v8 offset:10240\n"
    "v_mfma_f32_16x16x32_bf16 v[68:71], a[228:231], a[212:215], v[68:71]\n"
    "v_mfma_f32_16x16x32_bf16 v[72:75], a[224:227], a[200:203], v[72:75]\n"
    "ds_read_b64_tr_b16 a[244:245], v8 offset:12288\n"
    "v_mfma_f32_16x16x32_bf16 v[72:75], a[228:231], a[216:219], v[72:75]\n"
    "buffer_load_dwordx4 a[184:187], v0, s[40:43], s73 offen offset:1024 sc1\n"
    "v_mfma_f32_16x16x32_bf16 v[76:79], a[224:227], a[204:207], v[76:79]\n"
    "ds_read_b64_tr_b16 a[246:247], v8 offset:14336\n"
    "v_mfma_f32_16x16x32_bf16 v[76:79], a[228:231], a[220:223], v[76:79]\n"
    "s_waitcnt lgkmcnt(4)\n"
    "v_mfma_f32_16x16x32_bf16 v[80:83], a[232:235], a[192:195], v[80:83]\n"
    "ds_read_b64_tr_b16 a[248:249], v9 offset:8192\n"
    "v_mfma_f32_16x16x32_bf16 v[80:83], a[236:239], a[208:211], v[80:83]\n"
    "v_mfma_f32_16x16x32_bf16 v[84:87], a[232:235], a[196:199], v[84:87]\n"
    "ds_read_b64_tr_b16 a[250:251], v9 offset:10240\n"
    "v_mfma_f32_16x16x32_bf16 v[84:87], a[236:239], a[212:215], v[84:87]\n"
    "buffer_load_dwordx4 a[188:191], v0, s[40:43], s74 offen offset:1024 sc1\n"
    "v_mfma_f32_16x16x32_bf16 v[88:91], a[232:235], a[200:203], v[88:91]\n"
    "s_addk_i32 s67, 0x1000\n"
    "ds_read_b64_tr_b16 a[252:253], v9 offset:12288\n"
    "v_mfma_f32_16x16x32_bf16 v[88:91], a[236:239], a[216:219], v[88:91]\n"
    "v_mfma_f32_16x16x32_bf16 v[92:95], a[232:235], a[204:207], v[92:95]\n"
    "ds_read_b64_tr_b16 a[254:255], v9 offset:14336\n"
    "v_mfma_f32_16x16x32_bf16 v[92:95], a[236:239], a[220:223], v[92:95]\n"
    "s_waitcnt lgkmcnt(4)\n"
    "v_mfma_f32_16x16x32_bf16 v[96:99], a[240:243], a[192:195], v[96:99]\n"
    "ds_read_b64_tr_b16 a[224:225], v6 offset:16384\n"
    "v_mfma_f32_16x16x32_bf16 v[96:99], a[244:247], a[208:211], v[96:99]\n"
    "buffer_store_dwordx4 v[144:147], v0, s[40:43], s67 offen offset:0 sc1\n"
    "v_mfma_f32_16x16x32_bf16 v[100:103], a[240:243], a[196:199], v[100:103]\n"
    "ds_read_b64_tr_b16 a[226:227], v6 offset:18432\n"
    "v_mfma_f32_16x16x32_bf16 v[100:103], a[244:247], a[212:215], v[100:103]\n"
    "v_mfma_f32_16x16x32_bf16 v[104:107], a[240:243], a[200:203], v[104:107]\n"
    "ds_read_b64_tr_b16 a[228:229], v6 offset:20480\n"
    "v_mfma_f32_16x16x32_bf16 v[104:107], a[244:247], a[216:219], v[104:107]\n"
    "v_mfma_f32_16x16x32_bf16 v[108:111], a[240:243], a[204:207], v[108:111]\n"
    "ds_read_b64_tr_b16 a[230:231], v6 offset:22528\n"
    "v_mfma_f32_16x16x32_bf16 v[108:111], a[244:247], a[220:223], v[108:111]\n"
    "s_waitcnt lgkmcnt(4)\n"
    "v_mfma_f32_16x16x32_bf16 v[112:115], a[248:251], a[192:195], v[112:115]\n"
    "ds_read_b64_tr_b16 a[232:233], v7 offset:16384\n"
    "v_mfma_f32_16x16x32_bf16 v[112:115], a[252:255], a[208:211], v[112:115]\n"
    "buffer_store_dwordx4 v[148:151], v0, s[40:43], s67 offen offset:1024 sc1\n"
    "v_mfma_f32_16x16x32_bf16 v[116:119], a[248:251], a[196:199], v[116:119]\n"
    "ds_read_b64_tr_b16 a[234:235], v7 offset:18432\n"
    "v_mfma_f32_16x16x32_bf16 v[116:119], a[252:255], a[212:215], v[116:119]\n"
    "v_mfma_f32_16x16x32_bf16 v[120:123], a[248:251], a[200:203], v[120:123]\n"
    "ds_read_b64_tr_b16 a[236:237], v7 offset:20480\n"
    "v_mfma_f32_16x16x32_bf16 v[120:123], a[252:255], a[216:219], v[120:123]\n"
    "v_mfma_f32_16x16x32_bf16 v[124:127], a[248:251], a[204:207], v[124:127]\n"
    "ds_read_b64_tr_b16 a[238:239], v7 offset:22528\n"
    "v_mfma_f32_16x16x32_bf16 v[124:127], a[252:255], a[220:223], v[124:127]\n"
    "s_waitcnt lgkmcnt(4)\n"
    "v_mfma_f32_16x16x32_bf16 v[128:131], a[224:227], a[192:195], v[128:131]\n"
    "s_and_b32 s59, s58, 1\n"
    "ds_read_b64_tr_b16 a[240:241], v8 offset:16384\n"
    "v_mfma_f32_16x16x32_bf16 v[128:131], a[228:231], a[208:211], v[128:131]\n"
    "s_mul_i32 s60, s59, 5120\n"
    "v_mfma_f32_16x16x32_bf16 v[132:135], a[224:227], a[196:199], v[132:135]\n"
    "s_add_u32 s60, s60, 0x8000\n"
    "ds_read_b64_tr_b16 a[242:243], v8 offset:18432\n"
    "v_add_u32 v22, s60, v0\n"
    "v_mfma_f32_16x16x32_bf16 v[132:135], a[228:231], a[212:215], v[132:135]\n"
    "s_lshr_b32 s59, s58, 1\n"
    "v_mfma_f32_16x16x32_bf16 v[136:139], a[224:227], a[200:203], v[136:139]\n"
    "s_lshl_b32 s59, s59, 13\n"
    "ds_read_b64_tr_b16 a[244:245], v8 offset:20480\n"
    "v_add_u32 v23, s59, v0\n"
    "v_mfma_f32_16x16x32_bf16 v[136:139], a[228:231], a[216:219], v[136:139]\n"
    "buffer_store_dwordx4 v[152:155], v0, s[40:43], s67 offen offset:2048 sc1\n"
    "v_mfma_f32_16x16x32_bf16 v[140:143], a[224:227], a[204:207], v[140:143]\n"
    "s_add_u32 m0, s82, 0x20000\n"
    "ds_read_b64_tr_b16 a[246:247], v8 offset:22528\n"
    "v_and_b32 v28, 15, v12\n"
    "v_mfma_f32_16x16x32_bf16 v[140:143], a[228:231], a[220:223], v[140:143]\n"
    "s_waitcnt lgkmcnt(4)\n"
    "v_mul_u32_u24 v28, 0x500, v28\n"
    "v_lshrrev_b32 v29, 4, v12\n"
    "v_mfma_f32_16x16x32_bf16 a[16:19], a[232:235], a[192:195], a[16:19]\n"
    "ds_read_b64_tr_b16 a[248:249], v9 offset:16384\n"
    "v_lshl_add_u32 v24, v29, 3, v28\n"
    "v_mfma_f32_16x16x32_bf16 a[16:19], a[236:239], a[208:211], a[16:19]\n"
    "buffer_load_dwordx4 v3, s[52:55], s81 offen offset:0 lds\n"
    "v_mfma_f32_16x16x32_bf16 a[20:23], a[232:235], a[196:199], a[20:23]\n"
    "s_add_u32 s81, s81, s68\n"
    "ds_read_b64_tr_b16 a[250:251], v9 offset:18432\n"
    "v_min_u32 v29, 2, v12\n"
    "v_mfma_f32_16x16x32_bf16 a[20:23], a[236:239], a[212:215], a[20:23]\n"
    "v_add_u32 v28, s28, v29\n"
    "v_add_u32 v28, 1, v28\n"
    "v_mfma_f32_16x16x32_bf16 a[24:27], a[232:235], a[200:203], a[24:27]\n"
    "ds_read_b64_tr_b16 a[252:253], v9 offset:20480\n"
    "v_and_b32 v28, 3, v28\n"
    "v_mfma_f32_16x16x32_bf16 a[24:27], a[236:239], a[216:219], a[24:27]\n"
    "v_subrev_u32 v28, s28, v28\n"
    "v_lshlrev_b32 v28, 8, v28\n"
    "v_mfma_f32_16x16x32_bf16 a[28:31], a[232:235], a[204:207], a[28:31]\n"
    "ds_read_b64_tr_b16 a[254:255], v9 offset:22528\n"
    "v_lshl_add_u32 v28, v29, 2, v28\n"
    "v_mfma_f32_16x16x32_bf16 a[28:31], a[236:239], a[220:223], a[28:31]\n"
    "s_waitcnt lgkmcnt(4)\n"
    "v_add_u32 v25, s34, v28\n"
    "v_lshlrev_b32 v26, 3, v12\n"
    "v_mfma_f32_16x16x32_bf16 a[32:35], a[240:243], a[192:195], a[32:35]\n"
    "ds_read_b64_tr_b16 a[224:225], v6 offset:24576\n"
    "v_mfma_f32_16x16x32_bf16 a[32:35], a[244:247], a[208:211], a[32:35]\n"
    "buffer_load_dwordx4 v3, s[52:55], s81 offen offset:1024 lds\n"
    "v_mfma_f32_16x16x32_bf16 a[36:39], a[240:243], a[196:199], a[36:39]\n"
    "s_add_u32 s81, s81, s69\n"
    "ds_read_b64_tr_b16 a[226:227], v6 offset:26624\n"
    "v_mfma_f32_16x16x32_bf16 a[36:39], a[244:247], a[212:215], a[36:39]\n"
    "buffer_load_dwordx4 v3, s[52:55], s81 offen offset:2048 lds\n"
    "v_mfma_f32_16x16x32_bf16 a[40:43], a[240:243], a[200:203], a[40:43]\n"
    "s_add_u32 s81, s81, s68\n"
    "ds_read_b64_tr_b16 a[228:229], v6 offset:28672\n"
    "v_mfma_f32_16x16x32_bf16 a[40:43], a[244:247], a[216:219], a[40:43]\n"
    "v_mfma_f32_16x16x32_bf16 a[44:47], a[240:243], a[204:207], a[44:47]\n"
    "ds_read_b64_tr_b16 a[230:231], v6 offset:30720\n"
    "v_mfma_f32_16x16x32_bf16 a[44:47], a[244:247], a[220:223], a[44:47]\n"
    "s_waitcnt lgkmcnt(4)\n"
    "v_add_u32 v6, 0x8000, v6\n"
    "v_mfma_f32_16x16x32_bf16 a[48:51], a[248:251], a[192:195], a[48:51]\n"
    "ds_read_b64_tr_b16 a[232:233], v7 offset:24576\n"
    "v_mfma_f32_16x16x32_bf16 a[48:51], a[252:255], a[208:211], a[48:51]\n"
    "buffer_store_dwordx4 v[156:159], v0, s[40:43], s67 offen offset:3072 sc1\n"
    "v_mfma_f32_16x16x32_bf16 a[52:55], a[248:251], a[196:199], a[52:55]\n"
    "ds_read_b64_tr_b16 a[234:235], v7 offset:26624\n"
    "v_mfma_f32_16x16x32_bf16 a[52:55], a[252:255], a[212:215], a[52:55]\n"
    "v_mfma_f32_16x16x32_bf16 a[56:59], a[248:251], a[200:203], a[56:59]\n"
    "ds_read_b64_tr_b16 a[236:237], v7 offset:28672\n"
    "v_mfma_f32_16x16x32_bf16 a[56:59], a[252:255], a[216:219], a[56:59]\n"
    "buffer_load_dwordx4 v3, s[52:55], s81 offen offset:3072 lds\n"
    "v_mfma_f32_16x16x32_bf16 a[60:63], a[248:251], a[204:207], a[60:63]\n"
    "s_add_u32 s81, s81, s70\n"
    "ds_read_b64_tr_b16 a[238:239], v7 offset:30720\n"
    "v_mfma_f32_16x16x32_bf16 a[60:63], a[252:255], a[220:223], a[60:63]\n"
    "s_waitcnt lgkmcnt(4)\n"
    "v_add_u32 v7, 0x8000, v7\n"
    "v_mfma_f32_16x16x32_bf16 a[64:67], a[224:227], a[192:195], a[64:67]\n"
    "s_addk_i32 m0, 0x1000\n"
    "ds_read_b64_tr_b16 a[240:241], v8 offset:24576\n"
    "v_mfma_f32_16x16x32_bf16 a[64:67], a[228:231], a[208:211], a[64:67]\n"
    "v_mfma_f32_16x16x32_bf16 a[68:71], a[224:227], a[196:199], a[68:71]\n"
    "ds_read_b64_tr_b16 a[242:243], v8 offset:26624\n"
    "v_mfma_f32_16x16x32_bf16 a[68:71], a[228:231], a[212:215], a[68:71]\n"
    "buffer_load_dwordx4 v3, s[52:55], s81 offen offset:0 lds\n"
    "v_mfma_f32_16x16x32_bf16 a[72:75], a[224:227], a[200:203], a[72:75]\n"
    "s_add_u32 s81, s81, s68\n"
    "ds_read_b64_tr_b16 a[244:245], v8 offset:28672\n"
    "v_mfma_f32_16x16x32_bf16 a[72:75], a[228:231], a[216:219], a[72:75]\n"
    "v_mfma_f32_16x16x32_bf16 a[76:79], a[224:227], a[204:207], a[76:79]\n"
    "ds_read_b64_tr_b16 a[246:247], v8 offset:30720\n"
    "v_mfma_f32_16x16x32_bf16 a[76:79], a[228:231], a[220:223], a[76:79]\n"
    "s_waitcnt lgkmcnt(4)\n"
    "v_add_u32 v8, 0x8000, v8\n"
    "v_mfma_f32_16x16x32_bf16 a[80:83], a[232:235], a[192:195], a[80:83]\n"
    "ds_read_b64_tr_b16 a[248:249], v9 offset:24576\n"
    "v_mfma_f32_16x16x32_bf16 a[80:83], a[236:239], a[208:211], a[80:83]\n"
    "buffer_load_dwordx4 v3, s[52:55], s81 offen offset:1024 lds\n"
    "v_mfma_f32_16x16x32_bf16 a[84:87], a[232:235], a[196:199], a[84:87]\n"
    "s_add_u32 s81, s81, s69\n"
    "ds_read_b64_tr_b16 a[250:251], v9 offset:26624\n"
    "v_mfma_f32_16x16x32_bf16 a[84:87], a[236:239], a[212:215], a[84:87]\n"
    "s_addk_i32 s67, 0x3000\n"
    "v_mfma_f32_16x16x32_bf16 a[88:91], a[232:235], a[200:203], a[88:91]\n"
    "s_waitcnt vmcnt(42)\n"
    "ds_read_b64_tr_b16 a[252:253], v9 offset:28672\n"
    "v_mfma_f32_16x16x32_bf16 a[88:91], a[236:239], a[216:219], a[88:91]\n"
    "s_barrier\n"
    "v_mfma_f32_16x16x32_bf16 a[92:95], a[232:235], a[204:207], a[92:95]\n"
    "ds_read_b64_tr_b16 a[254:255], v9 offset:30720\n"
    "v_mfma_f32_16x16x32_bf16 a[92:95], a[236:239], a[220:223], a[92:95]\n"
    "s_waitcnt lgkmcnt(4)\n"
    "v_add_u32 v9, 0x8000, v9\n"
    "v_mfma_f32_16x16x32_bf16 a[96:99], a[240:243], a[192:195], a[96:99]\n"
    "ds_read_b64_tr_b16 a[224:225], v6 offset:0\n"
    "v_mfma_f32_16x16x32_bf16 a[96:99], a[244:247], a[208:211], a[96:99]\n"
    "buffer_store_dwordx4 v[160:163], v0, s[40:43], s67 offen offset:0 sc1\n"
    "v_mfma_f32_16x16x32_bf16 a[100:103], a[240:243], a[196:199], a[100:103]\n"
    "ds_read_b64_tr_b16 a[226:227], v6 offset:2048\n"
    "v_mfma_f32_16x16x32_bf16 a[100:103], a[244:247], a[212:215], a[100:103]\n"
    "buffer_load_dwordx4 v3, s[52:55], s81 offen offset:2048 lds\n"
    "v_mfma_f32_16x16x32_bf16 a[104:107], a[240:243], a[200:203], a[104:107]\n"
    "s_add_u32 s81, s81, s68\n"
    "ds_read_b64_tr_b16 a[228:229], v6 offset:4096\n"
    "v_mfma_f32_16x16x32_bf16 a[104:107], a[244:247], a[216:219], a[104:107]\n"
    "v_mfma_f32_16x16x32_bf16 a[108:111], a[240:243], a[204:207], a[108:111]\n"
    "ds_read_b64_tr_b16 a[230:231], v6 offset:6144\n"
    "v_mfma_f32_16x16x32_bf16 a[108:111], a[244:247], a[220:223], a[108:111]\n"
    "s_waitcnt lgkmcnt(4)\n"
    "v_mfma_f32_16x16x32_bf16 a[112:115], a[248:251], a[192:195], a[112:115]\n"
    "ds_read_b64_tr_b16 a[232:233], v7 offset:0\n"
    "v_mfma_f32_16x16x32_bf16 a[112:115], a[252:255], a[208:211], a[112:115]\n"
    "buffer_load_dwordx4 v3, s[52:55], s81 offen offset:3072 lds\n"
    "v_mfma_f32_16x16x32_bf16 a[116:119], a[248:251], a[196:199], a[116:119]\n"
    "s_add_u32 s81, s81, s70\n"
    "ds_read_b64_tr_b16 a[234:235], v7 offset:2048\n"
    "v_mfma_f32_16x16x32_bf16 a[116:119], a[252:255], a[212:215], a[116:119]\n"
    "v_mfma_f32_16x16x32_bf16 a[120:123], a[248:251], a[200:203], a[120:123]\n"
    "ds_read_b64_tr_b16 a[236:237], v7 offset:4096\n"
    "v_accvgpr_read_b32 v144, a16\n"
    "v_mfma_f32_16x16x32_bf16 a[120:123], a[252:255], a[216:219], a[120:123]\n"
    "v_accvgpr_read_b32 v145, a17\n"
    "v_accvgpr_read_b32 v146, a18\n"
    "v_mfma_f32_16x16x32_bf16 a[124:127], a[248:251], a[204:207], a[124:127]\n"
    "s_waitcnt vmcnt(13)\n"
    "ds_read_b64_tr_b16 a[238:239], v7 offset:6144\n"
    "v_accvgpr_read_b32 v147, a19\n"
    "v_mfma_f32_16x16x32_bf16 a[124:127], a[252:255], a[220:223], a[124:127]\n"
    "s_waitcnt lgkmcnt(4)\n"
    "v_accvgpr_read_b32 v148, a20\n"
    "v_accvgpr_read_b32 v149, a21\n"
    "v_mfma_f32_16x16x32_bf16 a[128:131], a[224:227], a[160:163], a[128:131]\n"
    "ds_read_b64_tr_b16 a[240:241], v8 offset:0\n"
    "v_accvgpr_read_b32 v150, a22\n"
    "v_mfma_f32_16x16x32_bf16 a[128:131], a[228:231], a[176:179], a[128:131]\n"
    "v_accvgpr_read_b32 v151, a23\n"
    "v_accvgpr_read_b32 v152, a24\n"
    "v_mfma_f32_16x16x32_bf16 a[132:135], a[224:227], a[164:167], a[132:135]\n"
    "ds_read_b64_tr_b16 a[242:243], v8 offset:2048\n"
    "v_accvgpr_read_b32 v153, a25\n"
    "v_mfma_f32_16x16x32_bf16 a[132:135], a[228:231], a[180:183], a[132:135]\n"
    "v_accvgpr_read_b32 v154, a26\n"
    "v_accvgpr_read_b32 v155, a27\n"
    "v_mfma_f32_16x16x32_bf16 a[136:139], a[224:227], a[168:171], a[136:139]\n"
    "ds_read_b64_tr_b16 a[244:245], v8 offset:4096\n"
    "v_accvgpr_read_b32 v156, a28\n"
    "v_mfma_f32_16x16x32_bf16 a[136:139], a[228:231], a[184:187], a[136:139]\n"
    "buffer_load_dwordx4 a[192:195], v0, s[40:43], s71 offen offset:2048 sc1\n"
    "v_mfma_f32_16x16x32_bf16 a[140:143], a[224:227], a[172:175], a[140:143]\n"
    "ds_read_b64_tr_b16 a[246:247], v8 offset:6144\n"
    "v_accvgpr_read_b32 v157, a29\n"
    "v_mfma_f32_16x16x32_bf16 a[140:143], a[228:231], a[188:191], a[140:143]\n"
    "s_waitcnt lgkmcnt(4)\n"
    "v_accvgpr_read_b32 v158, a30\n"
    "v_accvgpr_read_b32 v159, a31\n"
    "v_mfma_f32_16x16x32_bf16 a[144:147], a[232:235], a[160:163], a[144:147]\n"
    "ds_read_b64_tr_b16 a[248:249], v9 offset:0\n"
    "v_accvgpr_write_b32 a16, 0\n"
    "v_mfma_f32_16x16x32_bf16 a[144:147], a[236:239], a[176:179], a[144:147]\n"
    "buffer_load_dwordx4 a[196:199], v0, s[40:43], s72 offen offset:2048 sc1\n"
    "v_mfma_f32_16x16x32_bf16 a[148:151], a[232:235], a[164:167], a[148:151]\n"
    "ds_read_b64_tr_b16 a[250:251], v9 offset:2048\n"
    "v_accvgpr_write_b32 a17, 0\n"
    "v_mfma_f32_16x16x32_bf16 a[148:151], a[236:239], a[180:183], a[148:151]\n"
    "v_accvgpr_write_b32 a18, 0\n"
    "v_accvgpr_write_b32 a19, 0\n"
    "v_mfma_f32_16x16x32_bf16 a[152:155], a[232:235], a[168:171], a[152:155]\n"
    "ds_read_b64_tr_b16 a[252:253], v9 offset:4096\n"
    "v_accvgpr_write_b32 a20, 0\n"
    "v_mfma_f32_16x16x32_bf16 a[152:155], a[236:239], a[184:187], a[152:155]\n"
    "v_accvgpr_write_b32 a21, 0\n"
    "v_accvgpr_write_b32 a22, 0\n"
    "v_mfma_f32_16x16x32_bf16 a[156:159], a[232:235], a[172:175], a[156:159]\n"
    "ds_read_b64_tr_b16 a[254:255], v9 offset:6144\n"
    "v_accvgpr_write_b32 a23, 0\n"
    "v_mfma_f32_16x16x32_bf16 a[156:159], a[236:239], a[188:191], a[156:159]\n"
    "s_waitcnt lgkmcnt(4)\n"
    "v_accvgpr_write_b32 a24, 0\n"
    "v_accvgpr_write_b32 a25, 0\n"
    "v_mfma_f32_16x16x32_bf16 v[32:35], a[240:243], a[160:163], v[32:35]\n"
    "ds_read_b64_tr_b16 a[224:225], v6 offset:8192\n"
    "v_accvgpr_write_b32 a26, 0\n"
    "v_mfma_f32_16x16x32_bf16 v[32:35], a[244:247], a[176:179], v[32:35]\n"
    "buffer_load_dwordx4 a[200:203], v0, s[40:43], s73 offen offset:2048 sc1\n"
    "v_mfma_f32_16x16x32_bf16 v[36:39], a[240:243], a[164:167], v[36:39]\n"
    "ds_read_b64_tr_b16 a[226:227], v6 offset:10240\n"
    "v_accvgpr_write_b32 a27, 0\n"
    "v_mfma_f32_16x16x32_bf16 v[36:39], a[244:247], a[180:183], v[36:39]\n"
    "v_accvgpr_write_b32 a28, 0\n"
    "v_accvgpr_write_b32 a29, 0\n"
    "v_mfma_f32_16x16x32_bf16 v[40:43], a[240:243], a[168:171], v[40:43]\n"
    "ds_read_b64_tr_b16 a[228:229], v6 offset:12288\n"
    "v_accvgpr_write_b32 a30, 0\n"
    "v_mfma_f32_16x16x32_bf16 v[40:43], a[244:247], a[184:187], v[40:43]\n"
    "buffer_load_dwordx4 a[204:207], v0, s[40:43], s74 offen offset:2048 sc1\n"
    "v_mfma_f32_16x16x32_bf16 v[44:47], a[240:243], a[172:175], v[44:47]\n"
    "ds_read_b64_tr_b16 a[230:231], v6 offset:14336\n"
    "v_accvgpr_write_b32 a31, 0\n"
    "v_mfma_f32_16x16x32_bf16 v[44:47], a[244:247], a[188:191], v[44:47]\n"
    "s_waitcnt lgkmcnt(4)\n"
    "v_mfma_f32_16x16x32_bf16 v[48:51], a[248:251], a[160:163], v[48:51]\n"
    "ds_read_b64_tr_b16 a[232:233], v7 offset:8192\n"
    "v_mfma_f32_16x16x32_bf16 v[48:51], a[252:255], a[176:179], v[48:51]\n"
    "v_mfma_f32_16x16x32_bf16 v[52:55], a[248:251], a[164:167], v[52:55]\n"
    "ds_read_b64_tr_b16 a[234:235], v7 offset:10240\n"
    "v_mfma_f32_16x16x32_bf16 v[52:55], a[252:255], a[180:183], v[52:55]\n"
    "buffer_load_dwordx4 a[208:211], v0, s[40:43], s71 offen offset:3072 sc1\n"
    "v_mfma_f32_16x16x32_bf16 v[56:59], a[248:251], a[168:171], v[56:59]\n"
    "s_addk_i32 s71, 0x1000\n"
    "ds_read_b64_tr_b16 a[236:237], v7 offset:12288\n"
    "v_mfma_f32_16x16x32_bf16 v[56:59], a[252:255], a[184:187], v[56:59]\n"
    "v_mfma_f32_16x16x32_bf16 v[60:63], a[248:251], a[172:175], v[60:63]\n"
    "ds_read_b64_tr_b16 a[238:239], v7 offset:14336\n"
    "v_mfma_f32_16x16x32_bf16 v[60:63], a[252:255], a[188:191], v[60:63]\n"
    "s_waitcnt lgkmcnt(4)\n"
    "v_mfma_f32_16x16x32_bf16 v[64:67], a[224:227], a[160:163], v[64:67]\n"
    "ds_read_b64_tr_b16 a[240:241], v8 offset:8192\n"
    "v_mfma_f32_16x16x32_bf16 v[64:67], a[228:231], a[176:179], v[64:67]\n"
    "buffer_load_dwordx4 a[212:215], v0, s[40:43], s72 offen offset:3072 sc1\n"
    "v_mfma_f32_16x16x32_bf16 v[68:71], a[224:227], a[164:167], v[68:71]\n"
    "s_addk_i32 s72, 0x1000\n"
    "ds_read_b64_tr_b16 a[242:243], v8 offset:10240\n"
    "v_mfma_f32_16x16x32_bf16 v[68:71], a[228:231], a[180:183], v[68:71]\n"
    "v_mfma_f32_16x16x32_bf16 v[72:75], a[224:227], a[168:171], v[72:75]\n"
    "ds_read_b64_tr_b16 a[244:245], v8 offset:12288\n"
    "v_mfma_f32_16x16x32_bf16 v[72:75], a[228:231], a[184:187], v[72:75]\n"
    "buffer_load_dwordx4 a[216:219], v0, s[40:43], s73 offen offset:3072 sc1\n"
    "v_mfma_f32_16x16x32_bf16 v[76:79], a[224:227], a[172:175], v[76:79]\n"
    "s_addk_i32 s73, 0x1000\n"
    "ds_read_b64_tr_b16 a[246:247], v8 offset:14336\n"
    "v_mfma_f32_16x16x32_bf16 v[76:79], a[228:231], a[188:191], v[76:79]\n"
    "s_waitcnt lgkmcnt(4)\n"
    "v_mfma_f32_16x16x32_bf16 v[80:83], a[232:235], a[160:163], v[80:83]\n"
    "ds_read_b64_tr_b16 a[248:249], v9 offset:8192\n"
    "v_mfma_f32_16x16x32_bf16 v[80:83], a[236:239], a[176:179], v[80:83]\n"
    "v_mfma_f32_16x16x32_bf16 v[84:87], a[232:235], a[164:167], v[84:87]\n"
    "ds_read_b64_tr_b16 a[250:251], v9 offset:10240\n"
    "v_mfma_f32_16x16x32_bf16 v[84:87], a[236:239], a[180:183], v[84:87]\n"
    "buffer_load_dwordx4 a[220:223], v0, s[40:43], s74 offen offset:3072 sc1\n"
    "v_mfma_f32_16x16x32_bf16 v[88:91], a[232:235], a[168:171], v[88:91]\n"
    "s_addk_i32 s74, 0x1000\n"
    "ds_read_b64_tr_b16 a[252:253], v9 offset:12288\n"
    "v_mfma_f32_16x16x32_bf16 v[88:91], a[236:239], a[184:187], v[88:91]\n"
    "v_mfma_f32_16x16x32_bf16 v[92:95], a[232:235], a[172:175], v[92:95]\n"
    "ds_read_b64_tr_b16 a[254:255], v9 offset:14336\n"
    "v_mfma_f32_16x16x32_bf16 v[92:95], a[236:239], a[188:191], v[92:95]\n"
    "s_waitcnt lgkmcnt(4)\n"
    "v_mfma_f32_16x16x32_bf16 v[96:99], a[240:243], a[160:163], v[96:99]\n"
    "ds_read_b64_tr_b16 a[224:225], v6 offset:16384\n"
    "v_mfma_f32_16x16x32_bf16 v[96:99], a[244:247], a[176:179], v[96:99]\n"
    "buffer_store_dwordx4 v[164:167], v0, s[40:43], s67 offen offset:1024 sc1\n"
    "v_mfma_f32_16x16x32_bf16 v[100:103], a[240:243], a[164:167], v[100:103]\n"
    "ds_read_b64_tr_b16 a[226:227], v6 offset:18432\n"
    "v_mfma_f32_16x16x32_bf16 v[100:103], a[244:247], a[180:183], v[100:103]\n"
    "v_mfma_f32_16x16x32_bf16 v[104:107], a[240:243], a[168:171], v[104:107]\n"
    "ds_read_b64_tr_b16 a[228:229], v6 offset:20480\n"
    "v_mfma_f32_16x16x32_bf16 v[104:107], a[244:247], a[184:187], v[104:107]\n"
    "v_mfma_f32_16x16x32_bf16 v[108:111], a[240:243], a[172:175], v[108:111]\n"
    "ds_read_b64_tr_b16 a[230:231], v6 offset:22528\n"
    "v_mfma_f32_16x16x32_bf16 v[108:111], a[244:247], a[188:191], v[108:111]\n"
    "s_waitcnt lgkmcnt(4)\n"
    "v_mfma_f32_16x16x32_bf16 v[112:115], a[248:251], a[160:163], v[112:115]\n"
    "ds_read_b64_tr_b16 a[232:233], v7 offset:16384\n"
    "v_mfma_f32_16x16x32_bf16 v[112:115], a[252:255], a[176:179], v[112:115]\n"
    "buffer_store_dwordx4 v[168:171], v0, s[40:43], s67 offen offset:2048 sc1\n"
    "v_mfma_f32_16x16x32_bf16 v[116:119], a[248:251], a[164:167], v[116:119]\n"
    "ds_read_b64_tr_b16 a[234:235], v7 offset:18432\n"
    "v_mfma_f32_16x16x32_bf16 v[116:119], a[252:255], a[180:183], v[116:119]\n"
    "v_mfma_f32_16x16x32_bf16 v[120:123], a[248:251], a[168:171], v[120:123]\n"
    "ds_read_b64_tr_b16 a[236:237], v7 offset:20480\n"
    "v_mfma_f32_16x16x32_bf16 v[120:123], a[252:255], a[184:187], v[120:123]\n"
    "v_mfma_f32_16x16x32_bf16 v[124:127], a[248:251], a[172:175], v[124:127]\n"
    "ds_read_b64_tr_b16 a[238:239], v7 offset:22528\n"
    "v_mfma_f32_16x16x32_bf16 v[124:127], a[252:255], a[188:191], v[124:127]\n"
    "s_waitcnt lgkmcnt(4)\n"
    "v_mfma_f32_16x16x32_bf16 v[128:131], a[224:227], a[160:163], v[128:131]\n"
    "ds_read_b64_tr_b16 a[240:241], v8 offset:16384\n"
    "v_mfma_f32_16x16x32_bf16 v[128:131], a[228:231], a[176:179], v[128:131]\n"
    "v_mfma_f32_16x16x32_bf16 v[132:135], a[224:227], a[164:167], v[132:135]\n"
    "ds_read_b64_tr_b16 a[242:243], v8 offset:18432\n"
    "v_mfma_f32_16x16x32_bf16 v[132:135], a[228:231], a[180:183], v[132:135]\n"
    "v_mfma_f32_16x16x32_bf16 v[136:139], a[224:227], a[168:171], v[136:139]\n"
    "ds_read_b64_tr_b16 a[244:245], v8 offset:20480\n"
    "v_mfma_f32_16x16x32_bf16 v[136:139], a[228:231], a[184:187], v[136:139]\n"
    "buffer_store_dwordx4 v[172:175], v0, s[40:43], s67 offen offset:3072 sc1\n"
    "v_mfma_f32_16x16x32_bf16 v[140:143], a[224:227], a[172:175], v[140:143]\n"
    "s_add_u32 m0, s82, 0\n"
    "ds_read_b64_tr_b16 a[246:247], v8 offset:22528\n"
    "v_mfma_f32_16x16x32_bf16 v[140:143], a[228:231], a[188:191], v[140:143]\n"
    "s_waitcnt lgkmcnt(4)\n"
    "v_mfma_f32_16x16x32_bf16 v[144:147], a[232:235], a[160:163], v[144:147]\n"
    "ds_read_b64_tr_b16 a[248:249], v9 offset:16384\n"
    "v_mfma_f32_16x16x32_bf16 v[144:147], a[236:239], a[176:179], v[144:147]\n"
    "buffer_load_dwordx4 v3, s[52:55], s81 offen offset:0 lds\n"
    "v_mfma_f32_16x16x32_bf16 v[148:151], a[232:235], a[164:167], v[148:151]\n"
    "s_add_u32 s81, s81, s68\n"
    "ds_read_b64_tr_b16 a[250:251], v9 offset:18432\n"
    "v_mfma_f32_16x16x32_bf16 v[148:151], a[236:239], a[180:183], v[148:151]\n"
    "v_mfma_f32_16x16x32_bf16 v[152:155], a[232:235], a[168:171], v[152:155]\n"
    "ds_read_b64_tr_b16 a[252:253], v9 offset:20480\n"
    "v_mfma_f32_16x16x32_bf16 v[152:155], a[236:239], a[184:187], v[152:155]\n"
    "v_mfma_f32_16x16x32_bf16 v[156:159], a[232:235], a[172:175], v[156:159]\n"
    "ds_read_b64_tr_b16 a[254:255], v9 offset:22528\n"
    "v_mfma_f32_16x16x32_bf16 v[156:159], a[236:239], a[188:191], v[156:159]\n"
    "s_waitcnt lgkmcnt(4)\n"
    "v_mfma_f32_16x16x32_bf16 a[32:35], a[240:243], a[160:163], a[32:35]\n"
    "ds_read_b64_tr_b16 a[224:225], v6 offset:24576\n"
    "v_mfma_f32_16x16x32_bf16 a[32:35], a[244:247], a[176:179], a[32:35]\n"
    "buffer_load_dwordx4 v3, s[52:55], s81 offen offset:1024 lds\n"
    "v_mfma_f32_16x16x32_bf16 a[36:39], a[240:243], a[164:167], a[36:39]\n"
    "s_add_u32 s81, s81, s69\n"
    "ds_read_b64_tr_b16 a[226:227], v6 offset:26624\n"
    "v_mfma_f32_16x16x32_bf16 a[36:39], a[244:247], a[180:183], a[36:39]\n"
    "v_mfma_f32_16x16x32_bf16 a[40:43], a[240:243], a[168:171], a[40:43]\n"
    "ds_read_b64_tr_b16 a[228:229], v6 offset:28672\n"
    "v_mfma_f32_16x16x32_bf16 a[40:43], a[244:247], a[184:187], a[40:43]\n"
    "buffer_load_dwordx4 v3, s[52:55], s81 offen offset:2048 lds\n"
    "v_mfma_f32_16x16x32_bf16 a[44:47], a[240:243], a[172:175], a[44:47]\n"
    "s_add_u32 s81, s81, s68\n"
    "ds_read_b64_tr_b16 a[230:231], v6 offset:30720\n"
    "v_mfma_f32_16x16x32_bf16 a[44:47], a[244:247], a[188:191], a[44:47]\n"
    "s_waitcnt lgkmcnt(4)\n"
    "v_add_u32 v6, 0x8000, v6\n"
    "v_mfma_f32_16x16x32_bf16 a[48:51], a[248:251], a[160:163], a[48:51]\n"
    "ds_read_b64_tr_b16 a[232:233], v7 offset:24576\n"
    "v_mfma_f32_16x16x32_bf16 a[48:51], a[252:255], a[176:179], a[48:51]\n"
    "v_mfma_f32_16x16x32_bf16 a[52:55], a[248:251], a[164:167], a[52:55]\n"
    "ds_read_b64_tr_b16 a[234:235], v7 offset:26624\n"
    "v_mfma_f32_16x16x32_bf16 a[52:55], a[252:255], a[180:183], a[52:55]\n"
    "buffer_load_dwordx4 v3, s[52:55], s81 offen offset:3072 lds\n"
    "v_mfma_f32_16x16x32_bf16 a[56:59], a[248:251], a[168:171], a[56:59]\n"
    "s_add_u32 s81, s81, s70\n"
    "ds_read_b64_tr_b16 a[236:237], v7 offset:28672\n"
    "v_mfma_f32_16x16x32_bf16 a[56:59], a[252:255], a[184:187], a[56:59]\n"
    "s_addk_i32 s67, 0x1000\n"
    "v_mfma_f32_16x16x32_bf16 a[60:63], a[248:251], a[172:175], a[60:63]\n"
    "ds_read_b64_tr_b16 a[238:239], v7 offset:30720\n"
    "v_mfma_f32_16x16x32_bf16 a[60:63], a[252:255], a[188:191], a[60:63]\n"
    "s_waitcnt lgkmcnt(4)\n"
    "v_add_u32 v7, 0x8000, v7\n"
    "v_mfma_f32_16x16x32_bf16 a[64:67], a[224:227], a[160:163], a[64:67]\n"
    "ds_read_b64_tr_b16 a[240:241], v8 offset:24576\n"
    "v_mfma_f32_16x16x32_bf16 a[64:67], a[228:231], a[176:179], a[64:67]\n"
    "buffer_store_dwordx4 v[176:179], v0, s[40:43], s67 offen offset:0 sc1\n"
    "v_mfma_f32_16x16x32_bf16 a[68:71], a[224:227], a[164:167], a[68:71]\n"
    "s_addk_i32 m0, 0x1000\n"
    "ds_read_b64_tr_b16 a[242:243], v8 offset:26624\n"
    "v_mfma_f32_16x16x32_bf16 a[68:71], a[228:231], a[180:183], a[68:71]\n"
    "v_mfma_f32_16x16x32_bf16 a[72:75], a[224:227], a[168:171], a[72:75]\n"
    "ds_read_b64_tr_b16 a[244:245], v8 offset:28672\n"
    "v_mfma_f32_16x16x32_bf16 a[72:75], a[228:231], a[184:187], a[72:75]\n"
    "buffer_load_dwordx4 v3, s[52:55], s81 offen offset:0 lds\n"
    "v_mfma_f32_16x16x32_bf16 a[76:79], a[224:227], a[172:175], a[76:79]\n"
    "s_add_u32 s81, s81, s68\n"
    "ds_read_b64_tr_b16 a[246:247], v8 offset:30720\n"
    "v_mfma_f32_16x16x32_bf16 a[76:79], a[228:231], a[188:191], a[76:79]\n"
    "s_waitcnt lgkmcnt(4)\n"
    "v_add_u32 v8, 0x8000, v8\n"
    "v_mfma_f32_16x16x32_bf16 a[80:83], a[232:235], a[160:163], a[80:83]\n"
    "ds_read_b64_tr_b16 a[248:249], v9 offset:24576\n"
    "v_mfma_f32_16x16x32_bf16 a[80:83], a[236:239], a[176:179], a[80:83]\n"
    "v_mfma_f32_16x16x32_bf16 a[84:87], a[232:235], a[164:167], a[84:87]\n"
    "ds_read_b64_tr_b16 a[250:251], v9 offset:26624\n"
    "v_mfma_f32_16x16x32_bf16 a[84:87], a[236:239], a[180:183], a[84:87]\n"
    "buffer_load_dwordx4 v3, s[52:55], s81 offen offset:1024 lds\n"
    "v_mfma_f32_16x16x32_bf16 a[88:91], a[232:235], a[168:171], a[88:91]\n"
    "s_waitcnt vmcnt(47)\n"
    "ds_read_b64_tr_b16 a[252:253], v9 offset:28672\n"
    "v_mfma_f32_16x16x32_bf16 a[88:91], a[236:239], a[184:187], a[88:91]\n"
    "s_barrier\n"
    "v_mfma_f32_16x16x32_bf16 a[92:95], a[232:235], a[172:175], a[92:95]\n"
    "s_add_u32 s81, s81, s69\n"
    "ds_read_b64_tr_b16 a[254:255], v9 offset:30720\n"
    "v_mfma_f32_16x16x32_bf16 a[92:95], a[236:239], a[188:191], a[92:95]\n"
    "s_waitcnt lgkmcnt(4)\n"
    "v_add_u32 v9, 0x8000, v9\n"
    "v_mfma_f32_16x16x32_bf16 a[96:99], a[240:243], a[160:163], a[96:99]\n"
    "ds_read_b64_tr_b16 a[224:225], v6 offset:0\n"
    "v_mfma_f32_16x16x32_bf16 a[96:99], a[244:247], a[176:179], a[96:99]\n"
    "buffer_load_dwordx4 v3, s[52:55], s81 offen offset:2048 lds\n"
    "v_mfma_f32_16x16x32_bf16 a[100:103], a[240:243], a[164:167], a[100:103]\n"
    "s_add_u32 s81, s81, s68\n"
    "ds_read_b64_tr_b16 a[226:227], v6 offset:2048\n"
    "v_mfma_f32_16x16x32_bf16 a[100:103], a[244:247], a[180:183], a[100:103]\n"
    "v_mfma_f32_16x16x32_bf16 a[104:107], a[240:243], a[168:171], a[104:107]\n"
    "ds_read_b64_tr_b16 a[228:229], v6 offset:4096\n"
    "v_mfma_f32_16x16x32_bf16 a[104:107], a[244:247], a[184:187], a[104:107]\n"
    "v_mfma_f32_16x16x32_bf16 a[108:111], a[240:243], a[172:175], a[108:111]\n"
    "ds_read_b64_tr_b16 a[230:231], v6 offset:6144\n"
    "v_mfma_f32_16x16x32_bf16 a[108:111], a[244:247], a[188:191], a[108:111]\n"
    "s_waitcnt lgkmcnt(4)\n"
    "v_mfma_f32_16x16x32_bf16 a[112:115], a[248:251], a[160:163], a[112:115]\n"
    "ds_read_b64_tr_b16 a[232:233], v7 offset:0\n"
    "v_mfma_f32_16x16x32_bf16 a[112:115], a[252:255], a[176:179], a[112:115]\n"
    "buffer_load_dwordx4 v3, s[52:55], s81 offen offset:3072 lds\n"
    "v_mfma_f32_16x16x32_bf16 a[116:119], a[248:251], a[164:167], a[116:119]\n"
    "s_add_u32 s81, s81, s70\n"
    "ds_read_b64_tr_b16 a[234:235], v7 offset:2048\n"
    "v_mfma_f32_16x16x32_bf16 a[116:119], a[252:255], a[180:183], a[116:119]\n"
    "v_mfma_f32_16x16x32_bf16 a[120:123], a[248:251], a[168:171], a[120:123]\n"
    "ds_read_b64_tr_b16 a[236:237], v7 offset:4096\n"
    "v_accvgpr_read_b32 v160, a32\n"
    "v_mfma_f32_16x16x32_bf16 a[120:123], a[252:255], a[184:187], a[120:123]\n"
    "v_accvgpr_read_b32 v161, a33\n"
    "v_accvgpr_read_b32 v162, a34\n"
    "v_mfma_f32_16x16x32_bf16 a[124:127], a[248:251], a[172:175], a[124:127]\n"
    "s_waitcnt vmcnt(12)\n"
    "ds_read_b64_tr_b16 a[238:239], v7 offset:6144\n"
    "v_accvgpr_read_b32 v163, a35\n"
    "v_mfma_f32_16x16x32_bf16 a[124:127], a[252:255], a[188:191], a[124:127]\n"
    "s_waitcnt lgkmcnt(4)\n"
    "v_accvgpr_read_b32 v164, a36\n"
    "v_accvgpr_read_b32 v165, a37\n"
    "v_mfma_f32_16x16x32_bf16 a[128:131], a[224:227], a[192:195], a[128:131]\n"
    "ds_read_b64_tr_b16 a[240:241], v8 offset:0\n"
    "v_accvgpr_read_b32 v166, a38\n"
    "v_mfma_f32_16x16x32_bf16 a[128:131], a[228:231], a[208:211], a[128:131]\n"
    "v_accvgpr_read_b32 v167, a39\n"
    "v_accvgpr_read_b32 v168, a40\n"
    "v_mfma_f32_16x16x32_bf16 a[132:135], a[224:227], a[196:199], a[132:135]\n"
    "ds_read_b64_tr_b16 a[242:243], v8 offset:2048\n"
    "v_accvgpr_read_b32 v169, a41\n"
    "v_mfma_f32_16x16x32_bf16 a[132:135], a[228:231], a[212:215], a[132:135]\n"
    "v_accvgpr_read_b32 v170, a42\n"
    "v_accvgpr_read_b32 v171, a43\n"
    "v_mfma_f32_16x16x32_bf16 a[136:139], a[224:227], a[200:203], a[136:139]\n"
    "ds_read_b64_tr_b16 a[244:245], v8 offset:4096\n"
    "v_accvgpr_read_b32 v172, a44\n"
    "v_mfma_f32_16x16x32_bf16 a[136:139], a[228:231], a[216:219], a[136:139]\n"
    "buffer_load_dwordx4 a[160:163], v0, s[40:43], s71 offen offset:0 sc1\n"
    "v_mfma_f32_16x16x32_bf16 a[140:143], a[224:227], a[204:207], a[140:143]\n"
    "ds_read_b64_tr_b16 a[246:247], v8 offset:6144\n"
    "v_accvgpr_read_b32 v173, a45\n"
    "v_mfma_f32_16x16x32_bf16 a[140:143], a[228:231], a[220:223], a[140:143]\n"
    "s_waitcnt lgkmcnt(4)\n"
    "v_accvgpr_read_b32 v174, a46\n"
    "v_accvgpr_read_b32 v175, a47\n"
    "v_mfma_f32_16x16x32_bf16 a[144:147], a[232:235], a[192:195], a[144:147]\n"
    "ds_read_b64_tr_b16 a[248:249], v9 offset:0\n"
    "v_accvgpr_write_b32 a32, 0\n"
    "v_mfma_f32_16x16x32_bf16 a[144:147], a[236:239], a[208:211], a[144:147]\n"
    "buffer_load_dwordx4 a[164:167], v0, s[40:43], s72 offen offset:0 sc1\n"
    "v_mfma_f32_16x16x32_bf16 a[148:151], a[232:235], a[196:199], a[148:151]\n"
    "ds_read_b64_tr_b16 a[250:251], v9 offset:2048\n"
    "v_accvgpr_write_b32 a33, 0\n"
    "v_mfma_f32_16x16x32_bf16 a[148:151], a[236:239], a[212:215], a[148:151]\n"
    "v_accvgpr_write_b32 a34, 0\n"
    "v_accvgpr_write_b32 a35, 0\n"
    "v_mfma_f32_16x16x32_bf16 a[152:155], a[232:235], a[200:203], a[152:155]\n"
    "ds_read_b64_tr_b16 a[252:253], v9 offset:4096\n"
    "v_accvgpr_write_b32 a36, 0\n"
    "v_mfma_f32_16x16x32_bf16 a[152:155], a[236:239], a[216:219], a[152:155]\n"
    "v_accvgpr_write_b32 a37, 0\n"
    "v_accvgpr_write_b32 a38, 0\n"
    "v_mfma_f32_16x16x32_bf16 a[156:159], a[232:235], a[204:207], a[156:159]\n"
    "ds_read_b64_tr_b16 a[254:255], v9 offset:6144\n"
    "v_accvgpr_write_b32 a39, 0\n"
    "v_mfma_f32_16x16x32_bf16 a[156:159], a[236:239], a[220:223], a[156:159]\n"
    "s_waitcnt lgkmcnt(4)\n"
    "v_accvgpr_write_b32 a40, 0\n"
    "v_accvgpr_write_b32 a41, 0\n"
    "v_mfma_f32_16x16x32_bf16 v[32:35], a[240:243], a[192:195], v[32:35]\n"
    "ds_read_b64_tr_b16 a[224:225], v6 offset:8192\n"
    "v_accvgpr_write_b32 a42, 0\n"
    "v_mfma_f32_16x16x32_bf16 v[32:35], a[244:247], a[208:211], v[32:35]\n"
    "buffer_load_dwordx4 a[168:171], v0, s[40:43], s73 offen offset:0 sc1\n"
    "v_mfma_f32_16x16x32_bf16 v[36:39], a[240:243], a[196:199], v[36:39]\n"
    "ds_read_b64_tr_b16 a[226:227], v6 offset:10240\n"
    "v_accvgpr_write_b32 a43, 0\n"
    "v_mfma_f32_16x16x32_bf16 v[36:39], a[244:247], a[212:215], v[36:39]\n"
    "buffer_load_dwordx4 a[172:175], v0, s[40:43], s74 offen offset:0 sc1\n"
    "v_mfma_f32_16x16x32_bf16 v[40:43], a[240:243], a[200:203], v[40:43]\n"
    "ds_read_b64_tr_b16 a[228:229], v6 offset:12288\n"
    "v_accvgpr_write_b32 a44, 0\n"
    "v_mfma_f32_16x16x32_bf16 v[40:43], a[244:247], a[216:219], v[40:43]\n"
    "v_accvgpr_write_b32 a45, 0\n"
    "v_accvgpr_write_b32 a46, 0\n"
    "v_mfma_f32_16x16x32_bf16 v[44:47], a[240:243], a[204:207], v[44:47]\n"
    "ds_read_b64_tr_b16 a[230:231], v6 offset:14336\n"
    "v_accvgpr_write_b32 a47, 0\n"
    "v_mfma_f32_16x16x32_bf16 v[44:47], a[244:247], a[220:223], v[44:47]\n"
    "s_waitcnt lgkmcnt(4)\n"
    "v_mfma_f32_16x16x32_bf16 v[48:51], a[248:251], a[192:195], v[48:51]\n"
    "ds_read_b64_tr_b16 a[232:233], v7 offset:8192\n"
    "v_mfma_f32_16x16x32_bf16 v[48:51], a[252:255], a[208:211], v[48:51]\n"
    "buffer_load_dwordx4 a[176:179], v0, s[40:43], s71 offen offset:1024 sc1\n"
    "v_mfma_f32_16x16x32_bf16 v[52:55], a[248:251], a[196:199], v[52:55]\n"
    "ds_read_b64_tr_b16 a[234:235], v7 offset:10240\n"
    "v_mfma_f32_16x16x32_bf16 v[52:55], a[252:255], a[212:215], v[52:55]\n"
    "v_mfma_f32_16x16x32_bf16 v[56:59], a[248:251], a[200:203], v[56:59]\n"
    "ds_read_b64_tr_b16 a[236:237], v7 offset:12288\n"
    "v_mfma_f32_16x16x32_bf16 v[56:59], a[252:255], a[216:219], v[56:59]\n"
    "buffer_load_dwordx4 a[180:183], v0, s[40:43], s72 offen offset:1024 sc1\n"
    "v_mfma_f32_16x16x32_bf16 v[60:63], a[248:251], a[204:207], v[60:63]\n"
    "ds_read_b64_tr_b16 a[238:239], v7 offset:14336\n"
    "v_mfma_f32_16x16x32_bf16 v[60:63], a[252:255], a[220:223], v[60:63]\n"
    "s_waitcnt lgkmcnt(4)\n"
    "v_mfma_f32_16x16x32_bf16 v[64:67], a[224:227], a[192:195], v[64:67]\n"
    "ds_read_b64_tr_b16 a[240:241], v8 offset:8192\n"
    "v_mfma_f32_16x16x32_bf16 v[64:67], a[228:231], a[208:211], v[64:67]\n"
    "v_mfma_f32_16x16x32_bf16 v[68:71], a[224:227], a[196:199], v[68:71]\n"
    "ds_read_b64_tr_b16 a[242:243], v8 offset:10240\n"
    "v_mfma_f32_16x16x32_bf16 v[68:71], a[228:231], a[212:215], v[68:71]\n"
    "buffer_load_dwordx4 a[184:187], v0, s[40:43], s73 offen offset:1024 sc1\n"
    "v_mfma_f32_16x16x32_bf16 v[72:75], a[224:227], a[200:203], v[72:75]\n"
    "ds_read_b64_tr_b16 a[244:245], v8 offset:12288\n"
    "v_mfma_f32_16x16x32_bf16 v[72:75], a[228:231], a[216:219], v[72:75]\n"
    "v_mfma_f32_16x16x32_bf16 v[76:79], a[224:227], a[204:207], v[76:79]\n"
    "ds_read_b64_tr_b16 a[246:247], v8 offset:14336\n"
    "v_mfma_f32_16x16x32_bf16 v[76:79], a[228:231], a[220:223], v[76:79]\n"
    "s_waitcnt lgkmcnt(4)\n"
    "v_mfma_f32_16x16x32_bf16 v[80:83], a[232:235], a[192:195], v[80:83]\n"
    "ds_read_b64_tr_b16 a[248:249], v9 offset:8192\n"
    "v_mfma_f32_16x16x32_bf16 v[80:83], a[236:239], a[208:211], v[80:83]\n"
    "buffer_load_dwordx4 a[188:191], v0, s[40:43], s74 offen offset:1024 sc1\n"
    "v_mfma_f32_16x16x32_bf16 v[84:87], a[232:235], a[196:199], v[84:87]\n"
    "ds_read_b64_tr_b16 a[250:251], v9 offset:10240\n"
    "v_mfma_f32_16x16x32_bf16 v[84:87], a[236:239], a[212:215], v[84:87]\n"
    "v_mfma_f32_16x16x32_bf16 v[88:91], a[232:235], a[200:203], v[88:91]\n"
    "ds_read_b64_tr_b16 a[252:253], v9 offset:12288\n"
    "v_mfma_f32_16x16x32_bf16 v[88:91], a[236:239], a[216:219], v[88:91]\n"
    "buffer_store_dwordx4 v[180:183], v0, s[40:43], s67 offen offset:1024 sc1\n"
    "v_mfma_f32_16x16x32_bf16 v[92:95], a[232:235], a[204:207], v[92:95]\n"
    "ds_read_b64_tr_b16 a[254:255], v9 offset:14336\n"
    "v_mfma_f32_16x16x32_bf16 v[92:95], a[236:239], a[220:223], v[92:95]\n"
    "s_waitcnt lgkmcnt(4)\n"
    "v_mfma_f32_16x16x32_bf16 v[96:99], a[240:243], a[192:195], v[96:99]\n"
    "ds_read_b64_tr_b16 a[224:225], v6 offset:16384\n"
    "v_mfma_f32_16x16x32_bf16 v[96:99], a[244:247], a[208:211], v[96:99]\n"
    "buffer_store_dwordx4 v[184:187], v0, s[40:43], s67 offen offset:2048 sc1\n"
    "v_mfma_f32_16x16x32_bf16 v[100:103], a[240:243], a[196:199], v[100:103]\n"
    "ds_read_b64_tr_b16 a[226:227], v6 offset:18432\n"
    "v_mfma_f32_16x16x32_bf16 v[100:103], a[244:247], a[212:215], v[100:103]\n"
    "v_mfma_f32_16x16x32_bf16 v[104:107], a[240:243], a[200:203], v[104:107]\n"
    "ds_read_b64_tr_b16 a[228:229], v6 offset:20480\n"
    "v_mfma_f32_16x16x32_bf16 v[104:107], a[244:247], a[216:219], v[104:107]\n"
    "v_mfma_f32_16x16x32_bf16 v[108:111], a[240:243], a[204:207], v[108:111]\n"
    "ds_read_b64_tr_b16 a[230:231], v6 offset:22528\n"
    "v_mfma_f32_16x16x32_bf16 v[108:111], a[244:247], a[220:223], v[108:111]\n"
    "s_waitcnt lgkmcnt(4)\n"
    "v_mfma_f32_16x16x32_bf16 v[112:115], a[248:251], a[192:195], v[112:115]\n"
    "ds_read_b64_tr_b16 a[232:233], v7 offset:16384\n"
    "v_mfma_f32_16x16x32_bf16 v[112:115], a[252:255], a[208:211], v[112:115]\n"
    "buffer_store_dwordx4 v[188:191], v0, s[40:43], s67 offen offset:3072 sc1\n"
    "v_mfma_f32_16x16x32_bf16 v[116:119], a[248:251], a[196:199], v[116:119]\n"
    "ds_read_b64_tr_b16 a[234:235], v7 offset:18432\n"
    "v_mfma_f32_16x16x32_bf16 v[116:119], a[252:255], a[212:215], v[116:119]\n"
    "v_mfma_f32_16x16x32_bf16 v[120:123], a[248:251], a[200:203], v[120:123]\n"
    "ds_read_b64_tr_b16 a[236:237], v7 offset:20480\n"
    "v_mfma_f32_16x16x32_bf16 v[120:123], a[252:255], a[216:219], v[120:123]\n"
    "v_mfma_f32_16x16x32_bf16 v[124:127], a[248:251], a[204:207], v[124:127]\n"
    "ds_read_b64_tr_b16 a[238:239], v7 offset:22528\n"
    "v_mfma_f32_16x16x32_bf16 v[124:127], a[252:255], a[220:223], v[124:127]\n"
    "s_waitcnt lgkmcnt(4)\n"
    "v_mfma_f32_16x16x32_bf16 v[128:131], a[224:227], a[192:195], v[128:131]\n"
    "ds_read_b64_tr_b16 a[240:241], v8 offset:16384\n"
    "v_mfma_f32_16x16x32_bf16 v[128:131], a[228:231], a[208:211], v[128:131]\n"
    "v_mfma_f32_16x16x32_bf16 v[132:135], a[224:227], a[196:199], v[132:135]\n"
    "s_addk_i32 s67, 0x3000\n"
    "ds_read_b64_tr_b16 a[242:243], v8 offset:18432\n"
    "v_mfma_f32_16x16x32_bf16 v[132:135], a[228:231], a[212:215], v[132:135]\n"
    "v_mfma_f32_16x16x32_bf16 v[136:139], a[224:227], a[200:203], v[136:139]\n"
    "ds_read_b64_tr_b16 a[244:245], v8 offset:20480\n"
    "v_mfma_f32_16x16x32_bf16 v[136:139], a[228:231], a[216:219], v[136:139]\n"
    "buffer_store_dwordx4 v[192:195], v0, s[40:43], s67 offen offset:0 sc1\n"
    "v_mfma_f32_16x16x32_bf16 v[140:143], a[224:227], a[204:207], v[140:143]\n"
    "s_add_u32 m0, s82, 0x8000\n"
    "ds_read_b64_tr_b16 a[246:247], v8 offset:22528\n"
    "v_mfma_f32_16x16x32_bf16 v[140:143], a[228:231], a[220:223], v[140:143]\n"
    "s_waitcnt lgkmcnt(4)\n"
    "v_mfma_f32_16x16x32_bf16 v[144:147], a[232:235], a[192:195], v[144:147]\n"
    "ds_read_b64_tr_b16 a[248:249], v9 offset:16384\n"
    "v_mfma_f32_16x16x32_bf16 v[144:147], a[236:239], a[208:211], v[144:147]\n"
    "buffer_load_dwordx4 v3, s[52:55], s81 offen offset:0 lds\n"
    "v_mfma_f32_16x16x32_bf16 v[148:151], a[232:235], a[196:199], v[148:151]\n"
    "s_add_u32 s81, s81, s68\n"
    "ds_read_b64_tr_b16 a[250:251], v9 offset:18432\n"
    "v_mfma_f32_16x16x32_bf16 v[148:151], a[236:239], a[212:215], v[148:151]\n"
    "v_mfma_f32_16x16x32_bf16 v[152:155], a[232:235], a[200:203], v[152:155]\n"
    "ds_read_b64_tr_b16 a[252:253], v9 offset:20480\n"
    "v_mfma_f32_16x16x32_bf16 v[152:155], a[236:239], a[216:219], v[152:155]\n"
    "v_mfma_f32_16x16x32_bf16 v[156:159], a[232:235], a[204:207], v[156:159]\n"
    "ds_read_b64_tr_b16 a[254:255], v9 offset:22528\n"
    "v_mfma_f32_16x16x32_bf16 v[156:159], a[236:239], a[220:223], v[156:159]\n"
    "s_waitcnt lgkmcnt(4)\n"
    "v_mfma_f32_16x16x32_bf16 v[160:163], a[240:243], a[192:195], v[160:163]\n"
    "ds_read_b64_tr_b16 a[224:225], v6 offset:24576\n"
    "v_mfma_f32_16x16x32_bf16 v[160:163], a[244:247], a[208:211], v[160:163]\n"
    "buffer_load_dwordx4 v3, s[52:55], s81 offen offset:1024 lds\n"
    "v_mfma_f32_16x16x32_bf16 v[164:167], a[240:243], a[196:199], v[164:167]\n"
    "s_add_u32 s81, s81, s69\n"
    "ds_read_b64_tr_b16 a[226:227], v6 offset:26624\n"
    "v_mfma_f32_16x16x32_bf16 v[164:167], a[244:247], a[212:215], v[164:167]\n"
    "v_mfma_f32_16x16x32_bf16 v[168:171], a[240:243], a[200:203], v[168:171]\n"
    "ds_read_b64_tr_b16 a[228:229], v6 offset:28672\n"
    "v_mfma_f32_16x16x32_bf16 v[168:171], a[244:247], a[216:219], v[168:171]\n"
    "buffer_load_dwordx4 v3, s[52:55], s81 offen offset:2048 lds\n"
    "v_mfma_f32_16x16x32_bf16 v[172:175], a[240:243], a[204:207], v[172:175]\n"
    "s_add_u32 s81, s81, s68\n"
    "ds_read_b64_tr_b16 a[230:231], v6 offset:30720\n"
    "v_mfma_f32_16x16x32_bf16 v[172:175], a[244:247], a[220:223], v[172:175]\n"
    "s_waitcnt lgkmcnt(4)\n"
    "v_add_u32 v6, 0x8000, v6\n"
    "v_mfma_f32_16x16x32_bf16 a[48:51], a[248:251], a[192:195], a[48:51]\n"
    "ds_read_b64_tr_b16 a[232:233], v7 offset:24576\n"
    "v_mfma_f32_16x16x32_bf16 a[48:51], a[252:255], a[208:211], a[48:51]\n"
    "v_mfma_f32_16x16x32_bf16 a[52:55], a[248:251], a[196:199], a[52:55]\n"
    "ds_read_b64_tr_b16 a[234:235], v7 offset:26624\n"
    "v_mfma_f32_16x16x32_bf16 a[52:55], a[252:255], a[212:215], a[52:55]\n"
    "buffer_load_dwordx4 v3, s[52:55], s81 offen offset:3072 lds\n"
    "v_mfma_f32_16x16x32_bf16 a[56:59], a[248:251], a[200:203], a[56:59]\n"
    "s_add_u32 s81, s81, s70\n"
    "ds_read_b64_tr_b16 a[236:237], v7 offset:28672\n"
    "v_mfma_f32_16x16x32_bf16 a[56:59], a[252:255], a[216:219], a[56:59]\n"
    "v_mfma_f32_16x16x32_bf16 a[60:63], a[248:251], a[204:207], a[60:63]\n"
    "ds_read_b64_tr_b16 a[238:239], v7 offset:30720\n"
    "v_mfma_f32_16x16x32_bf16 a[60:63], a[252:255], a[220:223], a[60:63]\n"
    "s_waitcnt lgkmcnt(4)\n"
    "v_add_u32 v7, 0x8000, v7\n"
    "v_mfma_f32_16x16x32_bf16 a[64:67], a[224:227], a[192:195], a[64:67]\n"
    "ds_read_b64_tr_b16 a[240:241], v8 offset:24576\n"
    "v_mfma_f32_16x16x32_bf16 a[64:67], a[228:231], a[208:211], a[64:67]\n"
    "buffer_store_dwordx4 v[196:199], v0, s[40:43], s67 offen offset:1024 sc1\n"
    "v_mfma_f32_16x16x32_bf16 a[68:71], a[224:227], a[196:199], a[68:71]\n"
    "s_addk_i32 m0, 0x1000\n"
    "ds_read_b64_tr_b16 a[242:243], v8 offset:26624\n"
    "v_mfma_f32_16x16x32_bf16 a[68:71], a[228:231], a[212:215], a[68:71]\n"
    "v_mfma_f32_16x16x32_bf16 a[72:75], a[224:227], a[200:203], a[72:75]\n"
    "ds_read_b64_tr_b16 a[244:245], v8 offset:28672\n"
    "v_mfma_f32_16x16x32_bf16 a[72:75], a[228:231], a[216:219], a[72:75]\n"
    "buffer_load_dwordx4 v3, s[52:55], s81 offen offset:0 lds\n"
    "v_mfma_f32_16x16x32_bf16 a[76:79], a[224:227], a[204:207], a[76:79]\n"
    "s_add_u32 s81, s81, s68\n"
    "ds_read_b64_tr_b16 a[246:247], v8 offset:30720\n"
    "v_mfma_f32_16x16x32_bf16 a[76:79], a[228:231], a[220:223], a[76:79]\n"
    "s_waitcnt lgkmcnt(4)\n"
    "v_add_u32 v8, 0x8000, v8\n"
    "v_mfma_f32_16x16x32_bf16 a[80:83], a[232:235], a[192:195], a[80:83]\n"
    "ds_read_b64_tr_b16 a[248:249], v9 offset:24576\n"
    "v_mfma_f32_16x16x32_bf16 a[80:83], a[236:239], a[208:211], a[80:83]\n"
    "v_mfma_f32_16x16x32_bf16 a[84:87], a[232:235], a[196:199], a[84:87]\n"
    "ds_read_b64_tr_b16 a[250:251], v9 offset:26624\n"
    "v_mfma_f32_16x16x32_bf16 a[84:87], a[236:239], a[212:215], a[84:87]\n"
    "buffer_load_dwordx4 v3, s[52:55], s81 offen offset:1024 lds\n"
    "v_mfma_f32_16x16x32_bf16 a[88:91], a[232:235], a[200:203], a[88:91]\n"
    "s_waitcnt vmcnt(39)\n"
    "ds_read_b64_tr_b16 a[252:253], v9 offset:28672\n"
    "v_mfma_f32_16x16x32_bf16 a[88:91], a[236:239], a[216:219], a[88:91]\n"
    "s_barrier\n"
    "v_mfma_f32_16x16x32_bf16 a[92:95], a[232:235], a[204:207], a[92:95]\n"
    "s_add_u32 s81, s81, s69\n"
    "ds_read_b64_tr_b16 a[254:255], v9 offset:30720\n"
    "v_mfma_f32_16x16x32_bf16 a[92:95], a[236:239], a[220:223], a[92:95]\n"
    "s_waitcnt lgkmcnt(4)\n"
    "v_add_u32 v9, 0x8000, v9\n"
    "v_mfma_f32_16x16x32_bf16 a[96:99], a[240:243], a[192:195], a[96:99]\n"
    "ds_read_b64_tr_b16 a[224:225], v6 offset:0\n"
    "v_mfma_f32_16x16x32_bf16 a[96:99], a[244:247], a[208:211], a[96:99]\n"
    "buffer_load_dwordx4 v3, s[52:55], s81 offen offset:2048 lds\n"
    "v_mfma_f32_16x16x32_bf16 a[100:103], a[240:243], a[196:199], a[100:103]\n"
    "s_add_u32 s81, s81, s68\n"
    "ds_read_b64_tr_b16 a[226:227], v6 offset:2048\n"
    "v_mfma_f32_16x16x32_bf16 a[100:103], a[244:247], a[212:215], a[100:103]\n"
    "v_mfma_f32_16x16x32_bf16 a[104:107], a[240:243], a[200:203], a[104:107]\n"
    "ds_read_b64_tr_b16 a[228:229], v6 offset:4096\n"
    "v_mfma_f32_16x16x32_bf16 a[104:107], a[244:247], a[216:219], a[104:107]\n"
    "v_mfma_f32_16x16x32_bf16 a[108:111], a[240:243], a[204:207], a[108:111]\n"
    "ds_read_b64_tr_b16 a[230:231], v6 offset:6144\n"
    "v_mfma_f32_16x16x32_bf16 a[108:111], a[244:247], a[220:223], a[108:111]\n"
    "s_waitcnt lgkmcnt(4)\n"
    "v_mfma_f32_16x16x32_bf16 a[112:115], a[248:251], a[192:195], a[112:115]\n"
    "ds_read_b64_tr_b16 a[232:233], v7 offset:0\n"
    "v_mfma_f32_16x16x32_bf16 a[112:115], a[252:255], a[208:211], a[112:115]\n"
    "buffer_load_dwordx4 v3, s[52:55], s81 offen offset:3072 lds\n"
    "v_mfma_f32_16x16x32_bf16 a[116:119], a[248:251], a[196:199], a[116:119]\n"
    "s_add_u32 s81, s81, s70\n"
    "ds_read_b64_tr_b16 a[234:235], v7 offset:2048\n"
    "v_mfma_f32_16x16x32_bf16 a[116:119], a[252:255], a[212:215], a[116:119]\n"
    "v_mfma_f32_16x16x32_bf16 a[120:123], a[248:251], a[200:203], a[120:123]\n"
    "ds_read_b64_tr_b16 a[236:237], v7 offset:4096\n"
    "v_accvgpr_read_b32 v176, a48\n"
    "v_mfma_f32_16x16x32_bf16 a[120:123], a[252:255], a[216:219], a[120:123]\n"
    "v_accvgpr_read_b32 v177, a49\n"
    "v_accvgpr_read_b32 v178, a50\n"
    "v_mfma_f32_16x16x32_bf16 a[124:127], a[248:251], a[204:207], a[124:127]\n"
    "s_waitcnt vmcnt(13)\n"
    "ds_read_b64_tr_b16 a[238:239], v7 offset:6144\n"
    "v_accvgpr_read_b32 v179, a51\n"
    "v_mfma_f32_16x16x32_bf16 a[124:127], a[252:255], a[220:223], a[124:127]\n"
    "s_waitcnt lgkmcnt(4)\n"
    "v_accvgpr_read_b32 v180, a52\n"
    "v_accvgpr_read_b32 v181, a53\n"
    "v_mfma_f32_16x16x32_bf16 a[128:131], a[224:227], a[160:163], a[128:131]\n"
    "ds_read_b64_tr_b16 a[240:241], v8 offset:0\n"
    "v_accvgpr_read_b32 v182, a54\n"
    "v_mfma_f32_16x16x32_bf16 a[128:131], a[228:231], a[176:179], a[128:131]\n"
    "v_accvgpr_read_b32 v183, a55\n"
    "v_accvgpr_read_b32 v184, a56\n"
    "v_mfma_f32_16x16x32_bf16 a[132:135], a[224:227], a[164:167], a[132:135]\n"
    "ds_read_b64_tr_b16 a[242:243], v8 offset:2048\n"
    "v_accvgpr_read_b32 v185, a57\n"
    "v_mfma_f32_16x16x32_bf16 a[132:135], a[228:231], a[180:183], a[132:135]\n"
    "v_accvgpr_read_b32 v186, a58\n"
    "v_accvgpr_read_b32 v187, a59\n"
    "v_mfma_f32_16x16x32_bf16 a[136:139], a[224:227], a[168:171], a[136:139]\n"
    "ds_read_b64_tr_b16 a[244:245], v8 offset:4096\n"
    "v_accvgpr_read_b32 v188, a60\n"
    "v_mfma_f32_16x16x32_bf16 a[136:139], a[228:231], a[184:187], a[136:139]\n"
    "buffer_load_dwordx4 a[192:195], v0, s[40:43], s71 offen offset:2048 sc1\n"
    "v_mfma_f32_16x16x32_bf16 a[140:143], a[224:227], a[172:175], a[140:143]\n"
    "ds_read_b64_tr_b16 a[246:247], v8 offset:6144\n"
    "v_accvgpr_read_b32 v189, a61\n"
    "v_mfma_f32_16x16x32_bf16 a[140:143], a[228:231], a[188:191], a[140:143]\n"
    "s_waitcnt lgkmcnt(4)\n"
    "v_accvgpr_read_b32 v190, a62\n"
    "v_accvgpr_read_b32 v191, a63\n"
    "v_mfma_f32_16x16x32_bf16 a[144:147], a[232:235], a[160:163], a[144:147]\n"
    "ds_read_b64_tr_b16 a[248:249], v9 offset:0\n"
    "v_accvgpr_write_b32 a48, 0\n"
    "v_mfma_f32_16x16x32_bf16 a[144:147], a[236:239], a[176:179], a[144:147]\n"
    "buffer_load_dwordx4 a[196:199], v0, s[40:43], s72 offen offset:2048 sc1\n"
    "v_mfma_f32_16x16x32_bf16 a[148:151], a[232:235], a[164:167], a[148:151]\n"
    "ds_read_b64_tr_b16 a[250:251], v9 offset:2048\n"
    "v_accvgpr_write_b32 a49, 0\n"
    "v_mfma_f32_16x16x32_bf16 a[148:151], a[236:239], a[180:183], a[148:151]\n"
    "v_accvgpr_write_b32 a50, 0\n"
    "v_accvgpr_write_b32 a51, 0\n"
    "v_mfma_f32_16x16x32_bf16 a[152:155], a[232:235], a[168:171], a[152:155]\n"
    "ds_read_b64_tr_b16 a[252:253], v9 offset:4096\n"
    "v_accvgpr_write_b32 a52, 0\n"
    "v_mfma_f32_16x16x32_bf16 a[152:155], a[236:239], a[184:187], a[152:155]\n"
    "v_accvgpr_write_b32 a53, 0\n"
    "v_accvgpr_write_b32 a54, 0\n"
    "v_mfma_f32_16x16x32_bf16 a[156:159], a[232:235], a[172:175], a[156:159]\n"
    "ds_read_b64_tr_b16 a[254:255], v9 offset:6144\n"
    "v_accvgpr_write_b32 a55, 0\n"
    "v_mfma_f32_16x16x32_bf16 a[156:159], a[236:239], a[188:191], a[156:159]\n"
    "s_waitcnt lgkmcnt(4)\n"
    "v_accvgpr_write_b32 a56, 0\n"
    "v_accvgpr_write_b32 a57, 0\n"
    "v_mfma_f32_16x16x32_bf16 v[32:35], a[240:243], a[160:163], v[32:35]\n"
    "ds_read_b64_tr_b16 a[224:225], v6 offset:8192\n"
    "v_accvgpr_write_b32 a58, 0\n"
    "v_mfma_f32_16x16x32_bf16 v[32:35], a[244:247], a[176:179], v[32:35]\n"
    "buffer_load_dwordx4 a[200:203], v0, s[40:43], s73 offen offset:2048 sc1\n"
    "v_mfma_f32_16x16x32_bf16 v[36:39], a[240:243], a[164:167], v[36:39]\n"
    "ds_read_b64_tr_b16 a[226:227], v6 offset:10240\n"
    "v_accvgpr_write_b32 a59, 0\n"
    "v_mfma_f32_16x16x32_bf16 v[36:39], a[244:247], a[180:183], v[36:39]\n"
    "v_accvgpr_write_b32 a60, 0\n"
    "v_accvgpr_write_b32 a61, 0\n"
    "v_mfma_f32_16x16x32_bf16 v[40:43], a[240:243], a[168:171], v[40:43]\n"
    "ds_read_b64_tr_b16 a[228:229], v6 offset:12288\n"
    "v_accvgpr_write_b32 a62, 0\n"
    "v_mfma_f32_16x16x32_bf16 v[40:43], a[244:247], a[184:187], v[40:43]\n"
    "buffer_load_dwordx4 a[204:207], v0, s[40:43], s74 offen offset:2048 sc1\n"
    "v_mfma_f32_16x16x32_bf16 v[44:47], a[240:243], a[172:175], v[44:47]\n"
    "ds_read_b64_tr_b16 a[230:231], v6 offset:14336\n"
    "v_accvgpr_write_b32 a63, 0\n"
    "v_mfma_f32_16x16x32_bf16 v[44:47], a[244:247], a[188:191], v[44:47]\n"
    "s_waitcnt lgkmcnt(4)\n"
    "v_mfma_f32_16x16x32_bf16 v[48:51], a[248:251], a[160:163], v[48:51]\n"
    "ds_read_b64_tr_b16 a[232:233], v7 offset:8192\n"
    "v_mfma_f32_16x16x32_bf16 v[48:51], a[252:255], a[176:179], v[48:51]\n"
    "v_mfma_f32_16x16x32_bf16 v[52:55], a[248:251], a[164:167], v[52:55]\n"
    "ds_read_b64_tr_b16 a[234:235], v7 offset:10240\n"
    "v_mfma_f32_16x16x32_bf16 v[52:55], a[252:255], a[180:183], v[52:55]\n"
    "buffer_load_dwordx4 a[208:211], v0, s[40:43], s71 offen offset:3072 sc1\n"
    "v_mfma_f32_16x16x32_bf16 v[56:59], a[248:251], a[168:171], v[56:59]\n"
    "s_addk_i32 s71, 0x1000\n"
    "ds_read_b64_tr_b16 a[236:237], v7 offset:12288\n"
    "v_mfma_f32_16x16x32_bf16 v[56:59], a[252:255], a[184:187], v[56:59]\n"
    "v_mfma_f32_16x16x32_bf16 v[60:63], a[248:251], a[172:175], v[60:63]\n"
    "ds_read_b64_tr_b16 a[238:239], v7 offset:14336\n"
    "v_mfma_f32_16x16x32_bf16 v[60:63], a[252:255], a[188:191], v[60:63]\n"
    "s_waitcnt lgkmcnt(4)\n"
    "v_mfma_f32_16x16x32_bf16 v[64:67], a[224:227], a[160:163], v[64:67]\n"
    "ds_read_b64_tr_b16 a[240:241], v8 offset:8192\n"
    "v_mfma_f32_16x16x32_bf16 v[64:67], a[228:231], a[176:179], v[64:67]\n"
    "buffer_load_dwordx4 a[212:215], v0, s[40:43], s72 offen offset:3072 sc1\n"
    "v_mfma_f32_16x16x32_bf16 v[68:71], a[224:227], a[164:167], v[68:71]\n"
    "s_addk_i32 s72, 0x1000\n"
    "ds_read_b64_tr_b16 a[242:243], v8 offset:10240\n"
    "v_mfma_f32_16x16x32_bf16 v[68:71], a[228:231], a[180:183], v[68:71]\n"
    "v_mfma_f32_16x16x32_bf16 v[72:75], a[224:227], a[168:171], v[72:75]\n"
    "ds_read_b64_tr_b16 a[244:245], v8 offset:12288\n"
    "v_mfma_f32_16x16x32_bf16 v[72:75], a[228:231], a[184:187], v[72:75]\n"
    "buffer_load_dwordx4 a[216:219], v0, s[40:43], s73 offen offset:3072 sc1\n"
    "v_mfma_f32_16x16x32_bf16 v[76:79], a[224:227], a[172:175], v[76:79]\n"
    "s_addk_i32 s73, 0x1000\n"
    "ds_read_b64_tr_b16 a[246:247], v8 offset:14336\n"
    "v_mfma_f32_16x16x32_bf16 v[76:79], a[228:231], a[188:191], v[76:79]\n"
    "s_waitcnt lgkmcnt(4)\n"
    "v_mfma_f32_16x16x32_bf16 v[80:83], a[232:235], a[160:163], v[80:83]\n"
    "ds_read_b64_tr_b16 a[248:249], v9 offset:8192\n"
    "v_mfma_f32_16x16x32_bf16 v[80:83], a[236:239], a[176:179], v[80:83]\n"
    "v_mfma_f32_16x16x32_bf16 v[84:87], a[232:235], a[164:167], v[84:87]\n"
    "ds_read_b64_tr_b16 a[250:251], v9 offset:10240\n"
    "v_mfma_f32_16x16x32_bf16 v[84:87], a[236:239], a[180:183], v[84:87]\n"
    "buffer_load_dwordx4 a[220:223], v0, s[40:43], s74 offen offset:3072 sc1\n"
    "v_mfma_f32_16x16x32_bf16 v[88:91], a[232:235], a[168:171], v[88:91]\n"
    "s_addk_i32 s74, 0x1000\n"
    "ds_read_b64_tr_b16 a[252:253], v9 offset:12288\n"
    "v_mfma_f32_16x16x32_bf16 v[88:91], a[236:239], a[184:187], v[88:91]\n"
    "v_mfma_f32_16x16x32_bf16 v[92:95], a[232:235], a[172:175], v[92:95]\n"
    "ds_read_b64_tr_b16 a[254:255], v9 offset:14336\n"
    "v_mfma_f32_16x16x32_bf16 v[92:95], a[236:239], a[188:191], v[92:95]\n"
    "s_waitcnt lgkmcnt(4)\n"
    "v_mfma_f32_16x16x32_bf16 v[96:99], a[240:243], a[160:163], v[96:99]\n"
    "ds_read_b64_tr_b16 a[224:225], v6 offset:16384\n"
    "v_mfma_f32_16x16x32_bf16 v[96:99], a[244:247], a[176:179], v[96:99]\n"
    "buffer_store_dwordx4 v[200:203], v0, s[40:43], s67 offen offset:2048 sc1\n"
    "v_mfma_f32_16x16x32_bf16 v[100:103], a[240:243], a[164:167], v[100:103]\n"
    "ds_read_b64_tr_b16 a[226:227], v6 offset:18432\n"
    "v_mfma_f32_16x16x32_bf16 v[100:103], a[244:247], a[180:183], v[100:103]\n"
    "v_mfma_f32_16x16x32_bf16 v[104:107], a[240:243], a[168:171], v[104:107]\n"
    "ds_read_b64_tr_b16 a[228:229], v6 offset:20480\n"
    "v_mfma_f32_16x16x32_bf16 v[104:107], a[244:247], a[184:187], v[104:107]\n"
    "v_mfma_f32_16x16x32_bf16 v[108:111], a[240:243], a[172:175], v[108:111]\n"
    "ds_read_b64_tr_b16 a[230:231], v6 offset:22528\n"
    "v_mfma_f32_16x16x32_bf16 v[108:111], a[244:247], a[188:191], v[108:111]\n"
    "s_waitcnt lgkmcnt(4)\n"
    "v_mfma_f32_16x16x32_bf16 v[112:115], a[248:251], a[160:163], v[112:115]\n"
    "ds_read_b64_tr_b16 a[232:233], v7 offset:16384\n"
    "v_mfma_f32_16x16x32_bf16 v[112:115], a[252:255], a[176:179], v[112:115]\n"
    "buffer_store_dwordx4 v[204:207], v0, s[40:43], s67 offen offset:3072 sc1\n"
    "v_mfma_f32_16x16x32_bf16 v[116:119], a[248:251], a[164:167], v[116:119]\n"
    "ds_read_b64_tr_b16 a[234:235], v7 offset:18432\n"
    "v_mfma_f32_16x16x32_bf16 v[116:119], a[252:255], a[180:183], v[116:119]\n"
    "v_mfma_f32_16x16x32_bf16 v[120:123], a[248:251], a[168:171], v[120:123]\n"
    "ds_read_b64_tr_b16 a[236:237], v7 offset:20480\n"
    "v_mfma_f32_16x16x32_bf16 v[120:123], a[252:255], a[184:187], v[120:123]\n"
    "v_mfma_f32_16x16x32_bf16 v[124:127], a[248:251], a[172:175], v[124:127]\n"
    "ds_read_b64_tr_b16 a[238:239], v7 offset:22528\n"
    "v_mfma_f32_16x16x32_bf16 v[124:127], a[252:255], a[188:191], v[124:127]\n"
    "s_waitcnt lgkmcnt(4)\n"
    "v_mfma_f32_16x16x32_bf16 v[128:131], a[224:227], a[160:163], v[128:131]\n"
    "s_and_b32 s59, s58, 1\n"
    "ds_read_b64_tr_b16 a[240:241], v8 offset:16384\n"
    "v_mfma_f32_16x16x32_bf16 v[128:131], a[228:231], a[176:179], v[128:131]\n"
    "s_lshl_b32 s59, s59, 12\n"
    "v_mfma_f32_16x16x32_bf16 v[132:135], a[224:227], a[164:167], v[132:135]\n"
    "s_addk_i32 s67, 0x1000\n"
    "ds_read_b64_tr_b16 a[242:243], v8 offset:18432\n"
    "v_mfma_f32_16x16x32_bf16 v[132:135], a[228:231], a[180:183], v[132:135]\n"
    "s_lshl_b32 s60, s58, 12\n"
    "v_mfma_f32_16x16x32_bf16 v[136:139], a[224:227], a[168:171], v[136:139]\n"
    "s_add_u32 s88, s91, s60\n"
    "ds_read_b64_tr_b16 a[244:245], v8 offset:20480\n"
    "v_mfma_f32_16x16x32_bf16 v[136:139], a[228:231], a[184:187], v[136:139]\n"
    "buffer_store_dwordx4 v[208:211], v0, s[40:43], s67 offen offset:0 sc1\n"
    "v_mfma_f32_16x16x32_bf16 v[140:143], a[224:227], a[172:175], v[140:143]\n"
    "s_add_u32 m0, s82, 0x10000\n"
    "ds_read_b64_tr_b16 a[246:247], v8 offset:22528\n"
    "v_mfma_f32_16x16x32_bf16 v[140:143], a[228:231], a[188:191], v[140:143]\n"
    "s_waitcnt lgkmcnt(4)\n"
    "v_mfma_f32_16x16x32_bf16 v[144:147], a[232:235], a[160:163], v[144:147]\n"
    "s_lshl_b32 s60, s57, 16\n"
    "ds_read_b64_tr_b16 a[248:249], v9 offset:16384\n"
    "v_mfma_f32_16x16x32_bf16 v[144:147], a[236:239], a[176:179], v[144:147]\n"
    "buffer_load_dwordx4 v3, s[52:55], s81 offen offset:0 lds\n"
    "v_mfma_f32_16x16x32_bf16 v[148:151], a[232:235], a[164:167], v[148:151]\n"
    "s_add_u32 s81, s81, s68\n"
    "ds_read_b64_tr_b16 a[250:251], v9 offset:18432\n"
    "v_mfma_f32_16x16x32_bf16 v[148:151], a[236:239], a[180:183], v[148:151]\n"
    "s_add_u32 s86, s88, s60\n"
    "v_mfma_f32_16x16x32_bf16 v[152:155], a[232:235], a[168:171], v[152:155]\n"
    "s_add_u32 s86, s86, 0x10000\n"
    "ds_read_b64_tr_b16 a[252:253], v9 offset:20480\n"
    "v_mfma_f32_16x16x32_bf16 v[152:155], a[236:239], a[184:187], v[152:155]\n"
    "s_add_u32 s87, s88, 0x100000\n"
    "v_mfma_f32_16x16x32_bf16 v[156:159], a[232:235], a[172:175], v[156:159]\n"
    "s_sub_u32 s87, s87, s60\n"
    "ds_read_b64_tr_b16 a[254:255], v9 offset:22528\n"
    "v_mfma_f32_16x16x32_bf16 v[156:159], a[236:239], a[188:191], v[156:159]\n"
    "s_waitcnt lgkmcnt(4)\n"
    "v_mfma_f32_16x16x32_bf16 v[160:163], a[240:243], a[160:163], v[160:163]\n"
    "s_sub_u32 s78, s87, 0x10000\n"
    "ds_read_b64_tr_b16 a[224:225], v6 offset:24576\n"
    "v_mfma_f32_16x16x32_bf16 v[160:163], a[244:247], a[176:179], v[160:163]\n"
    "buffer_load_dwordx4 v3, s[52:55], s81 offen offset:1024 lds\n"
    "v_mfma_f32_16x16x32_bf16 v[164:167], a[240:243], a[164:167], v[164:167]\n"
    "s_add_u32 s81, s81, s69\n"
    "ds_read_b64_tr_b16 a[226:227], v6 offset:26624\n"
    "v_mfma_f32_16x16x32_bf16 v[164:167], a[244:247], a[180:183], v[164:167]\n"
    "buffer_load_dwordx4 v3, s[52:55], s81 offen offset:2048 lds\n"
    "v_mfma_f32_16x16x32_bf16 v[168:171], a[240:243], a[168:171], v[168:171]\n"
    "s_add_u32 s81, s81, s68\n"
    "ds_read_b64_tr_b16 a[228:229], v6 offset:28672\n"
    "v_mfma_f32_16x16x32_bf16 v[168:171], a[244:247], a[184:187], v[168:171]\n"
    "s_add_u32 s60, s88, 0x70000\n"
    "v_mfma_f32_16x16x32_bf16 v[172:175], a[240:243], a[172:175], v[172:175]\n"
    "s_cmp_ge_u32 s57, 6\n"
    "ds_read_b64_tr_b16 a[230:231], v6 offset:30720\n"
    "v_mfma_f32_16x16x32_bf16 v[172:175], a[244:247], a[188:191], v[172:175]\n"
    "s_waitcnt lgkmcnt(4)\n"
    "v_add_u32 v6, 0xfffe0000, v6\n"
    "v_mfma_f32_16x16x32_bf16 v[176:179], a[248:251], a[160:163], v[176:179]\n"
    "s_cselect_b32 s26, 1, s60\n"
    "ds_read_b64_tr_b16 a[232:233], v7 offset:24576\n"
    "v_mfma_f32_16x16x32_bf16 v[176:179], a[252:255], a[176:179], v[176:179]\n"
    "buffer_store_dwordx4 v[212:215], v0, s[40:43], s67 offen offset:1024 sc1\n"
    "v_mfma_f32_16x16x32_bf16 v[180:183], a[248:251], a[164:167], v[180:183]\n"
    "s_cselect_b32 s27, s88, s86\n"
    "ds_read_b64_tr_b16 a[234:235], v7 offset:26624\n"
    "v_mfma_f32_16x16x32_bf16 v[180:183], a[252:255], a[180:183], v[180:183]\n"
    "v_mfma_f32_16x16x32_bf16 v[184:187], a[248:251], a[168:171], v[184:187]\n"
    "ds_read_b64_tr_b16 a[236:237], v7 offset:28672\n"
    "v_mfma_f32_16x16x32_bf16 v[184:187], a[252:255], a[184:187], v[184:187]\n"
    "buffer_load_dwordx4 v3, s[52:55], s81 offen offset:3072 lds\n"
    "v_mfma_f32_16x16x32_bf16 v[188:191], a[248:251], a[172:175], v[188:191]\n"
    "s_add_u32 s81, s81, s70\n"
    "ds_read_b64_tr_b16 a[238:239], v7 offset:30720\n"
    "v_mfma_f32_16x16x32_bf16 v[188:191], a[252:255], a[188:191], v[188:191]\n"
    "s_waitcnt lgkmcnt(4)\n"
    "v_add_u32 v7, 0xfffe0000, v7\n"
    "v_mfma_f32_16x16x32_bf16 a[64:67], a[224:227], a[160:163], a[64:67]\n"
    "s_addk_i32 m0, 0x1000\n"
    "ds_read_b64_tr_b16 a[240:241], v8 offset:24576\n"
    "v_mfma_f32_16x16x32_bf16 a[64:67], a[228:231], a[176:179], a[64:67]\n"
    "v_mfma_f32_16x16x32_bf16 a[68:71], a[224:227], a[164:167], a[68:71]\n"
    "ds_read_b64_tr_b16 a[242:243], v8 offset:26624\n"
    "v_mfma_f32_16x16x32_bf16 a[68:71], a[228:231], a[180:183], a[68:71]\n"
    "buffer_load_dwordx4 v3, s[52:55], s81 offen offset:0 lds\n"
    "v_mfma_f32_16x16x32_bf16 a[72:75], a[224:227], a[168:171], a[72:75]\n"
    "s_add_u32 s81, s81, s68\n"
    "ds_read_b64_tr_b16 a[244:245], v8 offset:28672\n"
    "v_mfma_f32_16x16x32_bf16 a[72:75], a[228:231], a[184:187], a[72:75]\n"
    "s_cmp_eq_u32 s57, 7\n"
    "v_mfma_f32_16x16x32_bf16 a[76:79], a[224:227], a[172:175], a[76:79]\n"
    "s_cselect_b32 s85, 1, s78\n"
    "ds_read_b64_tr_b16 a[246:247], v8 offset:30720\n"
    "v_mfma_f32_16x16x32_bf16 a[76:79], a[228:231], a[188:191], a[76:79]\n"
    "s_waitcnt lgkmcnt(4)\n"
    "v_add_u32 v8, 0xfffe0000, v8\n"
    "v_mfma_f32_16x16x32_bf16 a[80:83], a[232:235], a[160:163], a[80:83]\n"
    "s_cselect_b32 s60, s88, s60\n"
    "ds_read_b64_tr_b16 a[248:249], v9 offset:24576\n"
    "v_mfma_f32_16x16x32_bf16 a[80:83], a[236:239], a[176:179], a[80:83]\n"
    "buffer_load_dwordx4 v3, s[52:55], s81 offen offset:1024 lds\n"
    "v_mfma_f32_16x16x32_bf16 a[84:87], a[232:235], a[164:167], a[84:87]\n"
    "s_add_u32 s81, s81, s69\n"
    "ds_read_b64_tr_b16 a[250:251], v9 offset:26624\n"
    "v_mfma_f32_16x16x32_bf16 a[84:87], a[236:239], a[180:183], a[84:87]\n"
    "s_add_u32 s61, s60, 0x8000\n"
    "v_mfma_f32_16x16x32_bf16 a[88:91], a[232:235], a[168:171], a[88:91]\n"
    "s_waitcnt vmcnt(39)\n"
    "ds_read_b64_tr_b16 a[252:253], v9 offset:28672\n"
    "v_mfma_f32_16x16x32_bf16 a[88:91], a[236:239], a[184:187], a[88:91]\n"
    "s_barrier\n"
    "v_mfma_f32_16x16x32_bf16 a[92:95], a[232:235], a[172:175], a[92:95]\n"
    "s_add_u32 s64, s59, 0x2000\n"
    "ds_read_b64_tr_b16 a[254:255], v9 offset:30720\n"
    "v_mfma_f32_16x16x32_bf16 a[92:95], a[236:239], a[188:191], a[92:95]\n"
    "s_waitcnt lgkmcnt(4)\n"
    "v_add_u32 v9, 0xfffe0000, v9\n"
    "v_mfma_f32_16x16x32_bf16 a[96:99], a[240:243], a[160:163], a[96:99]\n"
    "ds_read_b64_tr_b16 a[224:225], v6 offset:0\n"
    "v_mfma_f32_16x16x32_bf16 a[96:99], a[244:247], a[176:179], a[96:99]\n"
    "buffer_store_dwordx4 v[216:219], v0, s[40:43], s67 offen offset:2048 sc1\n"
    "v_mfma_f32_16x16x32_bf16 a[100:103], a[240:243], a[164:167], a[100:103]\n"
    "ds_read_b64_tr_b16 a[226:227], v6 offset:2048\n"
    "v_mfma_f32_16x16x32_bf16 a[100:103], a[244:247], a[180:183], a[100:103]\n"
    "buffer_load_dwordx4 v3, s[52:55], s81 offen offset:2048 lds\n"
    "v_mfma_f32_16x16x32_bf16 a[104:107], a[240:243], a[168:171], a[104:107]\n"
    "s_add_u32 s81, s81, s68\n"
    "ds_read_b64_tr_b16 a[228:229], v6 offset:4096\n"
    "v_mfma_f32_16x16x32_bf16 a[104:107], a[244:247], a[184:187], a[104:107]\n"
    "v_mfma_f32_16x16x32_bf16 a[108:111], a[240:243], a[172:175], a[108:111]\n"
    "ds_read_b64_tr_b16 a[230:231], v6 offset:6144\n"
    "v_mfma_f32_16x16x32_bf16 a[108:111], a[244:247], a[188:191], a[108:111]\n"
    "s_waitcnt lgkmcnt(4)\n"
    "v_mfma_f32_16x16x32_bf16 a[112:115], a[248:251], a[160:163], a[112:115]\n"
    "ds_read_b64_tr_b16 a[232:233], v7 offset:0\n"
    "v_mfma_f32_16x16x32_bf16 a[112:115], a[252:255], a[176:179], a[112:115]\n"
    "buffer_load_dwordx4 v3, s[52:55], s81 offen offset:3072 lds\n"
    "v_mfma_f32_16x16x32_bf16 a[116:119], a[248:251], a[164:167], a[116:119]\n"
    "s_add_u32 s81, s81, s70\n"
    "ds_read_b64_tr_b16 a[234:235], v7 offset:2048\n"
    "v_mfma_f32_16x16x32_bf16 a[116:119], a[252:255], a[180:183], a[116:119]\n"
    "s_cmp_lt_u32 s58, 2\n"
    "v_mfma_f32_16x16x32_bf16 a[120:123], a[248:251], a[168:171], a[120:123]\n"
    "s_cselect_b32 s84, s60, 0xc0000000\n"
    "ds_read_b64_tr_b16 a[236:237], v7 offset:4096\n"
    "v_accvgpr_read_b32 v192, a64\n"
    "v_mfma_f32_16x16x32_bf16 a[120:123], a[252:255], a[184:187], a[120:123]\n"
    "s_cselect_b32 s80, s61, s60\n"
    "v_accvgpr_read_b32 v193, a65\n"
    "v_accvgpr_read_b32 v194, a66\n"
    "v_mfma_f32_16x16x32_bf16 a[124:127], a[248:251], a[172:175], a[124:127]\n"
    "s_waitcnt vmcnt(13)\n"
    "ds_read_b64_tr_b16 a[238:239], v7 offset:6144\n"
    "v_accvgpr_read_b32 v195, a67\n"
    "v_mfma_f32_16x16x32_bf16 a[124:127], a[252:255], a[188:191], a[124:127]\n"
    "s_waitcnt lgkmcnt(4)\n"
    "v_accvgpr_read_b32 v196, a68\n"
    "v_accvgpr_read_b32 v197, a69\n"
    "v_mfma_f32_16x16x32_bf16 a[128:131], a[224:227], a[192:195], a[128:131]\n"
    "s_cselect_b32 s64, s64, s59\n"
    "ds_read_b64_tr_b16 a[240:241], v8 offset:0\n"
    "v_mov_b32 v4, v0\n"
    "v_mfma_f32_16x16x32_bf16 a[128:131], a[228:231], a[208:211], a[128:131]\n"
    "s_mov_b32 s79, 0\n"
    "v_add_u32 v5, 0x8000, v0\n"
    "v_mov_b32 v2, 0xff800000\n"
    "v_mfma_f32_16x16x32_bf16 a[132:135], a[224:227], a[196:199], a[132:135]\n"
    "s_lshl_b32 s65, s77, 11\n"
    "ds_read_b64_tr_b16 a[242:243], v8 offset:2048\n"
    "v_accvgpr_read_b32 v198, a70\n"
    "v_mfma_f32_16x16x32_bf16 a[132:135], a[228:231], a[212:215], a[132:135]\n"
    "s_add_u32 s65, s65, 0x2300000\n"
    "v_accvgpr_read_b32 v199, a71\n"
    "v_accvgpr_read_b32 v200, a72\n"
    "v_mfma_f32_16x16x32_bf16 a[136:139], a[224:227], a[200:203], a[136:139]\n"
    "s_cmp_ge_u32 s77, 27\n"
    "ds_read_b64_tr_b16 a[244:245], v8 offset:4096\n"
    "v_accvgpr_read_b32 v201, a73\n"
    "v_mfma_f32_16x16x32_bf16 a[136:139], a[228:231], a[216:219], a[136:139]\n"
    "buffer_load_dwordx4 a[160:163], v0, s[40:43], s71 offen offset:0 sc1\n"
    "v_mfma_f32_16x16x32_bf16 a[140:143], a[224:227], a[204:207], a[140:143]\n"
    "s_cselect_b32 s65, 0xc0000000, s65\n"
    "ds_read_b64_tr_b16 a[246:247], v8 offset:6144\n"
    "v_accvgpr_read_b32 v202, a74\n"
    "v_mfma_f32_16x16x32_bf16 a[140:143], a[228:231], a[220:223], a[140:143]\n"
    "s_waitcnt lgkmcnt(4)\n"
    "v_accvgpr_read_b32 v203, a75\n"
    "v_accvgpr_read_b32 v204, a76\n"
    "v_mfma_f32_16x16x32_bf16 a[144:147], a[232:235], a[192:195], a[144:147]\n"
    "ds_read_b64_tr_b16 a[248:249], v9 offset:0\n"
    "v_accvgpr_read_b32 v205, a77\n"
    "v_mfma_f32_16x16x32_bf16 a[144:147], a[236:239], a[208:211], a[144:147]\n"
    "buffer_load_dwordx4 a[164:167], v0, s[40:43], s72 offen offset:0 sc1\n"
    "v_mfma_f32_16x16x32_bf16 a[148:151], a[232:235], a[196:199], a[148:151]\n"
    "ds_read_b64_tr_b16 a[250:251], v9 offset:2048\n"
    "v_accvgpr_read_b32 v206, a78\n"
    "v_mfma_f32_16x16x32_bf16 a[148:151], a[236:239], a[212:215], a[148:151]\n"
    "v_accvgpr_read_b32 v207, a79\n"
    "v_mfma_f32_16x16x32_bf16 a[152:155], a[232:235], a[200:203], a[152:155]\n"
    "ds_read_b64_tr_b16 a[252:253], v9 offset:4096\n"
    "v_accvgpr_write_b32 a64, 0\n"
    "v_mfma_f32_16x16x32_bf16 a[152:155], a[236:239], a[216:219], a[152:155]\n"
    "v_accvgpr_write_b32 a65, 0\n"
    "v_accvgpr_write_b32 a66, 0\n"
    "v_mfma_f32_16x16x32_bf16 a[156:159], a[232:235], a[204:207], a[156:159]\n"
    "ds_read_b64_tr_b16 a[254:255], v9 offset:6144\n"
    "v_accvgpr_write_b32 a67, 0\n"
    "v_mfma_f32_16x16x32_bf16 a[156:159], a[236:239], a[220:223], a[156:159]\n"
    "s_waitcnt lgkmcnt(4)\n"
    "v_accvgpr_write_b32 a68, 0\n"
    "v_accvgpr_write_b32 a69, 0\n"
    "v_mfma_f32_16x16x32_bf16 v[32:35], a[240:243], a[192:195], v[32:35]\n"
    "ds_read_b64_tr_b16 a[224:225], v6 offset:8192\n"
    "v_accvgpr_write_b32 a70, 0\n"
    "v_mfma_f32_16x16x32_bf16 v[32:35], a[244:247], a[208:211], v[32:35]\n"
    "buffer_load_dwordx4 a[168:171], v0, s[40:43], s73 offen offset:0 sc1\n"
    "v_mfma_f32_16x16x32_bf16 v[36:39], a[240:243], a[196:199], v[36:39]\n"
    "ds_read_b64_tr_b16 a[226:227], v6 offset:10240\n"
    "v_accvgpr_write_b32 a71, 0\n"
    "v_mfma_f32_16x16x32_bf16 v[36:39], a[244:247], a[212:215], v[36:39]\n"
    "v_accvgpr_write_b32 a72, 0\n"
    "v_accvgpr_write_b32 a73, 0\n"
    "v_mfma_f32_16x16x32_bf16 v[40:43], a[240:243], a[200:203], v[40:43]\n"
    "ds_read_b64_tr_b16 a[228:229], v6 offset:12288\n"
    "v_accvgpr_write_b32 a74, 0\n"
    "v_mfma_f32_16x16x32_bf16 v[40:43], a[244:247], a[216:219], v[40:43]\n"
    "buffer_load_dwordx4 a[172:175], v0, s[40:43], s74 offen offset:0 sc1\n"
    "v_mfma_f32_16x16x32_bf16 v[44:47], a[240:243], a[204:207], v[44:47]\n"
    "ds_read_b64_tr_b16 a[230:231], v6 offset:14336\n"
    "v_accvgpr_write_b32 a75, 0\n"
    "v_mfma_f32_16x16x32_bf16 v[44:47], a[244:247], a[220:223], v[44:47]\n"
    "s_waitcnt lgkmcnt(4)\n"
    "v_accvgpr_write_b32 a76, 0\n"
    "v_accvgpr_write_b32 a77, 0\n"
    "v_mfma_f32_16x16x32_bf16 v[48:51], a[248:251], a[192:195], v[48:51]\n"
    "ds_read_b64_tr_b16 a[232:233], v7 offset:8192\n"
    "v_accvgpr_write_b32 a78, 0\n"
    "v_mfma_f32_16x16x32_bf16 v[48:51], a[252:255], a[208:211], v[48:51]\n"
    "v_accvgpr_write_b32 a79, 0\n"
    "v_mfma_f32_16x16x32_bf16 v[52:55], a[248:251], a[196:199], v[52:55]\n"
    "ds_read_b64_tr_b16 a[234:235], v7 offset:10240\n"
    "v_mfma_f32_16x16x32_bf16 v[52:55], a[252:255], a[212:215], v[52:55]\n"
    "buffer_load_dwordx4 a[176:179], v0, s[40:43], s71 offen offset:1024 sc1\n"
    "v_mfma_f32_16x16x32_bf16 v[56:59], a[248:251], a[200:203], v[56:59]\n"
    "ds_read_b64_tr_b16 a[236:237], v7 offset:12288\n"
    "v_mfma_f32_16x16x32_bf16 v[56:59], a[252:255], a[216:219], v[56:59]\n"
    "v_mfma_f32_16x16x32_bf16 v[60:63], a[248:251], a[204:207], v[60:63]\n"
    "ds_read_b64_tr_b16 a[238:239], v7 offset:14336\n"
    "v_mfma_f32_16x16x32_bf16 v[60:63], a[252:255], a[220:223], v[60:63]\n"
    "s_waitcnt lgkmcnt(4)\n"
    "v_mfma_f32_16x16x32_bf16 v[64:67], a[224:227], a[192:195], v[64:67]\n"
    "ds_read_b64_tr_b16 a[240:241], v8 offset:8192\n"
    "v_mfma_f32_16x16x32_bf16 v[64:67], a[228:231], a[208:211], v[64:67]\n"
    "buffer_load_dwordx4 a[180:183], v0, s[40:43], s72 offen offset:1024 sc1\n"
    "v_mfma_f32_16x16x32_bf16 v[68:71], a[224:227], a[196:199], v[68:71]\n"
    "ds_read_b64_tr_b16 a[242:243], v8 offset:10240\n"
    "v_mfma_f32_16x16x32_bf16 v[68:71], a[228:231], a[212:215], v[68:71]\n"
    "v_mfma_f32_16x16x32_bf16 v[72:75], a[224:227], a[200:203], v[72:75]\n"
    "ds_read_b64_tr_b16 a[244:245], v8 offset:12288\n"
    "v_mfma_f32_16x16x32_bf16 v[72:75], a[228:231], a[216:219], v[72:75]\n"
    "buffer_load_dwordx4 a[184:187], v0, s[40:43], s73 offen offset:1024 sc1\n"
    "v_mfma_f32_16x16x32_bf16 v[76:79], a[224:227], a[204:207], v[76:79]\n"
    "ds_read_b64_tr_b16 a[246:247], v8 offset:14336\n"
    "v_mfma_f32_16x16x32_bf16 v[76:79], a[228:231], a[220:223], v[76:79]\n"
    "s_waitcnt lgkmcnt(4)\n"
    "v_mfma_f32_16x16x32_bf16 v[80:83], a[232:235], a[192:195], v[80:83]\n"
    "ds_read_b64_tr_b16 a[248:249], v9 offset:8192\n"
    "v_mfma_f32_16x16x32_bf16 v[80:83], a[236:239], a[208:211], v[80:83]\n"
    "v_mfma_f32_16x16x32_bf16 v[84:87], a[232:235], a[196:199], v[84:87]\n"
    "ds_read_b64_tr_b16 a[250:251], v9 offset:10240\n"
    "v_mfma_f32_16x16x32_bf16 v[84:87], a[236:239], a[212:215], v[84:87]\n"
    "buffer_load_dwordx4 a[188:191], v0, s[40:43], s74 offen offset:1024 sc1\n"
    "v_mfma_f32_16x16x32_bf16 v[88:91], a[232:235], a[200:203], v[88:91]\n"
    "ds_read_b64_tr_b16 a[252:253], v9 offset:12288\n"
    "v_mfma_f32_16x16x32_bf16 v[88:91], a[236:239], a[216:219], v[88:91]\n"
    "v_mfma_f32_16x16x32_bf16 v[92:95], a[232:235], a[204:207], v[92:95]\n"
    "ds_read_b64_tr_b16 a[254:255], v9 offset:14336\n"
    "v_mfma_f32_16x16x32_bf16 v[92:95], a[236:239], a[220:223], v[92:95]\n"
    "s_waitcnt lgkmcnt(4)\n"
    "v_mfma_f32_16x16x32_bf16 v[96:99], a[240:243], a[192:195], v[96:99]\n"
    "ds_read_b64_tr_b16 a[224:225], v6 offset:16384\n"
    "v_mfma_f32_16x16x32_bf16 v[96:99], a[244:247], a[208:211], v[96:99]\n"
    "buffer_store_dwordx4 v[220:223], v0, s[40:43], s67 offen offset:3072 sc1\n"
    "v_mfma_f32_16x16x32_bf16 v[100:103], a[240:243], a[196:199], v[100:103]\n"
    "s_addk_i32 s67, 0x3000\n"
    "ds_read_b64_tr_b16 a[226:227], v6 offset:18432\n"
    "v_mfma_f32_16x16x32_bf16 v[100:103], a[244:247], a[212:215], v[100:103]\n"
    "v_mfma_f32_16x16x32_bf16 v[104:107], a[240:243], a[200:203], v[104:107]\n"
    "ds_read_b64_tr_b16 a[228:229], v6 offset:20480\n"
    "v_mfma_f32_16x16x32_bf16 v[104:107], a[244:247], a[216:219], v[104:107]\n"
    "v_mfma_f32_16x16x32_bf16 v[108:111], a[240:243], a[204:207], v[108:111]\n"
    "ds_read_b64_tr_b16 a[230:231], v6 offset:22528\n"
    "v_mfma_f32_16x16x32_bf16 v[108:111], a[244:247], a[220:223], v[108:111]\n"
    "s_waitcnt lgkmcnt(4)\n"
    "v_mfma_f32_16x16x32_bf16 v[112:115], a[248:251], a[192:195], v[112:115]\n"
    "ds_read_b64_tr_b16 a[232:233], v7 offset:16384\n"
    "v_mfma_f32_16x16x32_bf16 v[112:115], a[252:255], a[208:211], v[112:115]\n"
    "buffer_store_dwordx4 v[224:227], v0, s[40:43], s67 offen offset:0 sc1\n"
    "v_mfma_f32_16x16x32_bf16 v[116:119], a[248:251], a[196:199], v[116:119]\n"
    "ds_read_b64_tr_b16 a[234:235], v7 offset:18432\n"
    "v_mfma_f32_16x16x32_bf16 v[116:119], a[252:255], a[212:215], v[116:119]\n"
    "v_mfma_f32_16x16x32_bf16 v[120:123], a[248:251], a[200:203], v[120:123]\n"
    "ds_read_b64_tr_b16 a[236:237], v7 offset:20480\n"
    "v_mfma_f32_16x16x32_bf16 v[120:123], a[252:255], a[216:219], v[120:123]\n"
    "v_mfma_f32_16x16x32_bf16 v[124:127], a[248:251], a[204:207], v[124:127]\n"
    "ds_read_b64_tr_b16 a[238:239], v7 offset:22528\n"
    "v_mfma_f32_16x16x32_bf16 v[124:127], a[252:255], a[220:223], v[124:127]\n"
    "s_waitcnt lgkmcnt(4)\n"
    "v_mfma_f32_16x16x32_bf16 v[128:131], a[224:227], a[192:195], v[128:131]\n"
    "ds_read_b64_tr_b16 a[240:241], v8 offset:16384\n"
    "v_mfma_f32_16x16x32_bf16 v[128:131], a[228:231], a[208:211], v[128:131]\n"
    "v_mfma_f32_16x16x32_bf16 v[132:135], a[224:227], a[196:199], v[132:135]\n"
    "ds_read_b64_tr_b16 a[242:243], v8 offset:18432\n"
    "v_mfma_f32_16x16x32_bf16 v[132:135], a[228:231], a[212:215], v[132:135]\n"
    "v_mfma_f32_16x16x32_bf16 v[136:139], a[224:227], a[200:203], v[136:139]\n"
    "ds_read_b64_tr_b16 a[244:245], v8 offset:20480\n"
    "v_mfma_f32_16x16x32_bf16 v[136:139], a[228:231], a[216:219], v[136:139]\n"
    "buffer_store_dwordx4 v[228:231], v0, s[40:43], s67 offen offset:1024 sc1\n"
    "v_mfma_f32_16x16x32_bf16 v[140:143], a[224:227], a[204:207], v[140:143]\n"
    "s_add_u32 m0, s82, 0x18000\n"
    "ds_read_b64_tr_b16 a[246:247], v8 offset:22528\n"
    "v_mfma_f32_16x16x32_bf16 v[140:143], a[228:231], a[220:223], v[140:143]\n"
    "s_waitcnt lgkmcnt(4)\n"
    "v_mfma_f32_16x16x32_bf16 v[144:147], a[232:235], a[192:195], v[144:147]\n"
    "ds_read_b64_tr_b16 a[248:249], v9 offset:16384\n"
    "v_mfma_f32_16x16x32_bf16 v[144:147], a[236:239], a[208:211], v[144:147]\n"
    "buffer_load_dwordx4 v3, s[52:55], s81 offen offset:0 lds\n"
    "v_mfma_f32_16x16x32_bf16 v[148:151], a[232:235], a[196:199], v[148:151]\n"
    "s_add_u32 s81, s81, s68\n"
    "ds_read_b64_tr_b16 a[250:251], v9 offset:18432\n"
    "v_mfma_f32_16x16x32_bf16 v[148:151], a[236:239], a[212:215], v[148:151]\n"
    "v_mfma_f32_16x16x32_bf16 v[152:155], a[232:235], a[200:203], v[152:155]\n"
    "ds_read_b64_tr_b16 a[252:253], v9 offset:20480\n"
    "v_mfma_f32_16x16x32_bf16 v[152:155], a[236:239], a[216:219], v[152:155]\n"
    "v_mfma_f32_16x16x32_bf16 v[156:159], a[232:235], a[204:207], v[156:159]\n"
    "ds_read_b64_tr_b16 a[254:255], v9 offset:22528\n"
    "v_mfma_f32_16x16x32_bf16 v[156:159], a[236:239], a[220:223], v[156:159]\n"
    "s_waitcnt lgkmcnt(4)\n"
    "v_mfma_f32_16x16x32_bf16 v[160:163], a[240:243], a[192:195], v[160:163]\n"
    "ds_read_b64_tr_b16 a[224:225], v6 offset:24576\n"
    "v_mfma_f32_16x16x32_bf16 v[160:163], a[244:247], a[208:211], v[160:163]\n"
    "buffer_load_dwordx4 v3, s[52:55], s81 offen offset:1024 lds\n"
    "v_mfma_f32_16x16x32_bf16 v[164:167], a[240:243], a[196:199], v[164:167]\n"
    "s_add_u32 s81, s81, s69\n"
    "ds_read_b64_tr_b16 a[226:227], v6 offset:26624\n"
    "v_mfma_f32_16x16x32_bf16 v[164:167], a[244:247], a[212:215], v[164:167]\n"
    "v_mfma_f32_16x16x32_bf16 v[168:171], a[240:243], a[200:203], v[168:171]\n"
    "ds_read_b64_tr_b16 a[228:229], v6 offset:28672\n"
    "v_mfma_f32_16x16x32_bf16 v[168:171], a[244:247], a[216:219], v[168:171]\n"
    "buffer_load_dwordx4 v3, s[52:55], s81 offen offset:2048 lds\n"
    "v_mfma_f32_16x16x32_bf16 v[172:175], a[240:243], a[204:207], v[172:175]\n"
    "s_add_u32 s81, s81, s68\n"
    "ds_read_b64_tr_b16 a[230:231], v6 offset:30720\n"
    "v_mfma_f32_16x16x32_bf16 v[172:175], a[244:247], a[220:223], v[172:175]\n"
    "s_waitcnt lgkmcnt(4)\n"
    "v_add_u32 v6, 0x8000, v6\n"
    "v_mfma_f32_16x16x32_bf16 v[176:179], a[248:251], a[192:195], v[176:179]\n"
    "ds_read_b64_tr_b16 a[232:233], v7 offset:24576\n"
    "v_mfma_f32_16x16x32_bf16 v[176:179], a[252:255], a[208:211], v[176:179]\n"
    "v_mfma_f32_16x16x32_bf16 v[180:183], a[248:251], a[196:199], v[180:183]\n"
    "ds_read_b64_tr_b16 a[234:235], v7 offset:26624\n"
    "v_mfma_f32_16x16x32_bf16 v[180:183], a[252:255], a[212:215], v[180:183]\n"
    "buffer_load_dwordx4 v3, s[52:55], s81 offen offset:3072 lds\n"
    "v_mfma_f32_16x16x32_bf16 v[184:187], a[248:251], a[200:203], v[184:187]\n"
    "s_add_u32 s81, s81, s70\n"
    "ds_read_b64_tr_b16 a[236:237], v7 offset:28672\n"
    "v_mfma_f32_16x16x32_bf16 v[184:187], a[252:255], a[216:219], v[184:187]\n"
    "v_mfma_f32_16x16x32_bf16 v[188:191], a[248:251], a[204:207], v[188:191]\n"
    "ds_read_b64_tr_b16 a[238:239], v7 offset:30720\n"
    "v_mfma_f32_16x16x32_bf16 v[188:191], a[252:255], a[220:223], v[188:191]\n"
    "s_waitcnt lgkmcnt(4)\n"
    "v_add_u32 v7, 0x8000, v7\n"
    "v_mfma_f32_16x16x32_bf16 v[192:195], a[224:227], a[192:195], v[192:195]\n"
    "ds_read_b64_tr_b16 a[240:241], v8 offset:24576\n"
    "v_mfma_f32_16x16x32_bf16 v[192:195], a[228:231], a[208:211], v[192:195]\n"
    "buffer_store_dwordx4 v[232:235], v0, s[40:43], s67 offen offset:2048 sc1\n"
    "v_mfma_f32_16x16x32_bf16 v[196:199], a[224:227], a[196:199], v[196:199]\n"
    "s_addk_i32 m0, 0x1000\n"
    "ds_read_b64_tr_b16 a[242:243], v8 offset:26624\n"
    "v_mfma_f32_16x16x32_bf16 v[196:199], a[228:231], a[212:215], v[196:199]\n"
    "v_mfma_f32_16x16x32_bf16 v[200:203], a[224:227], a[200:203], v[200:203]\n"
    "ds_read_b64_tr_b16 a[244:245], v8 offset:28672\n"
    "v_mfma_f32_16x16x32_bf16 v[200:203], a[228:231], a[216:219], v[200:203]\n"
    "buffer_load_dwordx4 v3, s[52:55], s81 offen offset:0 lds\n"
    "v_mfma_f32_16x16x32_bf16 v[204:207], a[224:227], a[204:207], v[204:207]\n"
    "s_add_u32 s81, s81, s68\n"
    "ds_read_b64_tr_b16 a[246:247], v8 offset:30720\n"
    "v_mfma_f32_16x16x32_bf16 v[204:207], a[228:231], a[220:223], v[204:207]\n"
    "s_waitcnt lgkmcnt(4)\n"
    "v_add_u32 v8, 0x8000, v8\n"
    "v_mfma_f32_16x16x32_bf16 a[80:83], a[232:235], a[192:195], a[80:83]\n"
    "ds_read_b64_tr_b16 a[248:249], v9 offset:24576\n"
    "v_mfma_f32_16x16x32_bf16 a[80:83], a[236:239], a[208:211], a[80:83]\n"
    "v_mfma_f32_16x16x32_bf16 a[84:87], a[232:235], a[196:199], a[84:87]\n"
    "ds_read_b64_tr_b16 a[250:251], v9 offset:26624\n"
    "v_mfma_f32_16x16x32_bf16 a[84:87], a[236:239], a[212:215], a[84:87]\n"
    "buffer_load_dwordx4 v3, s[52:55], s81 offen offset:1024 lds\n"
    "v_mfma_f32_16x16x32_bf16 a[88:91], a[232:235], a[200:203], a[88:91]\n"
    "s_waitcnt vmcnt(39)\n"
    "ds_read_b64_tr_b16 a[252:253], v9 offset:28672\n"
    "v_mfma_f32_16x16x32_bf16 a[88:91], a[236:239], a[216:219], a[88:91]\n"
    "s_barrier\n"
    "v_mfma_f32_16x16x32_bf16 a[92:95], a[232:235], a[204:207], a[92:95]\n"
    "s_add_u32 s81, s81, s69\n"
    "ds_read_b64_tr_b16 a[254:255], v9 offset:30720\n"
    "v_mfma_f32_16x16x32_bf16 a[92:95], a[236:239], a[220:223], a[92:95]\n"
    "s_waitcnt lgkmcnt(4)\n"
    "v_add_u32 v9, 0x8000, v9\n"
    "v_mfma_f32_16x16x32_bf16 a[96:99], a[240:243], a[192:195], a[96:99]\n"
    "ds_read_b64_tr_b16 a[224:225], v6 offset:0\n"
    "v_mfma_f32_16x16x32_bf16 a[96:99], a[244:247], a[208:211], a[96:99]\n"
    "buffer_load_dwordx4 v3, s[52:55], s81 offen offset:2048 lds\n"
    "v_mfma_f32_16x16x32_bf16 a[100:103], a[240:243], a[196:199], a[100:103]\n"
    "s_add_u32 s81, s81, s68\n"
    "ds_read_b64_tr_b16 a[226:227], v6 offset:2048\n"
    "v_mfma_f32_16x16x32_bf16 a[100:103], a[244:247], a[212:215], a[100:103]\n"
    "v_mfma_f32_16x16x32_bf16 a[104:107], a[240:243], a[200:203], a[104:107]\n"
    "ds_read_b64_tr_b16 a[228:229], v6 offset:4096\n"
    "v_mfma_f32_16x16x32_bf16 a[104:107], a[244:247], a[216:219], a[104:107]\n"
    "v_mfma_f32_16x16x32_bf16 a[108:111], a[240:243], a[204:207], a[108:111]\n"
    "ds_read_b64_tr_b16 a[230:231], v6 offset:6144\n"
    "v_mfma_f32_16x16x32_bf16 a[108:111], a[244:247], a[220:223], a[108:111]\n"
    "s_waitcnt lgkmcnt(4)\n"
    "v_mfma_f32_16x16x32_bf16 a[112:115], a[248:251], a[192:195], a[112:115]\n"
    "ds_read_b64_tr_b16 a[232:233], v7 offset:0\n"
    "v_mfma_f32_16x16x32_bf16 a[112:115], a[252:255], a[208:211], a[112:115]\n"
    "buffer_load_dwordx4 v3, s[52:55], s81 offen offset:3072 lds\n"
    "v_mfma_f32_16x16x32_bf16 a[116:119], a[248:251], a[196:199], a[116:119]\n"
    "s_add_u32 s81, s81, s70\n"
    "ds_read_b64_tr_b16 a[234:235], v7 offset:2048\n"
    "v_mfma_f32_16x16x32_bf16 a[116:119], a[252:255], a[212:215], a[116:119]\n"
    "v_mfma_f32_16x16x32_bf16 a[120:123], a[248:251], a[200:203], a[120:123]\n"
    "ds_read_b64_tr_b16 a[236:237], v7 offset:4096\n"
    "v_accvgpr_read_b32 v208, a80\n"
    "v_mfma_f32_16x16x32_bf16 a[120:123], a[252:255], a[216:219], a[120:123]\n"
    "v_accvgpr_read_b32 v209, a81\n"
    "v_accvgpr_read_b32 v210, a82\n"
    "v_mfma_f32_16x16x32_bf16 a[124:127], a[248:251], a[204:207], a[124:127]\n"
    "s_waitcnt vmcnt(12)\n"
    "ds_read_b64_tr_b16 a[238:239], v7 offset:6144\n"
    "v_accvgpr_read_b32 v211, a83\n"
    "v_mfma_f32_16x16x32_bf16 a[124:127], a[252:255], a[220:223], a[124:127]\n"
    "s_waitcnt lgkmcnt(4)\n"
    "v_accvgpr_read_b32 v212, a84\n"
    "v_accvgpr_read_b32 v213, a85\n"
    "v_mfma_f32_16x16x32_bf16 a[128:131], a[224:227], a[160:163], a[128:131]\n"
    "ds_read_b64_tr_b16 a[240:241], v8 offset:0\n"
    "v_accvgpr_read_b32 v214, a86\n"
    "v_mfma_f32_16x16x32_bf16 a[128:131], a[228:231], a[176:179], a[128:131]\n"
    "v_accvgpr_read_b32 v215, a87\n"
    "v_accvgpr_read_b32 v216, a88\n"
    "v_mfma_f32_16x16x32_bf16 a[132:135], a[224:227], a[164:167], a[132:135]\n"
    "ds_read_b64_tr_b16 a[242:243], v8 offset:2048\n"
    "v_accvgpr_read_b32 v217, a89\n"
    "v_mfma_f32_16x16x32_bf16 a[132:135], a[228:231], a[180:183], a[132:135]\n"
    "v_accvgpr_read_b32 v218, a90\n"
    "v_accvgpr_read_b32 v219, a91\n"
    "v_mfma_f32_16x16x32_bf16 a[136:139], a[224:227], a[168:171], a[136:139]\n"
    "ds_read_b64_tr_b16 a[244:245], v8 offset:4096\n"
    "v_accvgpr_read_b32 v220, a92\n"
    "v_mfma_f32_16x16x32_bf16 a[136:139], a[228:231], a[184:187], a[136:139]\n"
    "buffer_load_dwordx4 a[192:195], v0, s[40:43], s71 offen offset:2048 sc1\n"
    "v_mfma_f32_16x16x32_bf16 a[140:143], a[224:227], a[172:175], a[140:143]\n"
    "ds_read_b64_tr_b16 a[246:247], v8 offset:6144\n"
    "v_accvgpr_read_b32 v221, a93\n"
    "v_mfma_f32_16x16x32_bf16 a[140:143], a[228:231], a[188:191], a[140:143]\n"
    "s_waitcnt lgkmcnt(4)\n"
    "v_accvgpr_read_b32 v222, a94\n"
    "v_accvgpr_read_b32 v223, a95\n"
    "v_mfma_f32_16x16x32_bf16 a[144:147], a[232:235], a[160:163], a[144:147]\n"
    "ds_read_b64_tr_b16 a[248:249], v9 offset:0\n"
    "v_accvgpr_write_b32 a80, 0\n"
    "v_mfma_f32_16x16x32_bf16 a[144:147], a[236:239], a[176:179], a[144:147]\n"
    "buffer_load_dwordx4 a[196:199], v0, s[40:43], s72 offen offset:2048 sc1\n"
    "v_mfma_f32_16x16x32_bf16 a[148:151], a[232:235], a[164:167], a[148:151]\n"
    "ds_read_b64_tr_b16 a[250:251], v9 offset:2048\n"
    "v_accvgpr_write_b32 a81, 0\n"
    "v_mfma_f32_16x16x32_bf16 a[148:151], a[236:239], a[180:183], a[148:151]\n"
    "v_accvgpr_write_b32 a82, 0\n"
    "v_accvgpr_write_b32 a83, 0\n"
    "v_mfma_f32_16x16x32_bf16 a[152:155], a[232:235], a[168:171], a[152:155]\n"
    "ds_read_b64_tr_b16 a[252:253], v9 offset:4096\n"
    "v_accvgpr_write_b32 a84, 0\n"
    "v_mfma_f32_16x16x32_bf16 a[152:155], a[236:239], a[184:187], a[152:155]\n"
    "v_accvgpr_write_b32 a85, 0\n"
    "v_accvgpr_write_b32 a86, 0\n"
    "v_mfma_f32_16x16x32_bf16 a[156:159], a[232:235], a[172:175], a[156:159]\n"
    "ds_read_b64_tr_b16 a[254:255], v9 offset:6144\n"
    "v_accvgpr_write_b32 a87, 0\n"
    "v_mfma_f32_16x16x32_bf16 a[156:159], a[236:239], a[188:191], a[156:159]\n"
    "s_waitcnt lgkmcnt(4)\n"
    "v_accvgpr_write_b32 a88, 0\n"
    "v_accvgpr_write_b32 a89, 0\n"
    "v_mfma_f32_16x16x32_bf16 v[32:35], a[240:243], a[160:163], v[32:35]\n"
    "ds_read_b64_tr_b16 a[224:225], v6 offset:8192\n"
    "v_accvgpr_write_b32 a90, 0\n"
    "v_mfma_f32_16x16x32_bf16 v[32:35], a[244:247], a[176:179], v[32:35]\n"
    "buffer_load_dwordx4 a[200:203], v0, s[40:43], s73 offen offset:2048 sc1\n"
    "v_mfma_f32_16x16x32_bf16 v[36:39], a[240:243], a[164:167], v[36:39]\n"
    "ds_read_b64_tr_b16 a[226:227], v6 offset:10240\n"
    "v_accvgpr_write_b32 a91, 0\n"
    "v_mfma_f32_16x16x32_bf16 v[36:39], a[244:247], a[180:183], v[36:39]\n"
    "buffer_load_dwordx4 a[204:207], v0, s[40:43], s74 offen offset:2048 sc1\n"
    "v_mfma_f32_16x16x32_bf16 v[40:43], a[240:243], a[168:171], v[40:43]\n"
    "ds_read_b64_tr_b16 a[228:229], v6 offset:12288\n"
    "v_accvgpr_write_b32 a92, 0\n"
    "v_mfma_f32_16x16x32_bf16 v[40:43], a[244:247], a[184:187], v[40:43]\n"
    "v_accvgpr_write_b32 a93, 0\n"
    "v_accvgpr_write_b32 a94, 0\n"
    "v_mfma_f32_16x16x32_bf16 v[44:47], a[240:243], a[172:175], v[44:47]\n"
    "ds_read_b64_tr_b16 a[230:231], v6 offset:14336\n"
    "v_accvgpr_write_b32 a95, 0\n"
    "v_mfma_f32_16x16x32_bf16 v[44:47], a[244:247], a[188:191], v[44:47]\n"
    "s_waitcnt lgkmcnt(4)\n"
    "v_mfma_f32_16x16x32_bf16 v[48:51], a[248:251], a[160:163], v[48:51]\n"
    "ds_read_b64_tr_b16 a[232:233], v7 offset:8192\n"
    "v_mfma_f32_16x16x32_bf16 v[48:51], a[252:255], a[176:179], v[48:51]\n"
    "buffer_load_dwordx4 a[208:211], v0, s[40:43], s71 offen offset:3072 sc1\n"
    "v_mfma_f32_16x16x32_bf16 v[52:55], a[248:251], a[164:167], v[52:55]\n"
    "s_addk_i32 s71, 0x1000\n"
    "ds_read_b64_tr_b16 a[234:235], v7 offset:10240\n"
    "v_mfma_f32_16x16x32_bf16 v[52:55], a[252:255], a[180:183], v[52:55]\n"
    "v_mfma_f32_16x16x32_bf16 v[56:59], a[248:251], a[168:171], v[56:59]\n"
    "ds_read_b64_tr_b16 a[236:237], v7 offset:12288\n"
    "v_mfma_f32_16x16x32_bf16 v[56:59], a[252:255], a[184:187], v[56:59]\n"
    "buffer_load_dwordx4 a[212:215], v0, s[40:43], s72 offen offset:3072 sc1\n"
    "v_mfma_f32_16x16x32_bf16 v[60:63], a[248:251], a[172:175], v[60:63]\n"
    "s_addk_i32 s72, 0x1000\n"
    "ds_read_b64_tr_b16 a[238:239], v7 offset:14336\n"
    "v_mfma_f32_16x16x32_bf16 v[60:63], a[252:255], a[188:191], v[60:63]\n"
    "s_waitcnt lgkmcnt(4)\n"
    "v_mfma_f32_16x16x32_bf16 v[64:67], a[224:227], a[160:163], v[64:67]\n"
    "ds_read_b64_tr_b16 a[240:241], v8 offset:8192\n"
    "v_mfma_f32_16x16x32_bf16 v[64:67], a[228:231], a[176:179], v[64:67]\n"
    "v_mfma_f32_16x16x32_bf16 v[68:71], a[224:227], a[164:167], v[68:71]\n"
    "ds_read_b64_tr_b16 a[242:243], v8 offset:10240\n"
    "v_mfma_f32_16x16x32_bf16 v[68:71], a[228:231], a[180:183], v[68:71]\n"
    "buffer_load_dwordx4 a[216:219], v0, s[40:43], s73 offen offset:3072 sc1\n"
    "v_mfma_f32_16x16x32_bf16 v[72:75], a[224:227], a[168:171], v[72:75]\n"
    "s_addk_i32 s73, 0x1000\n"
    "ds_read_b64_tr_b16 a[244:245], v8 offset:12288\n"
    "v_mfma_f32_16x16x32_bf16 v[72:75], a[228:231], a[184:187], v[72:75]\n"
    "v_mfma_f32_16x16x32_bf16 v[76:79], a[224:227], a[172:175], v[76:79]\n"
    "ds_read_b64_tr_b16 a[246:247], v8 offset:14336\n"
    "v_mfma_f32_16x16x32_bf16 v[76:79], a[228:231], a[188:191], v[76:79]\n"
    "s_waitcnt lgkmcnt(4)\n"
    "v_mfma_f32_16x16x32_bf16 v[80:83], a[232:235], a[160:163], v[80:83]\n"
    "ds_read_b64_tr_b16 a[248:249], v9 offset:8192\n"
    "v_mfma_f32_16x16x32_bf16 v[80:83], a[236:239], a[176:179], v[80:83]\n"
    "buffer_load_dwordx4 a[220:223], v0, s[40:43], s74 offen offset:3072 sc1\n"
    "v_mfma_f32_16x16x32_bf16 v[84:87], a[232:235], a[164:167], v[84:87]\n"
    "s_addk_i32 s74, 0x1000\n"
    "ds_read_b64_tr_b16 a[250:251], v9 offset:10240\n"
    "v_mfma_f32_16x16x32_bf16 v[84:87], a[236:239], a[180:183], v[84:87]\n"
    "v_mfma_f32_16x16x32_bf16 v[88:91], a[232:235], a[168:171], v[88:91]\n"
    "ds_read_b64_tr_b16 a[252:253], v9 offset:12288\n"
    "v_mfma_f32_16x16x32_bf16 v[88:91], a[236:239], a[184:187], v[88:91]\n"
    "buffer_store_dwordx4 v[236:239], v0, s[40:43], s67 offen offset:3072 sc1\n"
    "v_mfma_f32_16x16x32_bf16 v[92:95], a[232:235], a[172:175], v[92:95]\n"
    "s_addk_i32 s67, 0x1000\n"
    "ds_read_b64_tr_b16 a[254:255], v9 offset:14336\n"
    "v_mfma_f32_16x16x32_bf16 v[92:95], a[236:239], a[188:191], v[92:95]\n"
    "s_waitcnt lgkmcnt(4)\n"
    "v_mfma_f32_16x16x32_bf16 v[96:99], a[240:243], a[160:163], v[96:99]\n"
    "ds_read_b64_tr_b16 a[224:225], v6 offset:16384\n"
    "v_mfma_f32_16x16x32_bf16 v[96:99], a[244:247], a[176:179], v[96:99]\n"
    "buffer_store_dwordx4 v[240:243], v0, s[40:43], s67 offen offset:0 sc1\n"
    "v_mfma_f32_16x16x32_bf16 v[100:103], a[240:243], a[164:167], v[100:103]\n"
    "ds_read_b64_tr_b16 a[226:227], v6 offset:18432\n"
    "v_mfma_f32_16x16x32_bf16 v[100:103], a[244:247], a[180:183], v[100:103]\n"
    "v_mfma_f32_16x16x32_bf16 v[104:107], a[240:243], a[168:171], v[104:107]\n"
    "ds_read_b64_tr_b16 a[228:229], v6 offset:20480\n"
    "v_mfma_f32_16x16x32_bf16 v[104:107], a[244:247], a[184:187], v[104:107]\n"
    "v_mfma_f32_16x16x32_bf16 v[108:111], a[240:243], a[172:175], v[108:111]\n"
    "ds_read_b64_tr_b16 a[230:231], v6 offset:22528\n"
    "v_mfma_f32_16x16x32_bf16 v[108:111], a[244:247], a[188:191], v[108:111]\n"
    "s_waitcnt lgkmcnt(4)\n"
    "v_mfma_f32_16x16x32_bf16 v[112:115], a[248:251], a[160:163], v[112:115]\n"
    "ds_read_b64_tr_b16 a[232:233], v7 offset:16384\n"
    "v_mfma_f32_16x16x32_bf16 v[112:115], a[252:255], a[176:179], v[112:115]\n"
    "buffer_store_dwordx4 v[244:247], v0, s[40:43], s67 offen offset:1024 sc1\n"
    "v_mfma_f32_16x16x32_bf16 v[116:119], a[248:251], a[164:167], v[116:119]\n"
    "ds_read_b64_tr_b16 a[234:235], v7 offset:18432\n"
    "v_mfma_f32_16x16x32_bf16 v[116:119], a[252:255], a[180:183], v[116:119]\n"
    "v_mfma_f32_16x16x32_bf16 v[120:123], a[248:251], a[168:171], v[120:123]\n"
    "ds_read_b64_tr_b16 a[236:237], v7 offset:20480\n"
    "v_mfma_f32_16x16x32_bf16 v[120:123], a[252:255], a[184:187], v[120:123]\n"
    "v_mfma_f32_16x16x32_bf16 v[124:127], a[248:251], a[172:175], v[124:127]\n"
    "ds_read_b64_tr_b16 a[238:239], v7 offset:22528\n"
    "s_getpc_b64 s[38:39]\n"
    "PCD_%=:\n"
    "s_lshr_b32 s59, %[bid], 3\n"
    "s_lshl2_add_u32 s59, s59, %[wid]\n"
    "s_mulk_i32 s59, 128\n"
    "v_lshrrev_b32 v27, 6, v12\n"
    "v_lshlrev_b32 v27, 7, v27\n"
    "s_add_u32 s59, s59, KVF_OK_%=-PCD_%=\n"
    "s_min_u32 s59, s59, MAXPASS_%=-PCD_%=-128\n"
    "s_add_u32 s38, s38, s59\n"
    "s_addc_u32 s39, s39, 0\n"
    "global_load_dword v27, v27, s[38:39]\n"
    "v_mfma_f32_16x16x32_bf16 v[124:127], a[252:255], a[188:191], v[124:127]\n"
    "s_waitcnt lgkmcnt(4)\n"
    "v_mfma_f32_16x16x32_bf16 v[128:131], a[224:227], a[160:163], v[128:131]\n"
    "ds_read_b64_tr_b16 a[240:241], v8 offset:16384\n"
    "v_mfma_f32_16x16x32_bf16 v[128:131], a[228:231], a[176:179], v[128:131]\n"
    "v_mfma_f32_16x16x32_bf16 v[132:135], a[224:227], a[164:167], v[132:135]\n"
    "ds_read_b64_tr_b16 a[242:243], v8 offset:18432\n"
    "v_mfma_f32_16x16x32_bf16 v[132:135], a[228:231], a[180:183], v[132:135]\n"
    "v_mfma_f32_16x16x32_bf16 v[136:139], a[224:227], a[168:171], v[136:139]\n"
    "ds_read_b64_tr_b16 a[244:245], v8 offset:20480\n"
    "v_mfma_f32_16x16x32_bf16 v[136:139], a[228:231], a[184:187], v[136:139]\n"
    "buffer_store_dwordx4 v[248:251], v0, s[40:43], s67 offen offset:2048 sc1\n"
    "v_mfma_f32_16x16x32_bf16 v[140:143], a[224:227], a[172:175], v[140:143]\n"
    "s_add_u32 m0, s82, 0x20000\n"
    "ds_read_b64_tr_b16 a[246:247], v8 offset:22528\n"
    "v_mfma_f32_16x16x32_bf16 v[140:143], a[228:231], a[188:191], v[140:143]\n"
    "s_waitcnt lgkmcnt(4)\n"
    "v_mfma_f32_16x16x32_bf16 v[144:147], a[232:235], a[160:163], v[144:147]\n"
    "ds_read_b64_tr_b16 a[248:249], v9 offset:16384\n"
    "v_mfma_f32_16x16x32_bf16 v[144:147], a[236:239], a[176:179], v[144:147]\n"
    "buffer_load_dwordx4 v3, s[52:55], s81 offen offset:0 lds\n"
    "v_mfma_f32_16x16x32_bf16 v[148:151], a[232:235], a[164:167], v[148:151]\n"
    "s_add_u32 s81, s81, s68\n"
    "ds_read_b64_tr_b16 a[250:251], v9 offset:18432\n"
    "v_mfma_f32_16x16x32_bf16 v[148:151], a[236:239], a[180:183], v[148:151]\n"
    "v_mfma_f32_16x16x32_bf16 v[152:155], a[232:235], a[168:171], v[152:155]\n"
    "ds_read_b64_tr_b16 a[252:253], v9 offset:20480\n"
    "v_mfma_f32_16x16x32_bf16 v[152:155], a[236:239], a[184:187], v[152:155]\n"
    "v_mfma_f32_16x16x32_bf16 v[156:159], a[232:235], a[172:175], v[156:159]\n"
    "ds_read_b64_tr_b16 a[254:255], v9 offset:22528\n"
    "v_mfma_f32_16x16x32_bf16 v[156:159], a[236:239], a[188:191], v[156:159]\n"
    "s_waitcnt lgkmcnt(4)\n"
    "v_mfma_f32_16x16x32_bf16 v[160:163], a[240:243], a[160:163], v[160:163]\n"
    "ds_read_b64_tr_b16 a[224:225], v6 offset:24576\n"
    "v_mfma_f32_16x16x32_bf16 v[160:163], a[244:247], a[176:179], v[160:163]\n"
    "buffer_load_dwordx4 v3, s[52:55], s81 offen offset:1024 lds\n"
    "v_mfma_f32_16x16x32_bf16 v[164:167], a[240:243], a[164:167], v[164:167]\n"
    "s_add_u32 s81, s81, s69\n"
    "ds_read_b64_tr_b16 a[226:227], v6 offset:26624\n"
    "v_mfma_f32_16x16x32_bf16 v[164:167], a[244:247], a[180:183], v[164:167]\n"
    "v_mfma_f32_16x16x32_bf16 v[168:171], a[240:243], a[168:171], v[168:171]\n"
    "ds_read_b64_tr_b16 a[228:229], v6 offset:28672\n"
    "v_mfma_f32_16x16x32_bf16 v[168:171], a[244:247], a[184:187], v[168:171]\n"
    "buffer_load_dwordx4 v3, s[52:55], s81 offen offset:2048 lds\n"
    "v_mfma_f32_16x16x32_bf16 v[172:175], a[240:243], a[172:175], v[172:175]\n"
    "s_add_u32 s81, s81, s68\n"
    "ds_read_b64_tr_b16 a[230:231], v6 offset:30720\n"
    "v_mfma_f32_16x16x32_bf16 v[172:175], a[244:247], a[188:191], v[172:175]\n"
    "s_waitcnt lgkmcnt(4)\n"
    "v_add_u32 v6, 0x8000, v6\n"
    "v_mfma_f32_16x16x32_bf16 v[176:179], a[248:251], a[160:163], v[176:179]\n"
    "ds_read_b64_tr_b16 a[232:233], v7 offset:24576\n"
    "v_mfma_f32_16x16x32_bf16 v[176:179], a[252:255], a[176:179], v[176:179]\n"
    "v_mfma_f32_16x16x32_bf16 v[180:183], a[248:251], a[164:167], v[180:183]\n"
    "ds_read_b64_tr_b16 a[234:235], v7 offset:26624\n"
    "v_mfma_f32_16x16x32_bf16 v[180:183], a[252:255], a[180:183], v[180:183]\n"
    "buffer_load_dwordx4 v3, s[52:55], s81 offen offset:3072 lds\n"
    "v_mfma_f32_16x16x32_bf16 v[184:187], a[248:251], a[168:171], v[184:187]\n"
    "s_add_u32 s81, s81, s70\n"
    "ds_read_b64_tr_b16 a[236:237], v7 offset:28672\n"
    "v_mfma_f32_16x16x32_bf16 v[184:187], a[252:255], a[184:187], v[184:187]\n"
    "v_mfma_f32_16x16x32_bf16 v[188:191], a[248:251], a[172:175], v[188:191]\n"
    "ds_read_b64_tr_b16 a[238:239], v7 offset:30720\n"
    "v_mfma_f32_16x16x32_bf16 v[188:191], a[252:255], a[188:191], v[188:191]\n"
    "s_waitcnt lgkmcnt(4)\n"
    "v_add_u32 v7, 0x8000, v7\n"
    "v_mfma_f32_16x16x32_bf16 v[192:195], a[224:227], a[160:163], v[192:195]\n"
    "ds_read_b64_tr_b16 a[240:241], v8 offset:24576\n"
    "v_mfma_f32_16x16x32_bf16 v[192:195], a[228:231], a[176:179], v[192:195]\n"
    "buffer_store_dwordx4 v[252:255], v0, s[40:43], s67 offen offset:3072 sc1\n"
    "v_mfma_f32_16x16x32_bf16 v[196:199], a[224:227], a[164:167], v[196:199]\n"
    "s_addk_i32 m0, 0x1000\n"
    "ds_read_b64_tr_b16 a[242:243], v8 offset:26624\n"
    "v_mfma_f32_16x16x32_bf16 v[196:199], a[228:231], a[180:183], v[196:199]\n"
    "v_mfma_f32_16x16x32_bf16 v[200:203], a[224:227], a[168:171], v[200:203]\n"
    "ds_read_b64_tr_b16 a[244:245], v8 offset:28672\n"
    "v_mfma_f32_16x16x32_bf16 v[200:203], a[228:231], a[184:187], v[200:203]\n"
    "buffer_load_dwordx4 v3, s[52:55], s81 offen offset:0 lds\n"
    "v_mfma_f32_16x16x32_bf16 v[204:207], a[224:227], a[172:175], v[204:207]\n"
    "s_add_u32 s81, s81, s68\n"
    "ds_read_b64_tr_b16 a[246:247], v8 offset:30720\n"
    "v_mfma_f32_16x16x32_bf16 v[204:207], a[228:231], a[188:191], v[204:207]\n"
    "s_waitcnt lgkmcnt(4)\n"
    "v_add_u32 v8, 0x8000, v8\n"
    "v_mfma_f32_16x16x32_bf16 v[208:211], a[232:235], a[160:163], v[208:211]\n"
    "ds_read_b64_tr_b16 a[248:249], v9 offset:24576\n"
    "v_mfma_f32_16x16x32_bf16 v[208:211], a[236:239], a[176:179], v[208:211]\n"
    "v_mfma_f32_16x16x32_bf16 v[212:215], a[232:235], a[164:167], v[212:215]\n"
    "ds_read_b64_tr_b16 a[250:251], v9 offset:26624\n"
    "v_mfma_f32_16x16x32_bf16 v[212:215], a[236:239], a[180:183], v[212:215]\n"
    "buffer_load_dwordx4 v3, s[52:55], s81 offen offset:1024 lds\n"
    "v_mfma_f32_16x16x32_bf16 v[216:219], a[232:235], a[168:171], v[216:219]\n"
    "s_waitcnt vmcnt(40)\n"
    "ds_read_b64_tr_b16 a[252:253], v9 offset:28672\n"
    "v_mfma_f32_16x16x32_bf16 v[216:219], a[236:239], a[184:187], v[216:219]\n"
    "s_barrier\n"
    "v_mfma_f32_16x16x32_bf16 v[220:223], a[232:235], a[172:175], v[220:223]\n"
    "s_add_u32 s81, s81, s69\n"
    "ds_read_b64_tr_b16 a[254:255], v9 offset:30720\n"
    "v_mfma_f32_16x16x32_bf16 v[220:223], a[236:239], a[188:191], v[220:223]\n"
    "s_waitcnt lgkmcnt(4)\n"
    "v_add_u32 v9, 0x8000, v9\n"
    "v_mfma_f32_16x16x32_bf16 a[96:99], a[240:243], a[160:163], a[96:99]\n"
    "ds_read_b64_tr_b16 a[224:225], v6 offset:0\n"
    "v_mfma_f32_16x16x32_bf16 a[96:99], a[244:247], a[176:179], a[96:99]\n"
    "buffer_load_dwordx4 v3, s[52:55], s81 offen offset:2048 lds\n"
    "v_mfma_f32_16x16x32_bf16 a[100:103], a[240:243], a[164:167], a[100:103]\n"
    "s_add_u32 s81, s81, s68\n"
    "ds_read_b64_tr_b16 a[226:227], v6 offset:2048\n"
    "v_mfma_f32_16x16x32_bf16 a[100:103], a[244:247], a[180:183], a[100:103]\n"
    "v_mfma_f32_16x16x32_bf16 a[104:107], a[240:243], a[168:171], a[104:107]\n"
    "ds_read_b64_tr_b16 a[228:229], v6 offset:4096\n"
    "v_mfma_f32_16x16x32_bf16 a[104:107], a[244:247], a[184:187], a[104:107]\n"
    "v_mfma_f32_16x16x32_bf16 a[108:111], a[240:243], a[172:175], a[108:111]\n"
    "ds_read_b64_tr_b16 a[230:231], v6 offset:6144\n"
    "v_mfma_f32_16x16x32_bf16 a[108:111], a[244:247], a[188:191], a[108:111]\n"
    "s_waitcnt lgkmcnt(4)\n"
    "v_mfma_f32_16x16x32_bf16 a[112:115], a[248:251], a[160:163], a[112:115]\n"
    "ds_read_b64_tr_b16 a[232:233], v7 offset:0\n"
    "v_mfma_f32_16x16x32_bf16 a[112:115], a[252:255], a[176:179], a[112:115]\n"
    "buffer_load_dwordx4 v3, s[52:55], s81 offen offset:3072 lds\n"
    "v_mfma_f32_16x16x32_bf16 a[116:119], a[248:251], a[164:167], a[116:119]\n"
    "s_add_u32 s81, s81, s70\n"
    "ds_read_b64_tr_b16 a[234:235], v7 offset:2048\n"
    "v_mfma_f32_16x16x32_bf16 a[116:119], a[252:255], a[180:183], a[116:119]\n"
    "s_and_b32 s59, %[bid], 1\n"
    "v_mfma_f32_16x16x32_bf16 a[120:123], a[248:251], a[168:171], a[120:123]\n"
    "s_lshl_b32 s59, s59, 4\n"
    "ds_read_b64_tr_b16 a[236:237], v7 offset:4096\n"
    "v_accvgpr_read_b32 v224, a96\n"
    "v_mfma_f32_16x16x32_bf16 a[120:123], a[252:255], a[184:187], a[120:123]\n"
    "s_add_u32 s59, s59, -16\n"
    "v_accvgpr_read_b32 v225, a97\n"
    "v_accvgpr_read_b32 v226, a98\n"
    "v_mfma_f32_16x16x32_bf16 a[124:127], a[248:251], a[172:175], a[124:127]\n"
    "s_waitcnt vmcnt(14)\n"
    "ds_read_b64_tr_b16 a[238:239], v7 offset:6144\n"
    "v_accvgpr_read_b32 v227, a99\n"
    "v_mfma_f32_16x16x32_bf16 a[124:127], a[252:255], a[188:191], a[124:127]\n"
    "s_waitcnt lgkmcnt(4)\n"
    "v_accvgpr_read_b32 v228, a100\n"
    "v_accvgpr_read_b32 v229, a101\n"
    "v_mfma_f32_16x16x32_bf16 a[128:131], a[224:227], a[192:195], a[128:131]\n"
    "s_lshr_b32 s60, %[bid], 3\n"
    "ds_read_b64_tr_b16 a[240:241], v8 offset:0\n"
    "v_accvgpr_read_b32 v230, a102\n"
    "v_mfma_f32_16x16x32_bf16 a[128:131], a[228:231], a[208:211], a[128:131]\n"
    "s_add_u32 s59, s59, s60\n"
    "v_accvgpr_read_b32 v231, a103\n"
    "v_accvgpr_read_b32 v232, a104\n"
    "v_mfma_f32_16x16x32_bf16 a[132:135], a[224:227], a[196:199], a[132:135]\n"
    "s_lshl2_add_u32 s59, s59, s58\n"
    "ds_read_b64_tr_b16 a[242:243], v8 offset:2048\n"
    "v_accvgpr_read_b32 v233, a105\n"
    "v_mfma_f32_16x16x32_bf16 a[132:135], a[228:231], a[212:215], a[132:135]\n"
    "s_mul_i32 s60, s59, 205\n"
    "v_accvgpr_read_b32 v234, a106\n"
    "v_accvgpr_read_b32 v235, a107\n"
    "v_mfma_f32_16x16x32_bf16 a[136:139], a[224:227], a[200:203], a[136:139]\n"
    "s_lshr_b32 s60, s60, 11\n"
    "ds_read_b64_tr_b16 a[244:245], v8 offset:4096\n"
    "v_accvgpr_read_b32 v236, a108\n"
    "v_mfma_f32_16x16x32_bf16 a[136:139], a[228:231], a[216:219], a[136:139]\n"
    "buffer_load_dwordx4 a[160:163], v0, s[40:43], s71 offen offset:0 sc1\n"
    "v_mfma_f32_16x16x32_bf16 a[140:143], a[224:227], a[204:207], a[140:143]\n"
    "s_mul_i32 s61, s60, 10\n"
    "ds_read_b64_tr_b16 a[246:247], v8 offset:6144\n"
    "v_accvgpr_read_b32 v237, a109\n"
    "v_mfma_f32_16x16x32_bf16 a[140:143], a[228:231], a[220:223], a[140:143]\n"
    "s_waitcnt lgkmcnt(4)\n"
    "v_accvgpr_read_b32 v238, a110\n"
    "v_accvgpr_read_b32 v239, a111\n"
    "v_mfma_f32_16x16x32_bf16 a[144:147], a[232:235], a[192:195], a[144:147]\n"
    "s_sub_u32 s54, s59, s61\n"
    "ds_read_b64_tr_b16 a[248:249], v9 offset:0\n"
    "v_accvgpr_read_b32 v240, a112\n"
    "v_mfma_f32_16x16x32_bf16 a[144:147], a[236:239], a[208:211], a[144:147]\n"
    "s_mul_i32 s61, s28, 23\n"
    "v_accvgpr_read_b32 v241, a113\n"
    "v_accvgpr_read_b32 v242, a114\n"
    "v_mfma_f32_16x16x32_bf16 a[148:151], a[232:235], a[196:199], a[148:151]\n"
    "s_add_u32 s61, s61, s60\n"
    "ds_read_b64_tr_b16 a[250:251], v9 offset:2048\n"
    "v_accvgpr_read_b32 v243, a115\n"
    "v_mfma_f32_16x16x32_bf16 a[148:151], a[236:239], a[212:215], a[148:151]\n"
    "buffer_load_dwordx4 a[164:167], v0, s[40:43], s72 offen offset:0 sc1\n"
    "v_mfma_f32_16x16x32_bf16 a[152:155], a[232:235], a[200:203], a[152:155]\n"
    "s_mul_i32 s53, s61, 81920\n"
    "ds_read_b64_tr_b16 a[252:253], v9 offset:4096\n"
    "v_accvgpr_read_b32 v244, a116\n"
    "v_mfma_f32_16x16x32_bf16 a[152:155], a[236:239], a[216:219], a[152:155]\n"
    "s_add_u32 s53, s53, 0x51c0000\n"
    "v_accvgpr_read_b32 v245, a117\n"
    "v_accvgpr_read_b32 v246, a118\n"
    "v_mfma_f32_16x16x32_bf16 a[156:159], a[232:235], a[204:207], a[156:159]\n"
    "s_mul_i32 s59, s28, 7\n"
    "ds_read_b64_tr_b16 a[254:255], v9 offset:6144\n"
    "v_accvgpr_read_b32 v247, a119\n"
    "v_mfma_f32_16x16x32_bf16 a[156:159], a[236:239], a[220:223], a[156:159]\n"
    "s_waitcnt lgkmcnt(4)\n"
    "v_accvgpr_read_b32 v248, a120\n"
    "v_accvgpr_read_b32 v249, a121\n"
    "v_mfma_f32_16x16x32_bf16 v[32:35], a[240:243], a[192:195], v[32:35]\n"
    "s_lshl_b32 s61, s28, 4\n"
    "ds_read_b64_tr_b16 a[224:225], v6 offset:8192\n"
    "v_accvgpr_read_b32 v250, a122\n"
    "v_mfma_f32_16x16x32_bf16 v[32:35], a[244:247], a[208:211], v[32:35]\n"
    "s_sub_u32 s61, s61, 7\n"
    "v_accvgpr_read_b32 v251, a123\n"
    "v_accvgpr_read_b32 v252, a124\n"
    "v_mfma_f32_16x16x32_bf16 v[36:39], a[240:243], a[196:199], v[36:39]\n"
    "s_cmp_lt_u32 s60, 7\n"
    "ds_read_b64_tr_b16 a[226:227], v6 offset:10240\n"
    "v_accvgpr_read_b32 v253, a125\n"
    "v_mfma_f32_16x16x32_bf16 v[36:39], a[244:247], a[212:215], v[36:39]\n"
    "buffer_load_dwordx4 a[168:171], v0, s[40:43], s73 offen offset:0 sc1\n"
    "v_mfma_f32_16x16x32_bf16 v[40:43], a[240:243], a[200:203], v[40:43]\n"
    "s_cselect_b64 s[48:49], %[wd], %[wo]\n"
    "ds_read_b64_tr_b16 a[228:229], v6 offset:12288\n"
    "v_accvgpr_read_b32 v254, a126\n"
    "v_mfma_f32_16x16x32_bf16 v[40:43], a[244:247], a[216:219], v[40:43]\n"
    "s_cselect_b32 s59, s59, s61\n"
    "v_accvgpr_read_b32 v255, a127\n"
    "v_accvgpr_write_b32 a96, 0\n"
    "v_mfma_f32_16x16x32_bf16 v[44:47], a[240:243], a[204:207], v[44:47]\n"
    "s_mov_b32 s61, 0x500000\n"
    "ds_read_b64_tr_b16 a[230:231], v6 offset:14336\n"
    "v_accvgpr_write_b32 a97, 0\n"
    "v_mfma_f32_16x16x32_bf16 v[44:47], a[244:247], a[220:223], v[44:47]\n"
    "s_waitcnt lgkmcnt(4)\n"
    "v_accvgpr_write_b32 a98, 0\n"
    "v_accvgpr_write_b32 a99, 0\n"
    "v_mfma_f32_16x16x32_bf16 v[48:51], a[248:251], a[192:195], v[48:51]\n"
    "s_cselect_b32 s50, 0x21c000, s61\n"
    "ds_read_b64_tr_b16 a[232:233], v7 offset:8192\n"
    "v_accvgpr_write_b32 a100, 0\n"
    "v_mfma_f32_16x16x32_bf16 v[48:51], a[252:255], a[208:211], v[48:51]\n"
    "buffer_load_dwordx4 a[172:175], v0, s[40:43], s74 offen offset:0 sc1\n"
    "v_mfma_f32_16x16x32_bf16 v[52:55], a[248:251], a[196:199], v[52:55]\n"
    "s_add_u32 s59, s59, s60\n"
    "ds_read_b64_tr_b16 a[234:235], v7 offset:10240\n"
    "v_accvgpr_write_b32 a101, 0\n"
    "v_mfma_f32_16x16x32_bf16 v[52:55], a[252:255], a[212:215], v[52:55]\n"
    "s_mul_i32 s52, s59, 81920\n"
    "v_accvgpr_write_b32 a102, 0\n"
    "v_accvgpr_write_b32 a103, 0\n"
    "v_mfma_f32_16x16x32_bf16 v[56:59], a[248:251], a[200:203], v[56:59]\n"
    "s_lshl_b32 s59, s54, 7\n"
    "ds_read_b64_tr_b16 a[236:237], v7 offset:12288\n"
    "v_accvgpr_write_b32 a104, 0\n"
    "v_mfma_f32_16x16x32_bf16 v[56:59], a[252:255], a[216:219], v[56:59]\n"
    "s_add_u32 s52, s52, s59\n"
    "v_accvgpr_write_b32 a105, 0\n"
    "v_accvgpr_write_b32 a106, 0\n"
    "v_mfma_f32_16x16x32_bf16 v[60:63], a[248:251], a[204:207], v[60:63]\n"
    "s_cmp_ge_u32 s60, 23\n"
    "ds_read_b64_tr_b16 a[238:239], v7 offset:14336\n"
    "v_accvgpr_write_b32 a107, 0\n"
    "v_mfma_f32_16x16x32_bf16 v[60:63], a[252:255], a[220:223], v[60:63]\n"
    "s_waitcnt lgkmcnt(4)\n"
    "v_accvgpr_write_b32 a108, 0\n"
    "v_accvgpr_write_b32 a109, 0\n"
    "v_mfma_f32_16x16x32_bf16 v[64:67], a[224:227], a[192:195], v[64:67]\n"
    "s_cselect_b32 s52, 0xc0000000, s52\n"
    "ds_read_b64_tr_b16 a[240:241], v8 offset:8192\n"
    "v_accvgpr_write_b32 a110, 0\n"
    "v_mfma_f32_16x16x32_bf16 v[64:67], a[228:231], a[208:211], v[64:67]\n"
    "buffer_load_dwordx4 a[176:179], v0, s[40:43], s71 offen offset:1024 sc1\n"
    "v_mfma_f32_16x16x32_bf16 v[68:71], a[224:227], a[196:199], v[68:71]\n"
    "s_cselect_b32 s53, 0xc0000000, s53\n"
    "ds_read_b64_tr_b16 a[242:243], v8 offset:10240\n"
    "v_accvgpr_write_b32 a111, 0\n"
    "v_mfma_f32_16x16x32_bf16 v[68:71], a[228:231], a[212:215], v[68:71]\n"
    "s_lshl_b32 s54, s54, 2\n"
    "v_accvgpr_write_b32 a112, 0\n"
    "v_accvgpr_write_b32 a113, 0\n"
    "v_mfma_f32_16x16x32_bf16 v[72:75], a[224:227], a[200:203], v[72:75]\n"
    "s_lshl_b32 s55, s58, 13\n"
    "ds_read_b64_tr_b16 a[244:245], v8 offset:12288\n"
    "v_accvgpr_write_b32 a114, 0\n"
    "v_mfma_f32_16x16x32_bf16 v[72:75], a[228:231], a[216:219], v[72:75]\n"
    "s_add_u32 s55, s55, 0x20000\n"
    "v_lshrrev_b32 v28, 3, v12\n"
    "v_and_b32 v29, 7, v12\n"
    "v_mfma_f32_16x16x32_bf16 v[76:79], a[224:227], a[204:207], v[76:79]\n"
    "ds_read_b64_tr_b16 a[246:247], v8 offset:14336\n"
    "v_and_b32 v30, 6, v28\n"
    "v_mfma_f32_16x16x32_bf16 v[76:79], a[228:231], a[220:223], v[76:79]\n"
    "s_waitcnt lgkmcnt(4)\n"
    "v_xor_b32 v29, v29, v30\n"
    "v_mul_u32_u24 v28, 0x500, v28\n"
    "v_mfma_f32_16x16x32_bf16 v[80:83], a[232:235], a[192:195], v[80:83]\n"
    "ds_read_b64_tr_b16 a[248:249], v9 offset:8192\n"
    "v_lshl_add_u32 v30, v29, 4, v28\n"
    "v_mfma_f32_16x16x32_bf16 v[80:83], a[236:239], a[208:211], v[80:83]\n"
    "buffer_load_dwordx4 a[180:183], v0, s[40:43], s72 offen offset:1024 sc1\n"
    "v_mfma_f32_16x16x32_bf16 v[84:87], a[232:235], a[196:199], v[84:87]\n"
    "ds_read_b64_tr_b16 a[250:251], v9 offset:10240\n"
    "v_accvgpr_write_b32 a115, 0\n"
    "v_mfma_f32_16x16x32_bf16 v[84:87], a[236:239], a[212:215], v[84:87]\n"
    "v_accvgpr_write_b32 a116, 0\n"
    "v_accvgpr_write_b32 a117, 0\n"
    "v_mfma_f32_16x16x32_bf16 v[88:91], a[232:235], a[200:203], v[88:91]\n"
    "ds_read_b64_tr_b16 a[252:253], v9 offset:12288\n"
    "v_accvgpr_write_b32 a118, 0\n"
    "v_mfma_f32_16x16x32_bf16 v[88:91], a[236:239], a[216:219], v[88:91]\n"
    "v_accvgpr_write_b32 a119, 0\n"
    "v_accvgpr_write_b32 a120, 0\n"
    "v_mfma_f32_16x16x32_bf16 v[92:95], a[232:235], a[204:207], v[92:95]\n"
    "ds_read_b64_tr_b16 a[254:255], v9 offset:14336\n"
    "v_accvgpr_write_b32 a121, 0\n"
    "v_mfma_f32_16x16x32_bf16 v[92:95], a[236:239], a[220:223], v[92:95]\n"
    "s_waitcnt lgkmcnt(4)\n"
    "v_accvgpr_write_b32 a122, 0\n"
    "v_accvgpr_write_b32 a123, 0\n"
    "v_mfma_f32_16x16x32_bf16 v[96:99], a[240:243], a[192:195], v[96:99]\n"
    "ds_read_b64_tr_b16 a[224:225], v6 offset:16384\n"
    "v_accvgpr_write_b32 a124, 0\n"
    "v_mfma_f32_16x16x32_bf16 v[96:99], a[244:247], a[208:211], v[96:99]\n"
    "buffer_load_dwordx4 a[184:187], v0, s[40:43], s73 offen offset:1024 sc1\n"
    "v_mfma_f32_16x16x32_bf16 v[100:103], a[240:243], a[196:199], v[100:103]\n"
    "ds_read_b64_tr_b16 a[226:227], v6 offset:18432\n"
    "v_accvgpr_write_b32 a125, 0\n"
    "v_mfma_f32_16x16x32_bf16 v[100:103], a[244:247], a[212:215], v[100:103]\n"
    "v_accvgpr_write_b32 a126, 0\n"
    "v_accvgpr_write_b32 a127, 0\n"
    "v_mfma_f32_16x16x32_bf16 v[104:107], a[240:243], a[200:203], v[104:107]\n"
    "ds_read_b64_tr_b16 a[228:229], v6 offset:20480\n"
    "v_mfma_f32_16x16x32_bf16 v[104:107], a[244:247], a[216:219], v[104:107]\n"
    "buffer_load_dwordx4 a[188:191], v0, s[40:43], s74 offen offset:1024 sc1\n"
    "v_mfma_f32_16x16x32_bf16 v[108:111], a[240:243], a[204:207], v[108:111]\n"
    "ds_read_b64_tr_b16 a[230:231], v6 offset:22528\n"
    "v_mfma_f32_16x16x32_bf16 v[108:111], a[244:247], a[220:223], v[108:111]\n"
    "s_waitcnt lgkmcnt(4)\n"
    "v_mfma_f32_16x16x32_bf16 v[112:115], a[248:251], a[192:195], v[112:115]\n"
    "ds_read_b64_tr_b16 a[232:233], v7 offset:16384\n"
    "v_mfma_f32_16x16x32_bf16 v[112:115], a[252:255], a[208:211], v[112:115]\n"
    "v_mfma_f32_16x16x32_bf16 v[116:119], a[248:251], a[196:199], v[116:119]\n"
    "ds_read_b64_tr_b16 a[234:235], v7 offset:18432\n"
    "v_mfma_f32_16x16x32_bf16 v[116:119], a[252:255], a[212:215], v[116:119]\n"
    "v_mfma_f32_16x16x32_bf16 v[120:123], a[248:251], a[200:203], v[120:123]\n"
    "ds_read_b64_tr_b16 a[236:237], v7 offset:20480\n"
    "v_mfma_f32_16x16x32_bf16 v[120:123], a[252:255], a[216:219], v[120:123]\n"
    "v_mfma_f32_16x16x32_bf16 v[124:127], a[248:251], a[204:207], v[124:127]\n"
    "s_waitcnt vmcnt(12)\n"
    "ds_read_b64_tr_b16 a[238:239], v7 offset:22528\n"
    "v_mfma_f32_16x16x32_bf16 v[124:127], a[252:255], a[220:223], v[124:127]\n"
    "s_waitcnt lgkmcnt(4)\n"
    "v_mfma_f32_16x16x32_bf16 v[128:131], a[224:227], a[192:195], v[128:131]\n"
    "ds_read_b64_tr_b16 a[240:241], v8 offset:16384\n"
    "v_mfma_f32_16x16x32_bf16 v[128:131], a[228:231], a[208:211], v[128:131]\n"
    "v_mfma_f32_16x16x32_bf16 v[132:135], a[224:227], a[196:199], v[132:135]\n"
    "s_lshl_b32 s63, s56, 7\n"
    "ds_read_b64_tr_b16 a[242:243], v8 offset:18432\n"
    "v_mfma_f32_16x16x32_bf16 v[132:135], a[228:231], a[212:215], v[132:135]\n"
    "s_addk_i32 s63, 0x2000\n"
    "v_mfma_f32_16x16x32_bf16 v[136:139], a[224:227], a[200:203], v[136:139]\n"
    "s_lshl1_add_u32 s89, s75, s63\n"
    "ds_read_b64_tr_b16 a[244:245], v8 offset:20480\n"
    "v_mfma_f32_16x16x32_bf16 v[136:139], a[228:231], a[216:219], v[136:139]\n"
    "buffer_store_dword v13, off, s[40:43], s89 offset:0 sc1\n"
    "v_mfma_f32_16x16x32_bf16 v[140:143], a[224:227], a[204:207], v[140:143]\n"
    "ds_read_b64_tr_b16 a[246:247], v8 offset:22528\n"
    "v_mfma_f32_16x16x32_bf16 v[140:143], a[228:231], a[220:223], v[140:143]\n"
    "s_waitcnt lgkmcnt(4)\n"
    "v_mfma_f32_16x16x32_bf16 v[144:147], a[232:235], a[192:195], v[144:147]\n"
    "ds_read_b64_tr_b16 a[248:249], v9 offset:16384\n"
    "v_mfma_f32_16x16x32_bf16 v[144:147], a[236:239], a[208:211], v[144:147]\n"
    "v_mfma_f32_16x16x32_bf16 v[148:151], a[232:235], a[196:199], v[148:151]\n"
    "ds_read_b64_tr_b16 a[250:251], v9 offset:18432\n"
    "v_mfma_f32_16x16x32_bf16 v[148:151], a[236:239], a[212:215], v[148:151]\n"
    "v_mfma_f32_16x16x32_bf16 v[152:155], a[232:235], a[200:203], v[152:155]\n"
    "ds_read_b64_tr_b16 a[252:253], v9 offset:20480\n"
    "v_mfma_f32_16x16x32_bf16 v[152:155], a[236:239], a[216:219], v[152:155]\n"
    "v_mfma_f32_16x16x32_bf16 v[156:159], a[232:235], a[204:207], v[156:159]\n"
    "ds_read_b64_tr_b16 a[254:255], v9 offset:22528\n"
    "v_mfma_f32_16x16x32_bf16 v[156:159], a[236:239], a[220:223], v[156:159]\n"
    "s_waitcnt lgkmcnt(4)\n"
    "v_mfma_f32_16x16x32_bf16 v[160:163], a[240:243], a[192:195], v[160:163]\n"
    "ds_read_b64_tr_b16 a[224:225], v6 offset:24576\n"
    "v_mfma_f32_16x16x32_bf16 v[160:163], a[244:247], a[208:211], v[160:163]\n"
    "v_mfma_f32_16x16x32_bf16 v[164:167], a[240:243], a[196:199], v[164:167]\n"
    "ds_read_b64_tr_b16 a[226:227], v6 offset:26624\n"
    "v_mfma_f32_16x16x32_bf16 v[164:167], a[244:247], a[212:215], v[164:167]\n"
    "v_mfma_f32_16x16x32_bf16 v[168:171], a[240:243], a[200:203], v[168:171]\n"
    "ds_read_b64_tr_b16 a[228:229], v6 offset:28672\n"
    "v_mfma_f32_16x16x32_bf16 v[168:171], a[244:247], a[216:219], v[168:171]\n"
    "v_mfma_f32_16x16x32_bf16 v[172:175], a[240:243], a[204:207], v[172:175]\n"
    "ds_read_b64_tr_b16 a[230:231], v6 offset:30720\n"
    "v_mfma_f32_16x16x32_bf16 v[172:175], a[244:247], a[220:223], v[172:175]\n"
    "s_waitcnt lgkmcnt(4)\n"
    "v_add_u32 v6, 0x8000, v6\n"
    "v_mfma_f32_16x16x32_bf16 v[176:179], a[248:251], a[192:195], v[176:179]\n"
    "ds_read_b64_tr_b16 a[232:233], v7 offset:24576\n"
    "v_mfma_f32_16x16x32_bf16 v[176:179], a[252:255], a[208:211], v[176:179]\n"
    "v_mfma_f32_16x16x32_bf16 v[180:183], a[248:251], a[196:199], v[180:183]\n"
    "ds_read_b64_tr_b16 a[234:235], v7 offset:26624\n"
    "v_mfma_f32_16x16x32_bf16 v[180:183], a[252:255], a[212:215], v[180:183]\n"
    "v_mfma_f32_16x16x32_bf16 v[184:187], a[248:251], a[200:203], v[184:187]\n"
    "ds_read_b64_tr_b16 a[236:237], v7 offset:28672\n"
    "v_mfma_f32_16x16x32_bf16 v[184:187], a[252:255], a[216:219], v[184:187]\n"
    "v_mfma_f32_16x16x32_bf16 v[188:191], a[248:251], a[204:207], v[188:191]\n"
    "ds_read_b64_tr_b16 a[238:239], v7 offset:30720\n"
    "v_mfma_f32_16x16x32_bf16 v[188:191], a[252:255], a[220:223], v[188:191]\n"
    "s_waitcnt lgkmcnt(4)\n"
    "v_add_u32 v7, 0x8000, v7\n"
    "v_mfma_f32_16x16x32_bf16 v[192:195], a[224:227], a[192:195], v[192:195]\n"
    "ds_read_b64_tr_b16 a[240:241], v8 offset:24576\n"
    "v_mfma_f32_16x16x32_bf16 v[192:195], a[228:231], a[208:211], v[192:195]\n"
    "v_mfma_f32_16x16x32_bf16 v[196:199], a[224:227], a[196:199], v[196:199]\n"
    "ds_read_b64_tr_b16 a[242:243], v8 offset:26624\n"
    "v_mfma_f32_16x16x32_bf16 v[196:199], a[228:231], a[212:215], v[196:199]\n"
    "v_mfma_f32_16x16x32_bf16 v[200:203], a[224:227], a[200:203], v[200:203]\n"
    "ds_read_b64_tr_b16 a[244:245], v8 offset:28672\n"
    "v_mfma_f32_16x16x32_bf16 v[200:203], a[228:231], a[216:219], v[200:203]\n"
    "v_mfma_f32_16x16x32_bf16 v[204:207], a[224:227], a[204:207], v[204:207]\n"
    "ds_read_b64_tr_b16 a[246:247], v8 offset:30720\n"
    "v_mfma_f32_16x16x32_bf16 v[204:207], a[228:231], a[220:223], v[204:207]\n"
    "s_waitcnt lgkmcnt(4)\n"
    "v_add_u32 v8, 0x8000, v8\n"
    "v_mfma_f32_16x16x32_bf16 v[208:211], a[232:235], a[192:195], v[208:211]\n"
    "ds_read_b64_tr_b16 a[248:249], v9 offset:24576\n"
    "v_mfma_f32_16x16x32_bf16 v[208:211], a[236:239], a[208:211], v[208:211]\n"
    "v_mfma_f32_16x16x32_bf16 v[212:215], a[232:235], a[196:199], v[212:215]\n"
    "ds_read_b64_tr_b16 a[250:251], v9 offset:26624\n"
    "v_mfma_f32_16x16x32_bf16 v[212:215], a[236:239], a[212:215], v[212:215]\n"
    "v_mfma_f32_16x16x32_bf16 v[216:219], a[232:235], a[200:203], v[216:219]\n"
    "s_waitcnt vmcnt(31)\n"
    "ds_read_b64_tr_b16 a[252:253], v9 offset:28672\n"
    "v_mfma_f32_16x16x32_bf16 v[216:219], a[236:239], a[216:219], v[216:219]\n"
    "s_barrier\n"
    "v_mfma_f32_16x16x32_bf16 v[220:223], a[232:235], a[204:207], v[220:223]\n"
    "ds_read_b64_tr_b16 a[254:255], v9 offset:30720\n"
    "v_mfma_f32_16x16x32_bf16 v[220:223], a[236:239], a[220:223], v[220:223]\n"
    "s_waitcnt lgkmcnt(4)\n"
    "v_add_u32 v9, 0x8000, v9\n"
    "v_mfma_f32_16x16x32_bf16 v[224:227], a[240:243], a[192:195], v[224:227]\n"
    "ds_read_b64_tr_b16 a[224:225], v6 offset:0\n"
    "v_mfma_f32_16x16x32_bf16 v[224:227], a[244:247], a[208:211], v[224:227]\n"
    "v_mfma_f32_16x16x32_bf16 v[228:231], a[240:243], a[196:199], v[228:231]\n"
    "ds_read_b64_tr_b16 a[226:227], v6 offset:2048\n"
    "v_mfma_f32_16x16x32_bf16 v[228:231], a[244:247], a[212:215], v[228:231]\n"
    "v_mfma_f32_16x16x32_bf16 v[232:235], a[240:243], a[200:203], v[232:235]\n"
    "ds_read_b64_tr_b16 a[228:229], v6 offset:4096\n"
    "v_mfma_f32_16x16x32_bf16 v[232:235], a[244:247], a[216:219], v[232:235]\n"
    "v_mfma_f32_16x16x32_bf16 v[236:239], a[240:243], a[204:207], v[236:239]\n"
    "ds_read_b64_tr_b16 a[230:231], v6 offset:6144\n"
    "v_mfma_f32_16x16x32_bf16 v[236:239], a[244:247], a[220:223], v[236:239]\n"
    "s_waitcnt lgkmcnt(4)\n"
    "v_mfma_f32_16x16x32_bf16 v[240:243], a[248:251], a[192:195], v[240:243]\n"
    "ds_read_b64_tr_b16 a[232:233], v7 offset:0\n"
    "v_mfma_f32_16x16x32_bf16 v[240:243], a[252:255], a[208:211], v[240:243]\n"
    "v_mfma_f32_16x16x32_bf16 v[244:247], a[248:251], a[196:199], v[244:247]\n"
    "ds_read_b64_tr_b16 a[234:235], v7 offset:2048\n"
    "v_mfma_f32_16x16x32_bf16 v[244:247], a[252:255], a[212:215], v[244:247]\n"
    "v_mfma_f32_16x16x32_bf16 v[248:251], a[248:251], a[200:203], v[248:251]\n"
    "ds_read_b64_tr_b16 a[236:237], v7 offset:4096\n"
    "v_mfma_f32_16x16x32_bf16 v[248:251], a[252:255], a[216:219], v[248:251]\n"
    "v_mfma_f32_16x16x32_bf16 v[252:255], a[248:251], a[204:207], v[252:255]\n"
    "s_waitcnt vmcnt(1)\n"
    "ds_read_b64_tr_b16 a[238:239], v7 offset:6144\n"
    "v_mfma_f32_16x16x32_bf16 v[252:255], a[252:255], a[220:223], v[252:255]\n"
    "s_waitcnt lgkmcnt(4)\n"
    "v_mfma_f32_16x16x32_bf16 a[128:131], a[224:227], a[160:163], a[128:131]\n"
    "ds_read_b64_tr_b16 a[240:241], v8 offset:0\n"
    "v_mfma_f32_16x16x32_bf16 a[128:131], a[228:231], a[176:179], a[128:131]\n"
    "v_mfma_f32_16x16x32_bf16 a[132:135], a[224:227], a[164:167], a[132:135]\n"
    "ds_read_b64_tr_b16 a[242:243], v8 offset:2048\n"
    "v_mfma_f32_16x16x32_bf16 a[132:135], a[228:231], a[180:183], a[132:135]\n"
    "v_mfma_f32_16x16x32_bf16 a[136:139], a[224:227], a[168:171], a[136:139]\n"
    "ds_read_b64_tr_b16 a[244:245], v8 offset:4096\n"
    "v_mfma_f32_16x16x32_bf16 a[136:139], a[228:231], a[184:187], a[136:139]\n"
    "buffer_load_dwordx4 a[192:195], v0, s[40:43], s71 offen offset:2048 sc1\n"
    "v_mfma_f32_16x16x32_bf16 a[140:143], a[224:227], a[172:175], a[140:143]\n"
    "ds_read_b64_tr_b16 a[246:247], v8 offset:6144\n"
    "v_mfma_f32_16x16x32_bf16 a[140:143], a[228:231], a[188:191], a[140:143]\n"
    "s_waitcnt lgkmcnt(4)\n"
    "v_mfma_f32_16x16x32_bf16 a[144:147], a[232:235], a[160:163], a[144:147]\n"
    "ds_read_b64_tr_b16 a[248:249], v9 offset:0\n"
    "v_mfma_f32_16x16x32_bf16 a[144:147], a[236:239], a[176:179], a[144:147]\n"
    "v_mfma_f32_16x16x32_bf16 a[148:151], a[232:235], a[164:167], a[148:151]\n"
    "ds_read_b64_tr_b16 a[250:251], v9 offset:2048\n"
    "v_mfma_f32_16x16x32_bf16 a[148:151], a[236:239], a[180:183], a[148:151]\n"
    "buffer_load_dwordx4 a[196:199], v0, s[40:43], s72 offen offset:2048 sc1\n"
    "v_mfma_f32_16x16x32_bf16 a[152:155], a[232:235], a[168:171], a[152:155]\n"
    "ds_read_b64_tr_b16 a[252:253], v9 offset:4096\n"
    "v_mfma_f32_16x16x32_bf16 a[152:155], a[236:239], a[184:187], a[152:155]\n"
    "v_mfma_f32_16x16x32_bf16 a[156:159], a[232:235], a[172:175], a[156:159]\n"
    "ds_read_b64_tr_b16 a[254:255], v9 offset:6144\n"
    "v_mfma_f32_16x16x32_bf16 a[156:159], a[236:239], a[188:191], a[156:159]\n"
    "s_waitcnt lgkmcnt(4)\n"
    "v_mfma_f32_16x16x32_bf16 v[32:35], a[240:243], a[160:163], v[32:35]\n"
    "ds_read_b64_tr_b16 a[224:225], v6 offset:8192\n"
    "v_mfma_f32_16x16x32_bf16 v[32:35], a[244:247], a[176:179], v[32:35]\n"
    "buffer_load_dwordx4 a[200:203], v0, s[40:43], s73 offen offset:2048 sc1\n"
    "v_mfma_f32_16x16x32_bf16 v[36:39], a[240:243], a[164:167], v[36:39]\n"
    "ds_read_b64_tr_b16 a[226:227], v6 offset:10240\n"
    "v_mfma_f32_16x16x32_bf16 v[36:39], a[244:247], a[180:183], v[36:39]\n"
    "v_mfma_f32_16x16x32_bf16 v[40:43], a[240:243], a[168:171], v[40:43]\n"
    "ds_read_b64_tr_b16 a[228:229], v6 offset:12288\n"
    "v_mfma_f32_16x16x32_bf16 v[40:43], a[244:247], a[184:187], v[40:43]\n"
    "v_mfma_f32_16x16x32_bf16 v[44:47], a[240:243], a[172:175], v[44:47]\n"
    "ds_read_b64_tr_b16 a[230:231], v6 offset:14336\n"
    "v_mfma_f32_16x16x32_bf16 v[44:47], a[244:247], a[188:191], v[44:47]\n"
    "s_waitcnt lgkmcnt(4)\n"
    "v_mfma_f32_16x16x32_bf16 v[48:51], a[248:251], a[160:163], v[48:51]\n"
    "ds_read_b64_tr_b16 a[232:233], v7 offset:8192\n"
    "v_mfma_f32_16x16x32_bf16 v[48:51], a[252:255], a[176:179], v[48:51]\n"
    "buffer_load_dwordx4 a[204:207], v0, s[40:43], s74 offen offset:2048 sc1\n"
    "v_mfma_f32_16x16x32_bf16 v[52:55], a[248:251], a[164:167], v[52:55]\n"
    "ds_read_b64_tr_b16 a[234:235], v7 offset:10240\n"
    "v_mfma_f32_16x16x32_bf16 v[52:55], a[252:255], a[180:183], v[52:55]\n"
    "v_mfma_f32_16x16x32_bf16 v[56:59], a[248:251], a[168:171], v[56:59]\n"
    "ds_read_b64_tr_b16 a[236:237], v7 offset:12288\n"
    "v_mfma_f32_16x16x32_bf16 v[56:59], a[252:255], a[184:187], v[56:59]\n"
    "buffer_load_dword v1, v10, s[40:43], s63 offen offset:0 sc1\n"
    "v_mfma_f32_16x16x32_bf16 v[60:63], a[248:251], a[172:175], v[60:63]\n"
    "ds_read_b64_tr_b16 a[238:239], v7 offset:14336\n"
    "v_mfma_f32_16x16x32_bf16 v[60:63], a[252:255], a[188:191], v[60:63]\n"
    "s_waitcnt lgkmcnt(4)\n"
    "v_mfma_f32_16x16x32_bf16 v[64:67], a[224:227], a[160:163], v[64:67]\n"
    "ds_read_b64_tr_b16 a[240:241], v8 offset:8192\n"
    "v_mfma_f32_16x16x32_bf16 v[64:67], a[228:231], a[176:179], v[64:67]\n"
    "v_mfma_f32_16x16x32_bf16 v[68:71], a[224:227], a[164:167], v[68:71]\n"
    "ds_read_b64_tr_b16 a[242:243], v8 offset:10240\n"
    "v_mfma_f32_16x16x32_bf16 v[68:71], a[228:231], a[180:183], v[68:71]\n"
    "buffer_load_dwordx4 a[208:211], v0, s[40:43], s71 offen offset:3072 sc1\n"
    "v_mfma_f32_16x16x32_bf16 v[72:75], a[224:227], a[168:171], v[72:75]\n"
    "ds_read_b64_tr_b16 a[244:245], v8 offset:12288\n"
    "v_mfma_f32_16x16x32_bf16 v[72:75], a[228:231], a[184:187], v[72:75]\n"
    "v_mfma_f32_16x16x32_bf16 v[76:79], a[224:227], a[172:175], v[76:79]\n"
    "ds_read_b64_tr_b16 a[246:247], v8 offset:14336\n"
    "v_mfma_f32_16x16x32_bf16 v[76:79], a[228:231], a[188:191], v[76:79]\n"
    "s_waitcnt lgkmcnt(4)\n"
    "v_mfma_f32_16x16x32_bf16 v[80:83], a[232:235], a[160:163], v[80:83]\n"
    "ds_read_b64_tr_b16 a[248:249], v9 offset:8192\n"
    "v_mfma_f32_16x16x32_bf16 v[80:83], a[236:239], a[176:179], v[80:83]\n"
    "buffer_load_dwordx4 a[212:215], v0, s[40:43], s72 offen offset:3072 sc1\n"
    "v_mfma_f32_16x16x32_bf16 v[84:87], a[232:235], a[164:167], v[84:87]\n"
    "ds_read_b64_tr_b16 a[250:251], v9 offset:10240\n"
    "v_mfma_f32_16x16x32_bf16 v[84:87], a[236:239], a[180:183], v[84:87]\n"
    "v_mfma_f32_16x16x32_bf16 v[88:91], a[232:235], a[168:171], v[88:91]\n"
    "ds_read_b64_tr_b16 a[252:253], v9 offset:12288\n"
    "v_mfma_f32_16x16x32_bf16 v[88:91], a[236:239], a[184:187], v[88:91]\n"
    "v_mfma_f32_16x16x32_bf16 v[92:95], a[232:235], a[172:175], v[92:95]\n"
    "ds_read_b64_tr_b16 a[254:255], v9 offset:14336\n"
    "v_mfma_f32_16x16x32_bf16 v[92:95], a[236:239], a[188:191], v[92:95]\n"
    "s_waitcnt lgkmcnt(4)\n"
    "v_mfma_f32_16x16x32_bf16 v[96:99], a[240:243], a[160:163], v[96:99]\n"
    "ds_read_b64_tr_b16 a[224:225], v6 offset:16384\n"
    "v_mfma_f32_16x16x32_bf16 v[96:99], a[244:247], a[176:179], v[96:99]\n"
    "buffer_load_dwordx4 a[216:219], v0, s[40:43], s73 offen offset:3072 sc1\n"
    "v_mfma_f32_16x16x32_bf16 v[100:103], a[240:243], a[164:167], v[100:103]\n"
    "ds_read_b64_tr_b16 a[226:227], v6 offset:18432\n"
    "v_mfma_f32_16x16x32_bf16 v[100:103], a[244:247], a[180:183], v[100:103]\n"
    "v_mfma_f32_16x16x32_bf16 v[104:107], a[240:243], a[168:171], v[104:107]\n"
    "ds_read_b64_tr_b16 a[228:229], v6 offset:20480\n"
    "v_mfma_f32_16x16x32_bf16 v[104:107], a[244:247], a[184:187], v[104:107]\n"
    "buffer_load_dwordx4 a[220:223], v0, s[40:43], s74 offen offset:3072 sc1\n"
    "v_mfma_f32_16x16x32_bf16 v[108:111], a[240:243], a[172:175], v[108:111]\n"
    "s_add_u32 s59, s28, 1\n"
    "ds_read_b64_tr_b16 a[230:231], v6 offset:22528\n"
    "v_mfma_f32_16x16x32_bf16 v[108:111], a[244:247], a[188:191], v[108:111]\n"
    "s_waitcnt lgkmcnt(4)\n"
    "v_mfma_f32_16x16x32_bf16 v[112:115], a[248:251], a[160:163], v[112:115]\n"
    "s_and_b32 s59, s59, 3\n"
    "ds_read_b64_tr_b16 a[232:233], v7 offset:16384\n"
    "v_mfma_f32_16x16x32_bf16 v[112:115], a[252:255], a[176:179], v[112:115]\n"
    "s_sub_u32 s59, s59, s28\n"
    "v_mfma_f32_16x16x32_bf16 v[116:119], a[248:251], a[164:167], v[116:119]\n"
    "s_mul_i32 s59, s59, 196608\n"
    "ds_read_b64_tr_b16 a[234:235], v7 offset:18432\n"
    "v_mfma_f32_16x16x32_bf16 v[116:119], a[252:255], a[180:183], v[116:119]\n"
    "s_add_u32 s36, s33, s59\n"
    "v_mfma_f32_16x16x32_bf16 v[120:123], a[248:251], a[168:171], v[120:123]\n"
    "s_add_u32 s59, s28, 2\n"
    "ds_read_b64_tr_b16 a[236:237], v7 offset:20480\n"
    "v_mfma_f32_16x16x32_bf16 v[120:123], a[252:255], a[184:187], v[120:123]\n"
    "s_and_b32 s59, s59, 3\n"
    "v_mfma_f32_16x16x32_bf16 v[124:127], a[248:251], a[172:175], v[124:127]\n"
    "s_waitcnt vmcnt(4)\n"
    "ds_read_b64_tr_b16 a[238:239], v7 offset:22528\n"
    "v_mfma_f32_16x16x32_bf16 v[124:127], a[252:255], a[188:191], v[124:127]\n"
    "s_waitcnt lgkmcnt(4)\n"
    "v_cmp_ne_u32 vcc, v1, v13\n"
    "s_nop 1\n"
    "s_cbranch_vccnz KVF_SPIN_%=\n"
    "KVF_OK_%=:\n"
    "v_mfma_f32_16x16x32_bf16 v[128:131], a[224:227], a[160:163], v[128:131]\n"
    "s_sub_u32 s59, s59, s28\n"
    "ds_read_b64_tr_b16 a[240:241], v8 offset:16384\n"
    "v_mfma_f32_16x16x32_bf16 v[128:131], a[228:231], a[176:179], v[128:131]\n"
    "s_mul_i32 s59, s59, 196608\n"
    "v_mfma_f32_16x16x32_bf16 v[132:135], a[224:227], a[164:167], v[132:135]\n"
    "s_mov_b32 s83, s84\n"
    "ds_read_b64_tr_b16 a[242:243], v8 offset:18432\n"
    "v_mfma_f32_16x16x32_bf16 v[132:135], a[228:231], a[180:183], v[132:135]\n"
    "s_max_u32 m0, s64, s79\n"
    "v_mfma_f32_16x16x32_bf16 v[136:139], a[224:227], a[168:171], v[136:139]\n"
    "s_add_u32 s37, s33, s59\n"
    "ds_read_b64_tr_b16 a[244:245], v8 offset:20480\n"
    "v_mfma_f32_16x16x32_bf16 v[136:139], a[228:231], a[184:187], v[136:139]\n"
    "buffer_load_dwordx4 v0, s[40:43], s83 offen offset:0 sc1 lds\n"
    "v_mfma_f32_16x16x32_bf16 v[140:143], a[224:227], a[172:175], v[140:143]\n"
    "s_addk_i32 s64, 0x4000\n"
    "ds_read_b64_tr_b16 a[246:247], v8 offset:22528\n"
    "v_mfma_f32_16x16x32_bf16 v[140:143], a[228:231], a[188:191], v[140:143]\n"
    "s_waitcnt lgkmcnt(4)\n"
    "v_mfma_f32_16x16x32_bf16 v[144:147], a[232:235], a[160:163], v[144:147]\n"
    "s_addk_i32 s37, 0x2000\n"
    "ds_read_b64_tr_b16 a[248:249], v9 offset:16384\n"
    "v_mfma_f32_16x16x32_bf16 v[144:147], a[236:239], a[176:179], v[144:147]\n"
    "buffer_load_dwordx4 v0, s[40:43], s83 offen offset:1024 sc1 lds\n"
    "v_mfma_f32_16x16x32_bf16 v[148:151], a[232:235], a[164:167], v[148:151]\n"
    "s_and_b32 s64, s64, 0x1ffff\n"
    "ds_read_b64_tr_b16 a[250:251], v9 offset:18432\n"
    "v_mfma_f32_16x16x32_bf16 v[148:151], a[236:239], a[180:183], v[148:151]\n"
    "s_add_u32 s59, s28, 3\n"
    "v_mfma_f32_16x16x32_bf16 v[152:155], a[232:235], a[168:171], v[152:155]\n"
    "s_and_b32 s59, s59, 3\n"
    "ds_read_b64_tr_b16 a[252:253], v9 offset:20480\n"
    "v_mfma_f32_16x16x32_bf16 v[152:155], a[236:239], a[184:187], v[152:155]\n"
    "buffer_load_dwordx4 v0, s[40:43], s83 offen offset:2048 sc1 lds\n"
    "v_mfma_f32_16x16x32_bf16 v[156:159], a[232:235], a[172:175], v[156:159]\n"
    "s_addk_i32 s84, 0x4000\n"
    "ds_read_b64_tr_b16 a[254:255], v9 offset:22528\n"
    "v_mfma_f32_16x16x32_bf16 v[156:159], a[236:239], a[188:191], v[156:159]\n"
    "s_waitcnt lgkmcnt(4)\n"
    "v_mfma_f32_16x16x32_bf16 v[160:163], a[240:243], a[160:163], v[160:163]\n"
    "s_sub_u32 s59, s59, s28\n"
    "ds_read_b64_tr_b16 a[224:225], v6 offset:24576\n"
    "v_mfma_f32_16x16x32_bf16 v[160:163], a[244:247], a[176:179], v[160:163]\n"
    "s_mul_i32 s59, s59, 196608\n"
    "v_mfma_f32_16x16x32_bf16 v[164:167], a[240:243], a[164:167], v[164:167]\n"
    "s_add_u32 s67, s33, s59\n"
    "ds_read_b64_tr_b16 a[226:227], v6 offset:26624\n"
    "v_mfma_f32_16x16x32_bf16 v[164:167], a[244:247], a[180:183], v[164:167]\n"
    "buffer_load_dwordx4 v0, s[40:43], s83 offen offset:3072 sc1 lds\n"
    "v_mfma_f32_16x16x32_bf16 v[168:171], a[240:243], a[168:171], v[168:171]\n"
    "s_mov_b32 s83, s84\n"
    "ds_read_b64_tr_b16 a[228:229], v6 offset:28672\n"
    "v_mfma_f32_16x16x32_bf16 v[168:171], a[244:247], a[184:187], v[168:171]\n"
    "s_max_u32 m0, s64, s79\n"
    "v_mfma_f32_16x16x32_bf16 v[172:175], a[240:243], a[172:175], v[172:175]\n"
    "s_addk_i32 s67, 0x4000\n"
    "ds_read_b64_tr_b16 a[230:231], v6 offset:30720\n"
    "v_mfma_f32_16x16x32_bf16 v[172:175], a[244:247], a[188:191], v[172:175]\n"
    "s_waitcnt lgkmcnt(4)\n"
    "v_add_u32 v6, 0x8000, v6\n"
    "v_mfma_f32_16x16x32_bf16 v[176:179], a[248:251], a[160:163], v[176:179]\n"
    "s_lshl1_add_u32 s59, s58, 0\n"
    "ds_read_b64_tr_b16 a[232:233], v7 offset:24576\n"
    "v_mfma_f32_16x16x32_bf16 v[176:179], a[252:255], a[176:179], v[176:179]\n"
    "buffer_load_dwordx4 v0, s[40:43], s83 offen offset:0 sc1 lds\n"
    "v_mfma_f32_16x16x32_bf16 v[180:183], a[248:251], a[164:167], v[180:183]\n"
    "s_addk_i32 s64, 0x4000\n"
    "ds_read_b64_tr_b16 a[234:235], v7 offset:26624\n"
    "v_mfma_f32_16x16x32_bf16 v[180:183], a[252:255], a[180:183], v[180:183]\n"
    "buffer_load_dwordx4 v0, s[40:43], s83 offen offset:1024 sc1 lds\n"
    "v_mfma_f32_16x16x32_bf16 v[184:187], a[248:251], a[168:171], v[184:187]\n"
    "s_and_b32 s64, s64, 0x1ffff\n"
    "ds_read_b64_tr_b16 a[236:237], v7 offset:28672\n"
    "v_mfma_f32_16x16x32_bf16 v[184:187], a[252:255], a[184:187], v[184:187]\n"
    "s_sub_u32 s60, s59, s28\n"
    "v_mfma_f32_16x16x32_bf16 v[188:191], a[248:251], a[172:175], v[188:191]\n"
    "s_and_b32 s60, s60, 3\n"
    "ds_read_b64_tr_b16 a[238:239], v7 offset:30720\n"
    "v_mfma_f32_16x16x32_bf16 v[188:191], a[252:255], a[188:191], v[188:191]\n"
    "s_waitcnt lgkmcnt(4)\n"
    "v_add_u32 v7, 0x8000, v7\n"
    "v_mfma_f32_16x16x32_bf16 v[192:195], a[224:227], a[160:163], v[192:195]\n"
    "s_and_b32 s59, s59, 4\n"
    "ds_read_b64_tr_b16 a[240:241], v8 offset:24576\n"
    "v_mfma_f32_16x16x32_bf16 v[192:195], a[228:231], a[176:179], v[192:195]\n"
    "buffer_load_dwordx4 v0, s[40:43], s83 offen offset:2048 sc1 lds\n"
    "v_mfma_f32_16x16x32_bf16 v[196:199], a[224:227], a[164:167], v[196:199]\n"
    "s_mov_b32 s84, s80\n"
    "ds_read_b64_tr_b16 a[242:243], v8 offset:26624\n"
    "v_mfma_f32_16x16x32_bf16 v[196:199], a[228:231], a[180:183], v[196:199]\n"
    "s_or_b32 s59, s59, s60\n"
    "v_mfma_f32_16x16x32_bf16 v[200:203], a[224:227], a[168:171], v[200:203]\n"
    "s_lshl_b32 s70, s59, 11\n"
    "ds_read_b64_tr_b16 a[244:245], v8 offset:28672\n"
    "v_mfma_f32_16x16x32_bf16 v[200:203], a[228:231], a[184:187], v[200:203]\n"
    "buffer_load_dwordx4 v0, s[40:43], s83 offen offset:3072 sc1 lds\n"
    "v_mfma_f32_16x16x32_bf16 v[204:207], a[224:227], a[172:175], v[204:207]\n"
    "s_mov_b32 s83, s84\n"
    "ds_read_b64_tr_b16 a[246:247], v8 offset:30720\n"
    "v_mfma_f32_16x16x32_bf16 v[204:207], a[228:231], a[188:191], v[204:207]\n"
    "s_waitcnt lgkmcnt(4)\n"
    "v_add_u32 v8, 0x8000, v8\n"
    "v_mfma_f32_16x16x32_bf16 v[208:211], a[232:235], a[160:163], v[208:211]\n"
    "s_max_u32 m0, s64, s79\n"
    "ds_read_b64_tr_b16 a[248:249], v9 offset:24576\n"
    "v_mfma_f32_16x16x32_bf16 v[208:211], a[236:239], a[176:179], v[208:211]\n"
    "buffer_load_dwordx4 v0, s[40:43], s83 offen offset:0 sc1 lds\n"
    "v_mfma_f32_16x16x32_bf16 v[212:215], a[232:235], a[164:167], v[212:215]\n"
    "s_addk_i32 s64, 0x4000\n"
    "ds_read_b64_tr_b16 a[250:251], v9 offset:26624\n"
    "v_mfma_f32_16x16x32_bf16 v[212:215], a[236:239], a[180:183], v[212:215]\n"
    "s_lshl1_add_u32 s59, s58, 1\n"
    "v_mfma_f32_16x16x32_bf16 v[216:219], a[232:235], a[168:171], v[216:219]\n"
    "s_waitcnt vmcnt(27)\n"
    "ds_read_b64_tr_b16 a[252:253], v9 offset:28672\n"
    "v_mfma_f32_16x16x32_bf16 v[216:219], a[236:239], a[184:187], v[216:219]\n"
    "s_barrier\n"
    "v_mfma_f32_16x16x32_bf16 v[220:223], a[232:235], a[172:175], v[220:223]\n"
    "s_sub_u32 s60, s59, s28\n"
    "ds_read_b64_tr_b16 a[254:255], v9 offset:30720\n"
    "v_mfma_f32_16x16x32_bf16 v[220:223], a[236:239], a[188:191], v[220:223]\n"
    "s_waitcnt lgkmcnt(4)\n"
    "v_add_u32 v9, 0x8000, v9\n"
    "v_mfma_f32_16x16x32_bf16 v[224:227], a[240:243], a[160:163], v[224:227]\n"
    "s_and_b32 s60, s60, 3\n"
    "ds_read_b64_tr_b16 a[224:225], v6 offset:16384\n"
    "v_mfma_f32_16x16x32_bf16 v[224:227], a[244:247], a[176:179], v[224:227]\n"
    "buffer_load_dwordx4 v0, s[40:43], s83 offen offset:1024 sc1 lds\n"
    "v_mfma_f32_16x16x32_bf16 v[228:231], a[240:243], a[164:167], v[228:231]\n"
    "s_and_b32 s64, s64, 0x1ffff\n"
    "ds_read_b64_tr_b16 a[226:227], v6 offset:18432\n"
    "v_mfma_f32_16x16x32_bf16 v[228:231], a[244:247], a[180:183], v[228:231]\n"
    "buffer_load_dwordx4 v0, s[40:43], s83 offen offset:2048 sc1 lds\n"
    "v_mfma_f32_16x16x32_bf16 v[232:235], a[240:243], a[168:171], v[232:235]\n"
    "s_addk_i32 s84, 0x4000\n"
    "ds_read_b64_tr_b16 a[228:229], v6 offset:20480\n"
    "v_mfma_f32_16x16x32_bf16 v[232:235], a[244:247], a[184:187], v[232:235]\n"
    "s_and_b32 s59, s59, 4\n"
    "v_mfma_f32_16x16x32_bf16 v[236:239], a[240:243], a[172:175], v[236:239]\n"
    "s_or_b32 s59, s59, s60\n"
    "ds_read_b64_tr_b16 a[230:231], v6 offset:22528\n"
    "v_mfma_f32_16x16x32_bf16 v[236:239], a[244:247], a[188:191], v[236:239]\n"
    "s_waitcnt lgkmcnt(4)\n"
    "v_mfma_f32_16x16x32_bf16 v[240:243], a[248:251], a[160:163], v[240:243]\n"
    "s_lshl_b32 s81, s59, 11\n"
    "ds_read_b64_tr_b16 a[232:233], v6 offset:24576\n"
    "v_mfma_f32_16x16x32_bf16 v[240:243], a[252:255], a[176:179], v[240:243]\n"
    "buffer_load_dwordx4 v0, s[40:43], s83 offen offset:3072 sc1 lds\n"
    "v_mfma_f32_16x16x32_bf16 v[244:247], a[248:251], a[164:167], v[244:247]\n"
    "s_sub_u32 s81, s81, s70\n"
    "ds_read_b64_tr_b16 a[234:235], v6 offset:26624\n"
    "v_mfma_f32_16x16x32_bf16 v[244:247], a[252:255], a[180:183], v[244:247]\n"
    "s_mul_i32 s82, s58, 5120\n"
    "v_mfma_f32_16x16x32_bf16 v[248:251], a[248:251], a[168:171], v[248:251]\n"
    "s_cmp_eq_u32 s28, 3\n"
    "ds_read_b64_tr_b16 a[236:237], v6 offset:28672\n"
    "v_mfma_f32_16x16x32_bf16 v[248:251], a[252:255], a[184:187], v[248:251]\n"
    "s_cselect_b32 s66, 5, 6\n"
    "v_mfma_f32_16x16x32_bf16 v[252:255], a[248:251], a[172:175], v[252:255]\n"
    "s_waitcnt vmcnt(12)\n"
    "ds_read_b64_tr_b16 a[238:239], v6 offset:30720\n"
    "v_mfma_f32_16x16x32_bf16 v[252:255], a[252:255], a[188:191], v[252:255]\n"
    "s_waitcnt lgkmcnt(4)\n"
    "v_mfma_f32_16x16x32_bf16 v[128:131], a[224:227], a[192:195], v[128:131]\n"
    "s_lshl2_add_u32 s59, s29, s58\n"
    "ds_read_b64_tr_b16 a[240:241], v7 offset:16384\n"
    "v_mfma_f32_16x16x32_bf16 v[128:131], a[228:231], a[208:211], v[128:131]\n"
    "s_and_b32 s60, s30, 0\n"
    "v_mfma_f32_16x16x32_bf16 v[132:135], a[224:227], a[196:199], v[132:135]\n"
    "s_mov_b32 s83, s84\n"
    "ds_read_b64_tr_b16 a[242:243], v7 offset:18432\n"
    "v_mfma_f32_16x16x32_bf16 v[132:135], a[228:231], a[212:215], v[132:135]\n"
    "s_max_u32 m0, s64, s79\n"
    "v_mfma_f32_16x16x32_bf16 v[136:139], a[224:227], a[200:203], v[136:139]\n"
    "s_lshl1_add_u32 s59, s59, s60\n"
    "ds_read_b64_tr_b16 a[244:245], v7 offset:20480\n"
    "v_mfma_f32_16x16x32_bf16 v[136:139], a[228:231], a[216:219], v[136:139]\n"
    "buffer_load_dwordx4 v0, s[40:43], s83 offen offset:0 sc1 lds\n"
    "v_mfma_f32_16x16x32_bf16 v[140:143], a[224:227], a[204:207], v[140:143]\n"
    "s_addk_i32 s64, 0x4000\n"
    "ds_read_b64_tr_b16 a[246:247], v7 offset:22528\n"
    "v_mfma_f32_16x16x32_bf16 v[140:143], a[228:231], a[220:223], v[140:143]\n"
    "s_waitcnt lgkmcnt(4)\n"
    "v_mfma_f32_16x16x32_bf16 v[192:195], a[232:235], a[192:195], v[192:195]\n"
    "s_lshl_b32 s60, s60, 18\n"
    "ds_read_b64_tr_b16 a[248:249], v7 offset:24576\n"
    "v_mfma_f32_16x16x32_bf16 v[192:195], a[236:239], a[208:211], v[192:195]\n"
    "buffer_load_dwordx4 v0, s[40:43], s83 offen offset:1024 sc1 lds\n"
    "v_mfma_f32_16x16x32_bf16 v[196:199], a[232:235], a[196:199], v[196:199]\n"
    "s_and_b32 s64, s64, 0x1ffff\n"
    "ds_read_b64_tr_b16 a[250:251], v7 offset:26624\n"
    "v_mfma_f32_16x16x32_bf16 v[196:199], a[236:239], a[212:215], v[196:199]\n"
    "s_sub_u32 s74, 0x20000, s60\n"
    "v_mfma_f32_16x16x32_bf16 v[200:203], a[232:235], a[200:203], v[200:203]\n"
    "s_mul_i32 s71, s59, 55296\n"
    "ds_read_b64_tr_b16 a[252:253], v7 offset:28672\n"
    "v_mfma_f32_16x16x32_bf16 v[200:203], a[236:239], a[216:219], v[200:203]\n"
    "buffer_load_dwordx4 v0, s[40:43], s83 offen offset:2048 sc1 lds\n"
    "v_mfma_f32_16x16x32_bf16 v[204:207], a[232:235], a[204:207], v[204:207]\n"
    "s_addk_i32 s84, 0x4000\n"
    "ds_read_b64_tr_b16 a[254:255], v7 offset:30720\n"
    "v_mfma_f32_16x16x32_bf16 v[204:207], a[236:239], a[220:223], v[204:207]\n"
    "s_waitcnt lgkmcnt(4)\n"
    "v_mfma_f32_16x16x32_bf16 v[144:147], a[240:243], a[192:195], v[144:147]\n"
    "s_mul_i32 s60, s28, 14336\n"
    "ds_read_b64_tr_b16 a[224:225], v8 offset:16384\n"
    "v_mfma_f32_16x16x32_bf16 v[144:147], a[244:247], a[208:211], v[144:147]\n"
    "s_add_u32 s71, s71, s60\n"
    "v_mfma_f32_16x16x32_bf16 v[148:151], a[240:243], a[196:199], v[148:151]\n"
    "s_add_u32 s71, s71, 0x2300000\n"
    "ds_read_b64_tr_b16 a[226:227], v8 offset:18432\n"
    "v_mfma_f32_16x16x32_bf16 v[148:151], a[244:247], a[212:215], v[148:151]\n"
    "buffer_load_dwordx4 v0, s[40:43], s83 offen offset:3072 sc1 lds\n"
    "v_mfma_f32_16x16x32_bf16 v[152:155], a[240:243], a[200:203], v[152:155]\n"
    "s_mov_b32 s83, s84\n"
    "ds_read_b64_tr_b16 a[228:229], v8 offset:20480\n"
    "v_mfma_f32_16x16x32_bf16 v[152:155], a[244:247], a[216:219], v[152:155]\n"
    "s_max_u32 m0, s64, s79\n"
    "v_mfma_f32_16x16x32_bf16 v[156:159], a[240:243], a[204:207], v[156:159]\n"
    "s_cmp_eq_u32 s28, 3\n"
    "ds_read_b64_tr_b16 a[230:231], v8 offset:22528\n"
    "v_mfma_f32_16x16x32_bf16 v[156:159], a[244:247], a[220:223], v[156:159]\n"
    "s_waitcnt lgkmcnt(4)\n"
    "v_mfma_f32_16x16x32_bf16 v[208:211], a[248:251], a[192:195], v[208:211]\n"
    "s_cselect_b32 s72, 0xc0000000, s71\n"
    "ds_read_b64_tr_b16 a[232:233], v8 offset:24576\n"
    "v_mfma_f32_16x16x32_bf16 v[208:211], a[252:255], a[208:211], v[208:211]\n"
    "buffer_load_dwordx4 v0, s[40:43], s83 offen offset:0 sc1 lds\n"
    "v_mfma_f32_16x16x32_bf16 v[212:215], a[248:251], a[196:199], v[212:215]\n"
    "s_addk_i32 s64, 0x4000\n"
    "ds_read_b64_tr_b16 a[234:235], v8 offset:26624\n"
    "v_mfma_f32_16x16x32_bf16 v[212:215], a[252:255], a[212:215], v[212:215]\n"
    "buffer_load_dwordx4 v0, s[40:43], s83 offen offset:1024 sc1 lds\n"
    "v_mfma_f32_16x16x32_bf16 v[216:219], a[248:251], a[200:203], v[216:219]\n"
    "s_and_b32 s64, s64, 0x1ffff\n"
    "ds_read_b64_tr_b16 a[236:237], v8 offset:28672\n"
    "v_mfma_f32_16x16x32_bf16 v[216:219], a[252:255], a[216:219], v[216:219]\n"
    "s_lshl2_add_u32 s59, s59, s28\n"
    "v_mfma_f32_16x16x32_bf16 v[220:223], a[248:251], a[204:207], v[220:223]\n"
    "s_lshl_b32 s73, s59, 15\n"
    "ds_read_b64_tr_b16 a[238:239], v8 offset:30720\n"
    "v_mfma_f32_16x16x32_bf16 v[220:223], a[252:255], a[220:223], v[220:223]\n"
    "s_waitcnt lgkmcnt(4)\n"
    "v_mul_f32 v128, v128, v192\n"
    "v_mul_f32 v129, v129, v193\n"
    "v_mfma_f32_16x16x32_bf16 v[160:163], a[224:227], a[192:195], v[160:163]\n"
    "s_add_u32 s73, s73, 0x29c0000\n"
    "ds_read_b64_tr_b16 a[240:241], v9 offset:16384\n"
    "v_mul_f32 v130, v130, v194\n"
    "v_mfma_f32_16x16x32_bf16 v[160:163], a[228:231], a[208:211], v[160:163]\n"
    "buffer_load_dwordx4 v0, s[40:43], s83 offen offset:2048 sc1 lds\n"
    "v_mfma_f32_16x16x32_bf16 v[164:167], a[224:227], a[196:199], v[164:167]\n"
    "s_addk_i32 s84, 0x4000\n"
    "ds_read_b64_tr_b16 a[242:243], v9 offset:18432\n"
    "v_mul_f32 v131, v131, v195\n"
    "v_mfma_f32_16x16x32_bf16 v[164:167], a[228:231], a[212:215], v[164:167]\n"
    "s_mul_i32 s59, s28, 92\n"
    "v_mul_f32 v144, v144, v208\n"
    "v_mul_f32 v145, v145, v209\n"
    "v_mfma_f32_16x16x32_bf16 v[168:171], a[224:227], a[200:203], v[168:171]\n"
    "s_add_u32 s59, s59, s30\n"
    "ds_read_b64_tr_b16 a[244:245], v9 offset:20480\n"
    "v_mul_f32 v146, v146, v210\n"
    "v_mfma_f32_16x16x32_bf16 v[168:171], a[228:231], a[216:219], v[168:171]\n"
    "buffer_load_dwordx4 v0, s[40:43], s83 offen offset:3072 sc1 lds\n"
    "v_mfma_f32_16x16x32_bf16 v[172:175], a[224:227], a[204:207], v[172:175]\n"
    "s_mov_b32 s83, s84\n"
    "ds_read_b64_tr_b16 a[246:247], v9 offset:22528\n"
    "v_mul_f32 v147, v147, v211\n"
    "v_mfma_f32_16x16x32_bf16 v[172:175], a[228:231], a[220:223], v[172:175]\n"
    "s_waitcnt lgkmcnt(4)\n"
    "v_cvt_pk_bf16_f32 v128, v128, v129\n"
    "v_cvt_pk_bf16_f32 v129, v130, v131\n"
    "v_mfma_f32_16x16x32_bf16 v[224:227], a[232:235], a[192:195], v[224:227]\n"
    "s_max_u32 m0, s64, s79\n"
    "ds_read_b64_tr_b16 a[248:249], v9 offset:24576\n"
    "v_cvt_pk_bf16_f32 v130, v144, v145\n"
    "v_mfma_f32_16x16x32_bf16 v[224:227], a[236:239], a[208:211], v[224:227]\n"
    "buffer_load_dwordx4 v0, s[40:43], s83 offen offset:0 sc1 lds\n"
    "v_mfma_f32_16x16x32_bf16 v[228:231], a[232:235], a[196:199], v[228:231]\n"
    "s_addk_i32 s64, 0x4000\n"
    "ds_read_b64_tr_b16 a[250:251], v9 offset:26624\n"
    "v_cvt_pk_bf16_f32 v131, v146, v147\n"
    "v_mfma_f32_16x16x32_bf16 v[228:231], a[236:239], a[212:215], v[228:231]\n"
    "s_lshl2_add_u32 s59, s59, s58\n"
    "v_mul_f32 v132, v132, v196\n"
    "v_mul_f32 v133, v133, v197\n"
    "v_mfma_f32_16x16x32_bf16 v[232:235], a[232:235], a[200:203], v[232:235]\n"
    "s_mul_i32 s68, s59, 5120\n"
    "ds_read_b64_tr_b16 a[252:253], v9 offset:28672\n"
    "v_mul_f32 v134, v134, v198\n"
    "v_mfma_f32_16x16x32_bf16 v[232:235], a[236:239], a[216:219], v[232:235]\n"
    "s_add_u32 s68, s68, 0x524c000\n"
    "v_mul_f32 v135, v135, v199\n"
    "v_mul_f32 v148, v148, v212\n"
    "v_mfma_f32_16x16x32_bf16 v[236:239], a[232:235], a[204:207], v[236:239]\n"
    "s_and_b32 s59, s56, 7\n"
    "ds_read_b64_tr_b16 a[254:255], v9 offset:30720\n"
    "v_mul_f32 v149, v149, v213\n"
    "v_mfma_f32_16x16x32_bf16 v[236:239], a[236:239], a[220:223], v[236:239]\n"
    "s_waitcnt lgkmcnt(4)\n"
    "v_mul_f32 v150, v150, v214\n"
    "v_mul_f32 v151, v151, v215\n"
    "v_mfma_f32_16x16x32_bf16 v[176:179], a[240:243], a[192:195], v[176:179]\n"
    "s_lshl_b32 s69, s59, 12\n"
    "ds_read_b64_tr_b16 a[224:225], v6 offset:0\n"
    "v_cvt_pk_bf16_f32 v132, v132, v133\n"
    "v_mfma_f32_16x16x32_bf16 v[176:179], a[244:247], a[208:211], v[176:179]\n"
    "buffer_load_dwordx4 v0, s[40:43], s83 offen offset:1024 sc1 lds\n"
    "v_mfma_f32_16x16x32_bf16 v[180:183], a[240:243], a[196:199], v[180:183]\n"
    "s_and_b32 s64, s64, 0x1ffff\n"
    "ds_read_b64_tr_b16 a[226:227], v6 offset:2048\n"
    "v_cvt_pk_bf16_f32 v133, v134, v135\n"
    "v_mfma_f32_16x16x32_bf16 v[180:183], a[244:247], a[212:215], v[180:183]\n"
    "buffer_load_dwordx4 v0, s[40:43], s83 offen offset:2048 sc1 lds\n"
    "v_mfma_f32_16x16x32_bf16 v[184:187], a[240:243], a[200:203], v[184:187]\n"
    "s_addk_i32 s84, 0x4000\n"
    "ds_read_b64_tr_b16 a[228:229], v6 offset:4096\n"
    "v_cvt_pk_bf16_f32 v134, v148, v149\n"
    "v_mfma_f32_16x16x32_bf16 v[184:187], a[244:247], a[216:219], v[184:187]\n"
    "v_cvt_pk_bf16_f32 v135, v150, v151\n"
    "v_mul_f32 v136, v136, v200\n"
    "v_mfma_f32_16x16x32_bf16 v[188:191], a[240:243], a[204:207], v[188:191]\n"
    "ds_read_b64_tr_b16 a[230:231], v6 offset:6144\n"
    "v_mul_f32 v137, v137, v201\n"
    "v_mfma_f32_16x16x32_bf16 v[188:191], a[244:247], a[220:223], v[188:191]\n"
    "s_waitcnt lgkmcnt(4)\n"
    "v_mul_f32 v138, v138, v202\n"
    "v_mul_f32 v139, v139, v203\n"
    "v_mfma_f32_16x16x32_bf16 v[240:243], a[248:251], a[192:195], v[240:243]\n"
    "ds_read_b64_tr_b16 a[232:233], v7 offset:0\n"
    "v_mul_f32 v152, v152, v216\n"
    "v_mfma_f32_16x16x32_bf16 v[240:243], a[252:255], a[208:211], v[240:243]\n"
    "buffer_load_dwordx4 v0, s[40:43], s83 offen offset:3072 sc1 lds\n"
    "v_mfma_f32_16x16x32_bf16 v[244:247], a[248:251], a[196:199], v[244:247]\n"
    "ds_read_b64_tr_b16 a[234:235], v7 offset:2048\n"
    "v_mul_f32 v153, v153, v217\n"
    "v_mfma_f32_16x16x32_bf16 v[244:247], a[252:255], a[212:215], v[244:247]\n"
    "v_mul_f32 v154, v154, v218\n"
    "v_mul_f32 v155, v155, v219\n"
    "v_mfma_f32_16x16x32_bf16 v[248:251], a[248:251], a[200:203], v[248:251]\n"
    "ds_read_b64_tr_b16 a[236:237], v7 offset:4096\n"
    "v_cvt_pk_bf16_f32 v136, v136, v137\n"
    "v_mfma_f32_16x16x32_bf16 v[248:251], a[252:255], a[216:219], v[248:251]\n"
    "v_cvt_pk_bf16_f32 v137, v138, v139\n"
    "v_cvt_pk_bf16_f32 v138, v152, v153\n"
    "v_mfma_f32_16x16x32_bf16 v[252:255], a[248:251], a[204:207], v[252:255]\n"
    "ds_read_b64_tr_b16 a[238:239], v7 offset:6144\n"
    "v_cvt_pk_bf16_f32 v139, v154, v155\n"
    "v_mfma_f32_16x16x32_bf16 v[252:255], a[252:255], a[220:223], v[252:255]\n"
    "s_waitcnt lgkmcnt(4)\n"
    "v_mul_f32 v140, v140, v204\n"
    "v_mul_f32 v141, v141, v205\n"
    "v_mfma_f32_16x16x32_bf16 a[128:131], a[224:227], a[192:195], a[128:131]\n"
    "ds_read_b64_tr_b16 a[240:241], v8 offset:0\n"
    "v_mul_f32 v142, v142, v206\n"
    "v_mfma_f32_16x16x32_bf16 a[128:131], a[228:231], a[208:211], a[128:131]\n"
    "v_mul_f32 v143, v143, v207\n"
    "v_mul_f32 v156, v156, v220\n"
    "v_mfma_f32_16x16x32_bf16 a[132:135], a[224:227], a[196:199], a[132:135]\n"
    "ds_read_b64_tr_b16 a[242:243], v8 offset:2048\n"
    "v_mul_f32 v157, v157, v221\n"
    "v_mfma_f32_16x16x32_bf16 a[132:135], a[228:231], a[212:215], a[132:135]\n"
    "v_mul_f32 v158, v158, v222\n"
    "v_mul_f32 v159, v159, v223\n"
    "v_mfma_f32_16x16x32_bf16 a[136:139], a[224:227], a[200:203], a[136:139]\n"
    "ds_read_b64_tr_b16 a[244:245], v8 offset:4096\n"
    "v_cvt_pk_bf16_f32 v140, v140, v141\n"
    "v_mfma_f32_16x16x32_bf16 a[136:139], a[228:231], a[216:219], a[136:139]\n"
    "v_cvt_pk_bf16_f32 v141, v142, v143\n"
    "v_cvt_pk_bf16_f32 v142, v156, v157\n"
    "v_mfma_f32_16x16x32_bf16 a[140:143], a[224:227], a[204:207], a[140:143]\n"
    "ds_read_b64_tr_b16 a[246:247], v8 offset:6144\n"
    "v_cvt_pk_bf16_f32 v143, v158, v159\n"
    "v_mfma_f32_16x16x32_bf16 a[140:143], a[228:231], a[220:223], a[140:143]\n"
    "s_waitcnt lgkmcnt(4)\n"
    "v_mul_f32 v160, v160, v224\n"
    "v_mul_f32 v161, v161, v225\n"
    "v_mfma_f32_16x16x32_bf16 a[144:147], a[232:235], a[192:195], a[144:147]\n"
    "ds_read_b64_tr_b16 a[248:249], v9 offset:0\n"
    "v_mul_f32 v162, v162, v226\n"
    "v_mfma_f32_16x16x32_bf16 a[144:147], a[236:239], a[208:211], a[144:147]\n"
    "v_mul_f32 v163, v163, v227\n"
    "v_mul_f32 v176, v176, v240\n"
    "v_mfma_f32_16x16x32_bf16 a[148:151], a[232:235], a[196:199], a[148:151]\n"
    "ds_read_b64_tr_b16 a[250:251], v9 offset:2048\n"
    "v_mul_f32 v177, v177, v241\n"
    "v_mfma_f32_16x16x32_bf16 a[148:151], a[236:239], a[212:215], a[148:151]\n"
    "v_mul_f32 v178, v178, v242\n"
    "v_mul_f32 v179, v179, v243\n"
    "v_mfma_f32_16x16x32_bf16 a[152:155], a[232:235], a[200:203], a[152:155]\n"
    "ds_read_b64_tr_b16 a[252:253], v9 offset:4096\n"
    "v_cvt_pk_bf16_f32 v160, v160, v161\n"
    "v_mfma_f32_16x16x32_bf16 a[152:155], a[236:239], a[216:219], a[152:155]\n"
    "v_cvt_pk_bf16_f32 v161, v162, v163\n"
    "v_cvt_pk_bf16_f32 v162, v176, v177\n"
    "v_mfma_f32_16x16x32_bf16 a[156:159], a[232:235], a[204:207], a[156:159]\n"
    "ds_read_b64_tr_b16 a[254:255], v9 offset:6144\n"
    "v_accvgpr_read_b32 v192, a128\n"
    "v_mfma_f32_16x16x32_bf16 a[156:159], a[236:239], a[220:223], a[156:159]\n"
    "s_waitcnt lgkmcnt(4)\n"
    "v_accvgpr_read_b32 v193, a129\n"
    "v_accvgpr_read_b32 v194, a130\n"
    "v_mfma_f32_16x16x32_bf16 v[32:35], a[240:243], a[192:195], v[32:35]\n"
    "ds_read_b64_tr_b16 a[224:225], v6 offset:8192\n"
    "v_accvgpr_read_b32 v195, a131\n"
    "v_mfma_f32_16x16x32_bf16 v[32:35], a[244:247], a[208:211], v[32:35]\n"
    "v_accvgpr_read_b32 v196, a144\n"
    "v_accvgpr_read_b32 v197, a145\n"
    "v_mfma_f32_16x16x32_bf16 v[36:39], a[240:243], a[196:199], v[36:39]\n"
    "ds_read_b64_tr_b16 a[226:227], v6 offset:10240\n"
    "v_accvgpr_read_b32 v198, a146\n"
    "v_mfma_f32_16x16x32_bf16 v[36:39], a[244:247], a[212:215], v[36:39]\n"
    "v_accvgpr_read_b32 v199, a147\n"
    "v_cvt_pk_bf16_f32 v208, v192, v193\n"
    "v_mfma_f32_16x16x32_bf16 v[40:43], a[240:243], a[200:203], v[40:43]\n"
    "ds_read_b64_tr_b16 a[228:229], v6 offset:12288\n"
    "v_cvt_pk_bf16_f32 v209, v194, v195\n"
    "v_mfma_f32_16x16x32_bf16 v[40:43], a[244:247], a[216:219], v[40:43]\n"
    "v_cvt_pk_bf16_f32 v210, v196, v197\n"
    "v_cvt_pk_bf16_f32 v211, v198, v199\n"
    "v_mfma_f32_16x16x32_bf16 v[44:47], a[240:243], a[204:207], v[44:47]\n"
    "ds_read_b64_tr_b16 a[230:231], v6 offset:14336\n"
    "v_accvgpr_read_b32 v200, a132\n"
    "v_mfma_f32_16x16x32_bf16 v[44:47], a[244:247], a[220:223], v[44:47]\n"
    "s_waitcnt lgkmcnt(4)\n"
    "v_accvgpr_read_b32 v201, a133\n"
    "v_accvgpr_read_b32 v202, a134\n"
    "v_mfma_f32_16x16x32_bf16 v[48:51], a[248:251], a[192:195], v[48:51]\n"
    "ds_read_b64_tr_b16 a[232:233], v7 offset:8192\n"
    "v_accvgpr_read_b32 v203, a135\n"
    "v_mfma_f32_16x16x32_bf16 v[48:51], a[252:255], a[208:211], v[48:51]\n"
    "v_accvgpr_read_b32 v204, a148\n"
    "v_accvgpr_read_b32 v205, a149\n"
    "v_mfma_f32_16x16x32_bf16 v[52:55], a[248:251], a[196:199], v[52:55]\n"
    "ds_read_b64_tr_b16 a[234:235], v7 offset:10240\n"
    "v_accvgpr_read_b32 v206, a150\n"
    "v_mfma_f32_16x16x32_bf16 v[52:55], a[252:255], a[212:215], v[52:55]\n"
    "v_accvgpr_read_b32 v207, a151\n"
    "v_cvt_pk_bf16_f32 v212, v200, v201\n"
    "v_mfma_f32_16x16x32_bf16 v[56:59], a[248:251], a[200:203], v[56:59]\n"
    "ds_read_b64_tr_b16 a[236:237], v7 offset:12288\n"
    "v_cvt_pk_bf16_f32 v213, v202, v203\n"
    "v_mfma_f32_16x16x32_bf16 v[56:59], a[252:255], a[216:219], v[56:59]\n"
    "v_cvt_pk_bf16_f32 v214, v204, v205\n"
    "v_cvt_pk_bf16_f32 v215, v206, v207\n"
    "v_mfma_f32_16x16x32_bf16 v[60:63], a[248:251], a[204:207], v[60:63]\n"
    "ds_read_b64_tr_b16 a[238:239], v7 offset:14336\n"
    "v_accvgpr_read_b32 v192, a136\n"
    "v_mfma_f32_16x16x32_bf16 v[60:63], a[252:255], a[220:223], v[60:63]\n"
    "s_waitcnt lgkmcnt(4)\n"
    "v_accvgpr_read_b32 v193, a137\n"
    "v_accvgpr_read_b32 v194, a138\n"
    "v_mfma_f32_16x16x32_bf16 v[64:67], a[224:227], a[192:195], v[64:67]\n"
    "ds_read_b64_tr_b16 a[240:241], v8 offset:8192\n"
    "v_accvgpr_read_b32 v195, a139\n"
    "v_mfma_f32_16x16x32_bf16 v[64:67], a[228:231], a[208:211], v[64:67]\n"
    "v_accvgpr_read_b32 v196, a152\n"
    "v_accvgpr_read_b32 v197, a153\n"
    "v_mfma_f32_16x16x32_bf16 v[68:71], a[224:227], a[196:199], v[68:71]\n"
    "ds_read_b64_tr_b16 a[242:243], v8 offset:10240\n"
    "v_accvgpr_read_b32 v198, a154\n"
    "v_mfma_f32_16x16x32_bf16 v[68:71], a[228:231], a[212:215], v[68:71]\n"
    "v_accvgpr_read_b32 v199, a155\n"
    "v_cvt_pk_bf16_f32 v216, v192, v193\n"
    "v_mfma_f32_16x16x32_bf16 v[72:75], a[224:227], a[200:203], v[72:75]\n"
    "ds_read_b64_tr_b16 a[244:245], v8 offset:12288\n"
    "v_cvt_pk_bf16_f32 v217, v194, v195\n"
    "v_mfma_f32_16x16x32_bf16 v[72:75], a[228:231], a[216:219], v[72:75]\n"
    "v_cvt_pk_bf16_f32 v218, v196, v197\n"
    "v_cvt_pk_bf16_f32 v219, v198, v199\n"
    "v_mfma_f32_16x16x32_bf16 v[76:79], a[224:227], a[204:207], v[76:79]\n"
    "ds_read_b64_tr_b16 a[246:247], v8 offset:14336\n"
    "v_accvgpr_read_b32 v200, a140\n"
    "v_mfma_f32_16x16x32_bf16 v[76:79], a[228:231], a[220:223], v[76:79]\n"
    "s_waitcnt lgkmcnt(4)\n"
    "v_accvgpr_read_b32 v201, a141\n"
    "v_accvgpr_read_b32 v202, a142\n"
    "v_mfma_f32_16x16x32_bf16 v[80:83], a[232:235], a[192:195], v[80:83]\n"
    "ds_read_b64_tr_b16 a[248:249], v9 offset:8192\n"
    "v_accvgpr_read_b32 v203, a143\n"
    "v_mfma_f32_16x16x32_bf16 v[80:83], a[236:239], a[208:211], v[80:83]\n"
    "v_accvgpr_read_b32 v204, a156\n"
    "v_accvgpr_read_b32 v205, a157\n"
    "v_mfma_f32_16x16x32_bf16 v[84:87], a[232:235], a[196:199], v[84:87]\n"
    "ds_read_b64_tr_b16 a[250:251], v9 offset:10240\n"
    "v_accvgpr_read_b32 v206, a158\n"
    "v_mfma_f32_16x16x32_bf16 v[84:87], a[236:239], a[212:215], v[84:87]\n"
    "v_accvgpr_read_b32 v207, a159\n"
    "v_cvt_pk_bf16_f32 v220, v200, v201\n"
    "v_mfma_f32_16x16x32_bf16 v[88:91], a[232:235], a[200:203], v[88:91]\n"
    "s_waitcnt vmcnt(16)\n"
    "ds_read_b64_tr_b16 a[252:253], v9 offset:12288\n"
    "v_cvt_pk_bf16_f32 v221, v202, v203\n"
    "v_mfma_f32_16x16x32_bf16 v[88:91], a[236:239], a[216:219], v[88:91]\n"
    "s_barrier\n"
    "v_cvt_pk_bf16_f32 v222, v204, v205\n"
    "v_cvt_pk_bf16_f32 v223, v206, v207\n"
    "v_mfma_f32_16x16x32_bf16 v[92:95], a[232:235], a[204:207], v[92:95]\n"
    "ds_read_b64_tr_b16 a[254:255], v9 offset:14336\n"
    "v_cvt_pk_bf16_f32 v32, v32, v33\n"
    "v_mfma_f32_16x16x32_bf16 v[92:95], a[236:239], a[220:223], v[92:95]\n"
    "s_waitcnt lgkmcnt(4)\n"
    "v_cvt_pk_bf16_f32 v33, v34, v35\n"
    "v_cvt_pk_bf16_f32 v34, v48, v49\n"
    "v_mfma_f32_16x16x32_bf16 v[96:99], a[240:243], a[192:195], v[96:99]\n"
    "ds_read_b128 a[160:163], v4 offset:8192\n"
    "v_cvt_pk_bf16_f32 v35, v50, v51\n"
    "v_mfma_f32_16x16x32_bf16 v[96:99], a[244:247], a[208:211], v[96:99]\n"
    "v_cvt_pk_bf16_f32 v36, v36, v37\n"
    "v_cvt_pk_bf16_f32 v37, v38, v39\n"
    "v_mfma_f32_16x16x32_bf16 v[100:103], a[240:243], a[196:199], v[100:103]\n"
    "ds_read_b128 a[164:167], v4 offset:9216\n"
    "v_cvt_pk_bf16_f32 v38, v52, v53\n"
    "v_mfma_f32_16x16x32_bf16 v[100:103], a[244:247], a[212:215], v[100:103]\n"
    "v_cvt_pk_bf16_f32 v39, v54, v55\n"
    "v_cvt_pk_bf16_f32 v40, v40, v41\n"
    "v_mfma_f32_16x16x32_bf16 v[104:107], a[240:243], a[200:203], v[104:107]\n"
    "ds_read_b128 a[168:171], v4 offset:10240\n"
    "v_cvt_pk_bf16_f32 v41, v42, v43\n"
    "v_mfma_f32_16x16x32_bf16 v[104:107], a[244:247], a[216:219], v[104:107]\n"
    "v_cvt_pk_bf16_f32 v42, v56, v57\n"
    "v_cvt_pk_bf16_f32 v43, v58, v59\n"
    "v_mfma_f32_16x16x32_bf16 v[108:111], a[240:243], a[204:207], v[108:111]\n"
    "ds_read_b128 a[172:175], v4 offset:11264\n"
    "v_cvt_pk_bf16_f32 v44, v44, v45\n"
    "v_mfma_f32_16x16x32_bf16 v[108:111], a[244:247], a[220:223], v[108:111]\n"
    "s_waitcnt lgkmcnt(4)\n"
    "v_cvt_pk_bf16_f32 v45, v46, v47\n"
    "v_cvt_pk_bf16_f32 v46, v60, v61\n"
    "v_mfma_f32_16x16x32_bf16 v[112:115], a[248:251], a[192:195], v[112:115]\n"
    "ds_read_b128 a[176:179], v4 offset:12288\n"
    "v_cvt_pk_bf16_f32 v47, v62, v63\n"
    "v_mfma_f32_16x16x32_bf16 v[112:115], a[252:255], a[208:211], v[112:115]\n"
    "v_cvt_pk_bf16_f32 v48, v64, v65\n"
    "v_cvt_pk_bf16_f32 v49, v66, v67\n"
    "v_mfma_f32_16x16x32_bf16 v[116:119], a[248:251], a[196:199], v[116:119]\n"
    "ds_read_b128 a[180:183], v4 offset:13312\n"
    "v_cvt_pk_bf16_f32 v50, v80, v81\n"
    "v_mfma_f32_16x16x32_bf16 v[116:119], a[252:255], a[212:215], v[116:119]\n"
    "v_cvt_pk_bf16_f32 v51, v82, v83\n"
    "v_cvt_pk_bf16_f32 v52, v68, v69\n"
    "v_mfma_f32_16x16x32_bf16 v[120:123], a[248:251], a[200:203], v[120:123]\n"
    "ds_read_b128 a[184:187], v4 offset:14336\n"
    "v_cvt_pk_bf16_f32 v53, v70, v71\n"
    "v_mfma_f32_16x16x32_bf16 v[120:123], a[252:255], a[216:219], v[120:123]\n"
    "v_cvt_pk_bf16_f32 v54, v84, v85\n"
    "v_cvt_pk_bf16_f32 v55, v86, v87\n"
    "v_mfma_f32_16x16x32_bf16 v[124:127], a[248:251], a[204:207], v[124:127]\n"
    "ds_read_b128 a[188:191], v4 offset:15360\n"
    "v_cvt_pk_bf16_f32 v56, v72, v73\n"
    "v_mfma_f32_16x16x32_bf16 v[124:127], a[252:255], a[220:223], v[124:127]\n"
    "v_cvt_pk_bf16_f32 v57, v74, v75\n"
    "v_cvt_pk_bf16_f32 v58, v88, v89\n"
    "ATT_INIT_%=:\n"
    "s_nop 6\n"
    "v_cvt_pk_bf16_f32 v59, v90, v91\n"
    "v_cvt_pk_bf16_f32 v60, v76, v77\n"
    "v_cvt_pk_bf16_f32 v61, v78, v79\n"
    "v_cvt_pk_bf16_f32 v62, v92, v93\n"
    "v_cvt_pk_bf16_f32 v63, v94, v95\n"
    "v_cvt_pk_bf16_f32 v64, v96, v97\n"
    "v_cvt_pk_bf16_f32 v65, v98, v99\n"
    "v_cvt_pk_bf16_f32 v66, v112, v113\n"
    "v_cvt_pk_bf16_f32 v67, v114, v115\n"
    "v_cvt_pk_bf16_f32 v68, v100, v101\n"
    "v_cvt_pk_bf16_f32 v69, v102, v103\n"
    "v_cvt_pk_bf16_f32 v70, v116, v117\n"
    "v_cvt_pk_bf16_f32 v71, v118, v119\n"
    "v_cvt_pk_bf16_f32 v72, v104, v105\n"
    "v_cvt_pk_bf16_f32 v73, v106, v107\n"
    "v_cvt_pk_bf16_f32 v74, v120, v121\n"
    "v_cvt_pk_bf16_f32 v75, v122, v123\n"
    "v_cvt_pk_bf16_f32 v76, v108, v109\n"
    "v_cvt_pk_bf16_f32 v77, v110, v111\n"
    "v_cvt_pk_bf16_f32 v78, v124, v125\n"
    "v_cvt_pk_bf16_f32 v79, v126, v127\n"
    "s_waitcnt lgkmcnt(0)\n"
    "s_nop 0\n"
    "v_mfma_f32_16x16x32_bf16 v[96:99], a[160:163], v[216:219], 0\n"
    "s_mov_b32 s83, s84\n"
    "v_cvt_pk_bf16_f32 v163, v178, v179\n"
    "v_mul_f32 v164, v164, v228\n"
    "v_mfma_f32_16x16x32_bf16 v[96:99], a[164:167], v[40:43], v[96:99]\n"
    "s_max_u32 m0, s64, s79\n"
    "v_mul_f32 v165, v165, v229\n"
    "v_mul_f32 v166, v166, v230\n"
    "v_mfma_f32_16x16x32_bf16 v[96:99], a[168:171], v[56:59], v[96:99]\n"
    "buffer_load_dwordx4 v0, s[40:43], s83 offen offset:0 sc1 lds\n"
    "v_mfma_f32_16x16x32_bf16 v[96:99], a[172:175], v[72:75], v[96:99]\n"
    "s_addk_i32 s64, 0x4000\n"
    "v_mul_f32 v167, v167, v231\n"
    "v_mul_f32 v180, v180, v244\n"
    "v_mfma_f32_16x16x32_bf16 v[100:103], a[176:179], v[216:219], 0\n"
    "v_mul_f32 v181, v181, v245\n"
    "v_mul_f32 v182, v182, v246\n"
    "v_mfma_f32_16x16x32_bf16 v[100:103], a[180:183], v[40:43], v[100:103]\n"
    "v_mul_f32 v183, v183, v247\n"
    "v_cvt_pk_bf16_f32 v164, v164, v165\n"
    "v_mfma_f32_16x16x32_bf16 v[100:103], a[184:187], v[56:59], v[100:103]\n"
    "buffer_load_dwordx4 v0, s[40:43], s83 offen offset:1024 sc1 lds\n"
    "v_mfma_f32_16x16x32_bf16 v[100:103], a[188:191], v[72:75], v[100:103]\n"
    "s_and_b32 s64, s64, 0x1ffff\n"
    "v_cvt_pk_bf16_f32 v165, v166, v167\n"
    "v_cvt_pk_bf16_f32 v166, v180, v181\n"
    "v_mfma_f32_16x16x32_bf16 v[104:107], a[160:163], v[220:223], 0\n"
    "v_cvt_pk_bf16_f32 v167, v182, v183\n"
    "v_mul_f32 v168, v168, v232\n"
    "v_mfma_f32_16x16x32_bf16 v[104:107], a[164:167], v[44:47], v[104:107]\n"
    "v_mul_f32 v169, v169, v233\n"
    "v_mul_f32 v170, v170, v234\n"
    "v_mfma_f32_16x16x32_bf16 v[104:107], a[168:171], v[60:63], v[104:107]\n"
    "buffer_load_dwordx4 v0, s[40:43], s83 offen offset:2048 sc1 lds\n"
    "v_mfma_f32_16x16x32_bf16 v[104:107], a[172:175], v[76:79], v[104:107]\n"
    "s_addk_i32 s84, 0x4000\n"
    "v_mul_f32 v171, v171, v235\n"
    "v_mul_f32 v184, v184, v248\n"
    "v_mfma_f32_16x16x32_bf16 v[108:111], a[176:179], v[220:223], 0\n"
    "v_mul_f32 v185, v185, v249\n"
    "v_mul_f32 v186, v186, v250\n"
    "v_mfma_f32_16x16x32_bf16 v[108:111], a[180:183], v[44:47], v[108:111]\n"
    "v_mul_f32 v187, v187, v251\n"
    "v_cvt_pk_bf16_f32 v168, v168, v169\n"
    "v_mfma_f32_16x16x32_bf16 v[108:111], a[184:187], v[60:63], v[108:111]\n"
    "buffer_load_dwordx4 v0, s[40:43], s83 offen offset:3072 sc1 lds\n"
    "v_mfma_f32_16x16x32_bf16 v[108:111], a[188:191], v[76:79], v[108:111]\n"
    "v_cvt_pk_bf16_f32 v169, v170, v171\n"
    "v_cvt_pk_bf16_f32 v170, v184, v185\n"
    "v_mfma_f32_16x16x32_bf16 v[112:115], a[160:163], v[208:211], 0\n"
    "v_cvt_pk_bf16_f32 v171, v186, v187\n"
    "v_mul_f32 v172, v172, v236\n"
    "v_mfma_f32_16x16x32_bf16 v[112:115], a[164:167], v[32:35], v[112:115]\n"
    "v_mul_f32 v173, v173, v237\n"
    "v_mul_f32 v174, v174, v238\n"
    "v_mfma_f32_16x16x32_bf16 v[112:115], a[168:171], v[48:51], v[112:115]\n"
    "v_mul_f32 v175, v175, v239\n"
    "v_mul_f32 v188, v188, v252\n"
    "v_mfma_f32_16x16x32_bf16 v[112:115], a[172:175], v[64:67], v[112:115]\n"
    "v_mul_f32 v189, v189, v253\n"
    "v_mul_f32 v190, v190, v254\n"
    "v_mfma_f32_16x16x32_bf16 v[116:119], a[176:179], v[208:211], 0\n"
    "s_barrier\n"
    "v_mul_f32 v191, v191, v255\n"
    "v_cvt_pk_bf16_f32 v172, v172, v173\n"
    "v_mfma_f32_16x16x32_bf16 v[116:119], a[180:183], v[32:35], v[116:119]\n"
    "v_cvt_pk_bf16_f32 v173, v174, v175\n"
    "v_cvt_pk_bf16_f32 v174, v188, v189\n"
    "v_mfma_f32_16x16x32_bf16 v[116:119], a[184:187], v[48:51], v[116:119]\n"
    "v_cvt_pk_bf16_f32 v175, v190, v191\n"
    "v_max3_f32 v16, v96, v97, v98\n"
    "v_mfma_f32_16x16x32_bf16 v[116:119], a[188:191], v[64:67], v[116:119]\n"
    "v_max3_f32 v28, v99, v100, v101\n"
    "v_max3_f32 v16, v16, v102, v103\n"
    "v_mfma_f32_16x16x32_bf16 v[120:123], a[160:163], v[212:215], 0\n"
    "v_max_f32 v16, v16, v28\n"
    "v_max3_f32 v17, v104, v105, v106\n"
    "v_mfma_f32_16x16x32_bf16 v[120:123], a[164:167], v[36:39], v[120:123]\n"
    "v_max3_f32 v29, v107, v108, v109\n"
    "v_max3_f32 v17, v17, v110, v111\n"
    "v_mfma_f32_16x16x32_bf16 v[120:123], a[168:171], v[52:55], v[120:123]\n"
    "v_max_f32 v17, v17, v29\n"
    "v_mov_b32 v28, v16\n"
    "v_mfma_f32_16x16x32_bf16 v[120:123], a[172:175], v[68:71], v[120:123]\n"
    "v_mov_b32 v29, v17\n"
    "v_nop\n"
    "v_mfma_f32_16x16x32_bf16 v[124:127], a[176:179], v[212:215], 0\n"
    "v_permlane16_swap_b32 v16, v28\n"
    "v_permlane16_swap_b32 v17, v29\n"
    "v_mfma_f32_16x16x32_bf16 v[124:127], a[180:183], v[36:39], v[124:127]\n"
    "v_max_f32 v16, v16, v28\n"
    "v_max_f32 v17, v17, v29\n"
    "v_mfma_f32_16x16x32_bf16 v[124:127], a[184:187], v[52:55], v[124:127]\n"
    "v_mov_b32 v28, v16\n"
    "v_mov_b32 v29, v17\n"
    "v_mfma_f32_16x16x32_bf16 v[124:127], a[188:191], v[68:71], v[124:127]\n"
    "v_nop\n"
    "v_permlane32_swap_b32 v16, v28\n"
    "s_mul_i32 s61, s75, 55296\n"
    "s_add_u32 s61, s61, s65\n"
    "v_permlane32_swap_b32 v17, v29\n"
    "v_max_f32 v16, v16, v28\n"
    "v_max_f32 v17, v17, v29\n"
    "s_nop 3\n"
    "v_max3_f32 v18, v112, v113, v114\n"
    "v_max3_f32 v28, v115, v116, v117\n"
    "v_max3_f32 v18, v18, v118, v119\n"
    "v_max_f32 v18, v18, v28\n"
    "v_max3_f32 v19, v120, v121, v122\n"
    "v_max3_f32 v29, v123, v124, v125\n"
    "v_max3_f32 v19, v19, v126, v127\n"
    "v_max_f32 v19, v19, v29\n"
    "v_mov_b32 v28, v18\n"
    "buffer_store_dwordx4 v[128:131], v0, s[40:43], s61 offen offset:0 sc1\n"
    "s_add_u32 s61, s61, 55296\n"
    "v_mov_b32 v29, v19\n"
    "v_nop\n"
    "v_permlane16_swap_b32 v18, v28\n"
    "v_permlane16_swap_b32 v19, v29\n"
    "v_max_f32 v18, v18, v28\n"
    "v_max_f32 v19, v19, v29\n"
    "v_mov_b32 v28, v18\n"
    "v_mov_b32 v29, v19\n"
    "v_nop\n"
    "v_permlane32_swap_b32 v18, v28\n"
    "v_permlane32_swap_b32 v19, v29\n"
    "v_max_f32 v18, v18, v28\n"
    "v_max_f32 v19, v19, v29\n"
    "buffer_store_dwordx4 v[132:135], v0, s[40:43], s61 offen offset:0 sc1\n"
    "ATT_RESTART_%=:\n"
    "s_cmp_eq_u32 s57, 7\n"
    "s_cbranch_scc1 ATT_NOK1_%=\n"
    "v_add_u32 v4, 0x4000, v4\n"
    "v_and_b32 v4, 0x1ffff, v4\n"
    "ds_read_b128 a[160:163], v4 offset:8192\n"
    "ds_read_b128 a[164:167], v4 offset:9216\n"
    "ds_read_b128 a[168:171], v4 offset:10240\n"
    "ds_read_b128 a[172:175], v4 offset:11264\n"
    "ds_read_b128 a[176:179], v4 offset:12288\n"
    "ds_read_b128 a[180:183], v4 offset:13312\n"
    "ds_read_b128 a[184:187], v4 offset:14336\n"
    "ds_read_b128 a[188:191], v4 offset:15360\n"
    "ATT_NOK1_%=:\n"
    "s_mul_i32 s61, s76, 55296\n"
    "s_add_u32 s61, s61, s65\n"
    "v_mfma_f32_32x32x16_bf16 v[240:255], a[0:3], a[0:3], 0\n"
    "v_sub_f32 v96, v96, v16\n"
    "v_sub_f32 v97, v97, v16\n"
    "v_sub_f32 v98, v98, v16\n"
    "v_sub_f32 v99, v99, v16\n"
    "v_sub_f32 v100, v100, v16\n"
    "v_sub_f32 v101, v101, v16\n"
    "v_sub_f32 v102, v102, v16\n"
    "v_sub_f32 v103, v103, v16\n"
    "v_sub_f32 v104, v104, v17\n"
    "v_sub_f32 v105, v105, v17\n"
    "v_sub_f32 v106, v106, v17\n"
    "v_sub_f32 v107, v107, v17\n"
    "buffer_store_dwordx4 v[136:139], v0, s[40:43], s61 offen offset:0 sc1\n"
    "s_add_u32 s61, s61, 55296\n"
    "v_sub_f32 v108, v108, v17\n"
    "v_sub_f32 v109, v109, v17\n"
    "v_sub_f32 v110, v110, v17\n"
    "v_sub_f32 v111, v111, v17\n"
    "v_exp_f32 v96, v96\n"
    "v_sub_f32 v80, 0, v16\n"
    "v_sub_f32 v81, 0, v16\n"
    "v_exp_f32 v97, v97\n"
    "v_sub_f32 v82, 0, v16\n"
    "v_sub_f32 v83, 0, v16\n"
    "v_exp_f32 v98, v98\n"
    "v_sub_f32 v84, 0, v16\n"
    "v_sub_f32 v85, 0, v16\n"
    "buffer_store_dwordx4 v[140:143], v0, s[40:43], s61 offen offset:0 sc1\n"
    "s_mul_i32 s61, s75, 55296\n"
    "s_add_u32 s61, s61, s65\n"
    "v_exp_f32 v99, v99\n"
    "v_sub_f32 v86, 0, v16\n"
    "v_sub_f32 v87, 0, v16\n"
    "v_exp_f32 v100, v100\n"
    "v_sub_f32 v88, 0, v17\n"
    "v_sub_f32 v89, 0, v17\n"
    "v_exp_f32 v101, v101\n"
    "v_sub_f32 v90, 0, v17\n"
    "v_sub_f32 v91, 0, v17\n"
    "v_exp_f32 v102, v102\n"
    "v_sub_f32 v92, 0, v17\n"
    "v_sub_f32 v93, 0, v17\n"
    "v_exp_f32 v103, v103\n"
    "buffer_store_dwordx4 v[160:163], v0, s[40:43], s61 offen offset:1024 sc1\n"
    "s_add_u32 s61, s61, 55296\n"
    "v_sub_f32 v94, 0, v17\n"
    "v_sub_f32 v95, 0, v17\n"
    "v_exp_f32 v104, v104\n"
    "v_sub_f32 v224, 0, v18\n"
    "v_sub_f32 v225, 0, v18\n"
    "v_exp_f32 v105, v105\n"
    "v_sub_f32 v226, 0, v18\n"
    "v_sub_f32 v227, 0, v18\n"
    "v_exp_f32 v106, v106\n"
    "v_sub_f32 v228, 0, v18\n"
    "v_sub_f32 v229, 0, v18\n"
    "v_exp_f32 v107, v107\n"
    "v_sub_f32 v230, 0, v18\n"
    "buffer_store_dwordx4 v[164:167], v0, s[40:43], s61 offen offset:1024 sc1\n"
    "s_mul_i32 s61, s76, 55296\n"
    "s_add_u32 s61, s61, s65\n"
    "v_sub_f32 v231, 0, v18\n"
    "v_exp_f32 v108, v108\n"
    "v_sub_f32 v232, 0, v19\n"
    "v_sub_f32 v233, 0, v19\n"
    "v_exp_f32 v109, v109\n"
    "v_sub_f32 v234, 0, v19\n"
    "v_sub_f32 v235, 0, v19\n"
    "v_sub_f32 v236, 0, v19\n"
    "v_sub_f32 v237, 0, v19\n"
    "v_sub_f32 v238, 0, v19\n"
    "v_sub_f32 v239, 0, v19\n"
    "v_lshrrev_b32 v200, 4, v12\n"
    "v_xor_b32 v200, v200, v12\n"
    "buffer_store_dwordx4 v[168:171], v0, s[40:43], s61 offen offset:1024 sc1\n"
    "s_add_u32 s61, s61, 55296\n"
    "v_bfe_u32 v200, v200, 1, 1\n"
    "v_add_u32 v200, -1, v200\n"
    "v_and_b32 v200, 0x3f803f80, v200\n"
    "v_mov_b32 v201, v200\n"
    "v_mov_b32 v202, v200\n"
    "v_mov_b32 v203, v200\n"
    "v_and_b32 v204, 1, v12\n"
    "v_mul_u32_u24 v204, 0xaaaa, v204\n"
    "v_add_u32 v204, 0x4444, v204\n"
    "v_cvt_pk_bf16_f32 v192, v96, v97\n"
    "v_cvt_pk_bf16_f32 v193, v98, v99\n"
    "v_cvt_pk_bf16_f32 v194, v100, v101\n"
    "v_cvt_pk_bf16_f32 v195, v102, v103\n"
    "buffer_store_dwordx4 v[172:175], v0, s[40:43], s61 offen offset:1024 sc1\n"
    "s_lshl_b32 s59, s58, 13\n"
    "v_add_u32 v6, s59, v6\n"
    "v_add_u32 v7, s59, v7\n"
    "v_add_u32 v8, s59, v8\n"
    "v_add_u32 v9, s59, v9\n"
    "s_lshl_b32 s59, s57, 2\n"
    "s_sub_u32 vcc_lo, 27, s59\n"
    "s_and_b32 s60, s57, 3\n"
    "s_mul_i32 s60, s60, -2\n"
    "s_add_u32 s60, s60, 12\n"
    "s_mov_b32 s89, 0\n"
    "s_cmp_eq_u32 s90, 0\n"
    "s_cselect_b32 s65, s60, -1\n"
    "s_waitcnt vmcnt(20)\n"
    "s_waitcnt lgkmcnt(4)\n"
    "s_barrier\n"
    "s_cmp_eq_u32 s57, 7\n"
    "s_cbranch_scc1 ATT_A_ENTRY_%=\n"
    "s_min_u32 s63, s65, vcc_lo\n"
    "s_sub_u32 vcc_lo, vcc_lo, s63\n"
    "s_sub_u32 s65, s65, s63\n"
    "s_branch ATT_B2_%=\n"
    "ATT_MID_%=:\n"
    "s_bitcmp1_b32 s89, 0\n"
    "s_cbranch_scc1 ATT_MID2_%=\n"
    "s_lshl_b32 s59, s57, 5\n"
    "s_add_u32 s59, s59, s77\n"
    "s_lshl2_add_u32 s59, s59, 0x3000\n"
    "s_cmp_eq_u32 s58, 0\n"
    "s_cselect_b32 s59, s59, 0xc0000000\n"
    "buffer_store_dword v13, off, s[40:43], s59 offset:0 sc1\n"
    "s_bitcmp1_b32 %[bid], 0\n"
    "s_cselect_b32 s62, s76, s75\n"
    "s_add_u32 s59, s62, 0\n"
    "s_sub_u32 s61, s59, s28\n"
    "s_and_b32 s61, s61, 3\n"
    "s_and_b32 s59, s59, 4\n"
    "s_or_b32 s59, s59, s61\n"
    "s_lshl_b32 s59, s59, 11\n"
    "v_add_u32 v20, s59, v0\n"
    "s_add_u32 s59, s62, 1\n"
    "s_sub_u32 s61, s59, s28\n"
    "s_and_b32 s61, s61, 3\n"
    "s_and_b32 s59, s59, 4\n"
    "s_or_b32 s59, s59, s61\n"
    "s_lshl_b32 s59, s59, 11\n"
    "v_add_u32 v21, s59, v0\n"
    "s_getpc_b64 s[38:39]\n"
    "PCB_%=:\n"
    "s_lshr_b32 s59, %[bid], 3\n"
    "s_lshl2_add_u32 s59, s59, %[wid]\n"
    "s_mulk_i32 s59, 256\n"
    "v_lshrrev_b32 v27, 5, v12\n"
    "v_lshlrev_b32 v27, 7, v27\n"
    "s_add_u32 s59, s59, ATT_BLAST_%=-PCB_%=\n"
    "s_min_u32 s59, s59, MAXPASS_%=-PCB_%=-256\n"
    "s_add_u32 s38, s38, s59\n"
    "s_addc_u32 s39, s39, 0\n"
    "global_load_dword v27, v27, s[38:39]\n"
    "s_cmp_lt_u32 s57, 4\n"
    "s_cbranch_scc1 ATT_MID4_%=\n"
    "ATT_MIDD_%=:\n"
    "s_lshl_b32 s60, s58, 13\n"
    "s_add_u32 m0, s60, 0x20000\n"
    "s_nop 0\n"
    "buffer_load_dwordx4 v30, s[48:51], s52 offen offset:0 nt lds\n"
    "s_addk_i32 s52, 0x2400\n"
    "s_nop 0\n"
    "buffer_load_dwordx4 v30, s[48:51], s52 offen offset:1024 nt lds\n"
    "s_addk_i32 s52, 0x2400\n"
    "s_nop 0\n"
    "buffer_load_dwordx4 v30, s[48:51], s52 offen offset:2048 nt lds\n"
    "s_addk_i32 s52, 0x2400\n"
    "s_nop 0\n"
    "buffer_load_dwordx4 v30, s[48:51], s52 offen offset:3072 nt lds\n"
    "s_addk_i32 s52, 0x3400\n"
    "s_lshl_b32 s60, s58, 13\n"
    "s_add_u32 m0, s60, 0x21000\n"
    "s_nop 0\n"
    "buffer_load_dwordx4 v30, s[48:51], s52 offen offset:0 nt lds\n"
    "s_addk_i32 s52, 0x2400\n"
    "s_nop 0\n"
    "buffer_load_dwordx4 v30, s[48:51], s52 offen offset:1024 nt lds\n"
    "s_addk_i32 s52, 0x2400\n"
    "s_nop 0\n"
    "buffer_load_dwordx4 v30, s[48:51], s52 offen offset:2048 nt lds\n"
    "s_addk_i32 s52, 0x2400\n"
    "s_nop 0\n"
    "buffer_load_dwordx4 v30, s[48:51], s52 offen offset:3072 nt lds\n"
    "s_or_b32 s89, s89, 1\n"
    "s_mul_i32 s65, s57, 0\n"
    "s_add_u32 s65, s65, 3\n"
    "s_branch ATT_RES_%=\n"
    "ATT_MID2_%=:\n"
    "s_bitcmp1_b32 s89, 3\n"
    "s_cbranch_scc1 ATT_MID4_%=\n"
    "ds_read_b64_tr_b16 v[128:129], v6 offset:0\n"
    "ds_read_b64_tr_b16 v[130:131], v6 offset:2048\n"
    "ds_read_b64_tr_b16 v[132:133], v6 offset:4096\n"
    "ds_read_b64_tr_b16 v[134:135], v6 offset:6144\n"
    "ds_read_b64_tr_b16 v[136:137], v7 offset:0\n"
    "ds_read_b64_tr_b16 v[138:139], v7 offset:2048\n"
    "ds_read_b64_tr_b16 v[140:141], v7 offset:4096\n"
    "ds_read_b64_tr_b16 v[142:143], v7 offset:6144\n"
    "ds_read_b64_tr_b16 v[144:145], v8 offset:0\n"
    "ds_read_b64_tr_b16 v[146:147], v8 offset:2048\n"
    "ds_read_b64_tr_b16 v[148:149], v8 offset:4096\n"
    "ds_read_b64_tr_b16 v[150:151], v8 offset:6144\n"
    "ds_read_b64_tr_b16 v[152:153], v9 offset:0\n"
    "ds_read_b64_tr_b16 v[154:155], v9 offset:2048\n"
    "ds_read_b64_tr_b16 v[156:157], v9 offset:4096\n"
    "ds_read_b64_tr_b16 v[158:159], v9 offset:6144\n"
    "s_mul_i32 s61, s54, 26\n"
    "s_lshr_b32 s61, s61, 8\n"
    "s_mul_i32 s61, s61, 10\n"
    "s_add_u32 s61, s61, s54\n"
    "s_lshl_b32 s61, s61, 10\n"
    "s_add_u32 s61, s61, s53\n"
    "s_add_u32 s54, s54, 1\n"
    "s_waitcnt lgkmcnt(12)\n"
    "buffer_store_dwordx4 v[128:131], v0, s[40:43], s61 offen sc1\n"
    "s_addk_i32 s61, 0x2800\n"
    "buffer_store_dwordx4 v[132:135], v0, s[40:43], s61 offen sc1\n"
    "s_mul_i32 s61, s54, 26\n"
    "s_lshr_b32 s61, s61, 8\n"
    "s_mul_i32 s61, s61, 10\n"
    "s_add_u32 s61, s61, s54\n"
    "s_lshl_b32 s61, s61, 10\n"
    "s_add_u32 s61, s61, s53\n"
    "s_add_u32 s54, s54, 1\n"
    "s_waitcnt lgkmcnt(8)\n"
    "buffer_store_dwordx4 v[136:139], v0, s[40:43], s61 offen sc1\n"
    "s_addk_i32 s61, 0x2800\n"
    "buffer_store_dwordx4 v[140:143], v0, s[40:43], s61 offen sc1\n"
    "s_mul_i32 s61, s54, 26\n"
    "s_lshr_b32 s61, s61, 8\n"
    "s_mul_i32 s61, s61, 10\n"
    "s_add_u32 s61, s61, s54\n"
    "s_lshl_b32 s61, s61, 10\n"
    "s_add_u32 s61, s61, s53\n"
    "s_add_u32 s54, s54, 1\n"
    "s_waitcnt lgkmcnt(4)\n"
    "buffer_store_dwordx4 v[144:147], v0, s[40:43], s61 offen sc1\n"
    "s_addk_i32 s61, 0x2800\n"
    "buffer_store_dwordx4 v[148:151], v0, s[40:43], s61 offen sc1\n"
    "s_mul_i32 s61, s54, 26\n"
    "s_lshr_b32 s61, s61, 8\n"
    "s_mul_i32 s61, s61, 10\n"
    "s_add_u32 s61, s61, s54\n"
    "s_lshl_b32 s61, s61, 10\n"
    "s_add_u32 s61, s61, s53\n"
    "s_add_u32 s54, s54, 1\n"
    "s_waitcnt lgkmcnt(0)\n"
    "buffer_store_dwordx4 v[152:155], v0, s[40:43], s61 offen sc1\n"
    "s_addk_i32 s61, 0x2800\n"
    "buffer_store_dwordx4 v[156:159], v0, s[40:43], s61 offen sc1\n"
    "s_add_u32 s89, s89, 4\n"
    "s_mov_b32 s65, 3\n"
    "s_bitcmp1_b32 s89, 3\n"
    "s_cbranch_scc1 ATT_RES_%=\n"
    "s_and_b32 s59, %[bid], 1\n"
    "s_lshl_b32 s59, s59, 4\n"
    "s_add_u32 s59, s59, 16\n"
    "s_lshr_b32 s60, %[bid], 3\n"
    "s_add_u32 s59, s59, s60\n"
    "s_lshl2_add_u32 s59, s59, s58\n"
    "s_mul_i32 s60, s59, 205\n"
    "s_lshr_b32 s60, s60, 11\n"
    "s_mul_i32 s61, s60, 10\n"
    "s_sub_u32 s54, s59, s61\n"
    "s_mul_i32 s61, s28, 23\n"
    "s_add_u32 s61, s61, s60\n"
    "s_mul_i32 s53, s61, 81920\n"
    "s_add_u32 s53, s53, 0x51c0000\n"
    "s_mul_i32 s59, s28, 7\n"
    "s_lshl_b32 s61, s28, 4\n"
    "s_sub_u32 s61, s61, 7\n"
    "s_cmp_lt_u32 s60, 7\n"
    "s_cselect_b64 s[48:49], %[wd], %[wo]\n"
    "s_cselect_b32 s59, s59, s61\n"
    "s_mov_b32 s61, 0x500000\n"
    "s_cselect_b32 s50, 0x21c000, s61\n"
    "s_add_u32 s59, s59, s60\n"
    "s_mul_i32 s52, s59, 81920\n"
    "s_lshl_b32 s59, s54, 7\n"
    "s_add_u32 s52, s52, s59\n"
    "s_cmp_ge_u32 s60, 23\n"
    "s_cselect_b32 s52, 0xc0000000, s52\n"
    "s_cselect_b32 s53, 0xc0000000, s53\n"
    "s_lshl_b32 s54, s54, 2\n"
    "s_branch ATT_MIDD_%=\n"
    "ATT_MID4_%=:\n"
    "s_and_b32 s59, %[bid], 7\n"
    "s_lshl_b32 s59, s59, 5\n"
    "s_lshr_b32 s60, %[bid], 3\n"
    "s_add_u32 s59, s59, s60\n"
    "s_lshl2_add_u32 s59, s59, 0x3800\n"
    "s_cmp_eq_u32 s58, 0\n"
    "s_cselect_b32 s59, s59, 0xc0000000\n"
    "buffer_store_dword v13, off, s[40:43], s59 offset:0 sc1\n"
    "s_mov_b32 s65, -1\n"
    "ATT_RES_%=:\n"
    "s_bitcmp1_b32 s89, 1\n"
    "s_cbranch_scc1 ATT_A_NEXT_%=\n"
    "s_min_u32 s63, s65, vcc_lo\n"
    "s_sub_u32 vcc_lo, vcc_lo, s63\n"
    "s_sub_u32 s65, s65, s63\n"
    "s_sub_u32 s63, s63, 1\n"
    "s_cbranch_scc1 ATT_BLAST_%=\n"
    "ATT_B1_%=:\n"
    "v_mfma_f32_16x16x32_bf16 v[96:99], a[160:163], v[216:219], v[80:83]\n"
    "s_mov_b32 s83, s84\n"
    "ds_read_b128 a[192:195], v5 offset:0\n"
    "v_cvt_pk_bf16_f32 v196, v104, v105\n"
    "v_mfma_f32_16x16x32_bf16 v[96:99], a[164:167], v[40:43], v[96:99]\n"
    "s_max_u32 m0, s64, s79\n"
    "v_exp_f32 v110, v110\n"
    "v_mfma_f32_16x16x32_bf16 v[96:99], a[168:171], v[56:59], v[96:99]\n"
    "ds_read_b128 a[196:199], v5 offset:1024\n"
    "v_cvt_pk_bf16_f32 v197, v106, v107\n"
    "v_mfma_f32_16x16x32_bf16 v[96:99], a[172:175], v[72:75], v[96:99]\n"
    "s_waitcnt lgkmcnt(2)\n"
    "v_exp_f32 v111, v111\n"
    "v_mfma_f32_16x16x32_bf16 v[100:103], a[176:179], v[216:219], v[84:87]\n"
    "s_addk_i32 s64, 0x4000\n"
    "ds_read_b128 a[200:203], v5 offset:2048\n"
    "v_cvt_pk_bf16_f32 v198, v108, v109\n"
    "v_mfma_f32_16x16x32_bf16 v[100:103], a[180:183], v[40:43], v[100:103]\n"
    "buffer_load_dwordx4 v0, s[40:43], s83 offen offset:0 sc1 lds\n"
    "v_mfma_f32_16x16x32_bf16 v[100:103], a[184:187], v[56:59], v[100:103]\n"
    "s_and_b32 s64, s64, 0x1ffff\n"
    "ds_read_b128 a[204:207], v5 offset:3072\n"
    "v_cvt_pk_bf16_f32 v199, v110, v111\n"
    "v_mfma_f32_16x16x32_bf16 v[100:103], a[188:191], v[72:75], v[100:103]\n"
    "s_add_u32 s80, s84, 0x4000\n"
    "v_exp_f32 v96, v96\n"
    "v_mfma_f32_16x16x32_bf16 v[104:107], a[160:163], v[220:223], v[88:91]\n"
    "s_cmp_eq_u32 s80, s85\n"
    "ds_read_b128 a[208:211], v5 offset:4096\n"
    "v_add_u32 v4, 0x4000, v4\n"
    "v_mfma_f32_16x16x32_bf16 v[104:107], a[164:167], v[44:47], v[104:107]\n"
    "s_cselect_b32 s84, s27, s80\n"
    "v_exp_f32 v97, v97\n"
    "v_mfma_f32_16x16x32_bf16 v[104:107], a[168:171], v[60:63], v[104:107]\n"
    "s_cmp_eq_u32 s80, s26\n"
    "ds_read_b128 a[212:215], v5 offset:5120\n"
    "v_and_b32 v4, 0x1ffff, v4\n"
    "v_mfma_f32_16x16x32_bf16 v[104:107], a[172:175], v[76:79], v[104:107]\n"
    "s_cselect_b32 s84, s88, s84\n"
    "v_exp_f32 v98, v98\n"
    "v_mfma_f32_16x16x32_bf16 v[108:111], a[176:179], v[220:223], v[92:95]\n"
    "s_cmp_eq_u32 s80, s86\n"
    "ds_read_b128 a[216:219], v5 offset:6144\n"
    "v_exp_f32 v99, v99\n"
    "v_mfma_f32_16x16x32_bf16 v[108:111], a[180:183], v[44:47], v[108:111]\n"
    "buffer_load_dwordx4 v0, s[40:43], s83 offen offset:1024 sc1 lds\n"
    "v_mfma_f32_16x16x32_bf16 v[108:111], a[184:187], v[60:63], v[108:111]\n"
    "s_cselect_b32 s84, s78, s84\n"
    "ds_read_b128 a[220:223], v5 offset:7168\n"
    "v_add_u32 v5, 0x4000, v5\n"
    "v_mfma_f32_16x16x32_bf16 v[108:111], a[188:191], v[76:79], v[108:111]\n"
    "s_waitcnt lgkmcnt(6)\n"
    "v_exp_f32 v100, v100\n"
    "v_mfma_f32_16x16x32_bf16 a[0:3], a[192:195], v[192:195], a[0:3]\n"
    "s_cmp_eq_u32 s80, s87\n"
    "ds_read_b128 a[160:163], v4 offset:8192\n"
    "v_and_b32 v5, 0x1ffff, v5\n"
    "v_mfma_f32_16x16x32_bf16 a[8:11], a[196:199], v[192:195], a[8:11]\n"
    "s_cselect_b32 s84, 0xc0000000, s84\n"
    "v_exp_f32 v101, v101\n"
    "v_smfmac_f32_16x16x64_bf16 v[240:243], v[200:203], v[192:199], v204\n"
    "s_waitcnt lgkmcnt(4)\n"
    "ds_read_b128 a[164:167], v4 offset:9216\n"
    "v_mfma_f32_16x16x32_bf16 a[16:19], a[200:203], v[192:195], a[16:19]\n"
    "s_cselect_b32 s79, s55, s79\n"
    "v_exp_f32 v102, v102\n"
    "v_mfma_f32_16x16x32_bf16 a[24:27], a[204:207], v[192:195], a[24:27]\n"
    "ds_read_b128 a[168:171], v4 offset:10240\n"
    "v_exp_f32 v103, v103\n"
    "v_mfma_f32_16x16x32_bf16 a[32:35], a[208:211], v[192:195], a[32:35]\n"
    "s_waitcnt lgkmcnt(3)\n"
    "v_exp_f32 v104, v104\n"
    "v_mfma_f32_16x16x32_bf16 a[40:43], a[212:215], v[192:195], a[40:43]\n"
    "ds_read_b128 a[172:175], v4 offset:11264\n"
    "v_exp_f32 v105, v105\n"
    "v_mfma_f32_16x16x32_bf16 a[48:51], a[216:219], v[192:195], a[48:51]\n"
    "buffer_load_dwordx4 v0, s[40:43], s83 offen offset:2048 sc1 lds\n"
    "v_mfma_f32_16x16x32_bf16 a[56:59], a[220:223], v[192:195], a[56:59]\n"
    "ds_read_b128 a[176:179], v4 offset:12288\n"
    "v_cvt_pk_bf16_f32 v192, v96, v97\n"
    "v_mfma_f32_16x16x32_bf16 a[4:7], a[192:195], v[196:199], a[4:7]\n"
    "v_exp_f32 v106, v106\n"
    "v_mfma_f32_16x16x32_bf16 a[12:15], a[196:199], v[196:199], a[12:15]\n"
    "ds_read_b128 a[180:183], v4 offset:13312\n"
    "v_cvt_pk_bf16_f32 v193, v98, v99\n"
    "v_mfma_f32_16x16x32_bf16 a[20:23], a[200:203], v[196:199], a[20:23]\n"
    "s_waitcnt vmcnt(11)\n"
    "v_exp_f32 v107, v107\n"
    "v_mfma_f32_16x16x32_bf16 a[28:31], a[204:207], v[196:199], a[28:31]\n"
    "s_barrier\n"
    "ds_read_b128 a[184:187], v4 offset:14336\n"
    "v_cvt_pk_bf16_f32 v194, v100, v101\n"
    "v_mfma_f32_16x16x32_bf16 a[36:39], a[208:211], v[196:199], a[36:39]\n"
    "v_exp_f32 v108, v108\n"
    "v_mfma_f32_16x16x32_bf16 a[44:47], a[212:215], v[196:199], a[44:47]\n"
    "ds_read_b128 a[188:191], v4 offset:15360\n"
    "v_cvt_pk_bf16_f32 v195, v102, v103\n"
    "v_mfma_f32_16x16x32_bf16 a[52:55], a[216:219], v[196:199], a[52:55]\n"
    "s_waitcnt lgkmcnt(4)\n"
    "v_exp_f32 v109, v109\n"
    "v_mfma_f32_16x16x32_bf16 a[60:63], a[220:223], v[196:199], a[60:63]\n"
    "buffer_load_dwordx4 v0, s[40:43], s83 offen offset:3072 sc1 lds\n"
    "ATT_B2_%=:\n"
    "v_mfma_f32_16x16x32_bf16 v[96:99], a[160:163], v[216:219], v[80:83]\n"
    "s_mov_b32 s83, s84\n"
    "ds_read_b128 a[192:195], v5 offset:0\n"
    "v_cvt_pk_bf16_f32 v196, v104, v105\n"
    "v_mfma_f32_16x16x32_bf16 v[96:99], a[164:167], v[40:43], v[96:99]\n"
    "s_max_u32 m0, s64, s79\n"
    "v_exp_f32 v110, v110\n"
    "v_mfma_f32_16x16x32_bf16 v[96:99], a[168:171], v[56:59], v[96:99]\n"
    "ds_read_b128 a[196:199], v5 offset:1024\n"
    "v_cvt_pk_bf16_f32 v197, v106, v107\n"
    "v_mfma_f32_16x16x32_bf16 v[96:99], a[172:175], v[72:75], v[96:99]\n"
    "s_waitcnt lgkmcnt(2)\n"
    "v_exp_f32 v111, v111\n"
    "v_mfma_f32_16x16x32_bf16 v[100:103], a[176:179], v[216:219], v[84:87]\n"
    "s_addk_i32 s64, 0x4000\n"
    "ds_read_b128 a[200:203], v5 offset:2048\n"
    "v_cvt_pk_bf16_f32 v198, v108, v109\n"
    "v_mfma_f32_16x16x32_bf16 v[100:103], a[180:183], v[40:43], v[100:103]\n"
    "buffer_load_dwordx4 v0, s[40:43], s83 offen offset:0 sc1 lds\n"
    "v_mfma_f32_16x16x32_bf16 v[100:103], a[184:187], v[56:59], v[100:103]\n"
    "s_and_b32 s64, s64, 0x1ffff\n"
    "ds_read_b128 a[204:207], v5 offset:3072\n"
    "v_cvt_pk_bf16_f32 v199, v110, v111\n"
    "v_mfma_f32_16x16x32_bf16 v[100:103], a[188:191], v[72:75], v[100:103]\n"
    "s_add_u32 s80, s84, 0x4000\n"
    "v_exp_f32 v96, v96\n"
    "v_mfma_f32_16x16x32_bf16 v[104:107], a[160:163], v[220:223], v[88:91]\n"
    "s_cmp_eq_u32 s80, s85\n"
    "ds_read_b128 a[208:211], v5 offset:4096\n"
    "v_add_u32 v4, 0x4000, v4\n"
    "v_mfma_f32_16x16x32_bf16 v[104:107], a[164:167], v[44:47], v[104:107]\n"
    "s_cselect_b32 s84, s27, s80\n"
    "v_exp_f32 v97, v97\n"
    "v_mfma_f32_16x16x32_bf16 v[104:107], a[168:171], v[60:63], v[104:107]\n"
    "s_cmp_eq_u32 s80, s26\n"
    "ds_read_b128 a[212:215], v5 offset:5120\n"
    "v_and_b32 v4, 0x1ffff, v4\n"
    "v_mfma_f32_16x16x32_bf16 v[104:107], a[172:175], v[76:79], v[104:107]\n"
    "s_cselect_b32 s84, s88, s84\n"
    "v_exp_f32 v98, v98\n"
    "v_mfma_f32_16x16x32_bf16 v[108:111], a[176:179], v[220:223], v[92:95]\n"
    "s_cmp_eq_u32 s80, s86\n"
    "ds_read_b128 a[216:219], v5 offset:6144\n"
    "v_exp_f32 v99, v99\n"
    "v_mfma_f32_16x16x32_bf16 v[108:111], a[180:183], v[44:47], v[108:111]\n"
    "buffer_load_dwordx4 v0, s[40:43], s83 offen offset:1024 sc1 lds\n"
    "v_mfma_f32_16x16x32_bf16 v[108:111], a[184:187], v[60:63], v[108:111]\n"
    "s_cselect_b32 s84, s78, s84\n"
    "ds_read_b128 a[220:223], v5 offset:7168\n"
    "v_add_u32 v5, 0x4000, v5\n"
    "v_mfma_f32_16x16x32_bf16 v[108:111], a[188:191], v[76:79], v[108:111]\n"
    "s_waitcnt lgkmcnt(6)\n"
    "v_exp_f32 v100, v100\n"
    "v_mfma_f32_16x16x32_bf16 a[0:3], a[192:195], v[192:195], a[0:3]\n"
    "s_cmp_eq_u32 s80, s87\n"
    "ds_read_b128 a[160:163], v4 offset:8192\n"
    "v_and_b32 v5, 0x1ffff, v5\n"
    "v_mfma_f32_16x16x32_bf16 a[8:11], a[196:199], v[192:195], a[8:11]\n"
    "s_cselect_b32 s84, 0xc0000000, s84\n"
    "v_exp_f32 v101, v101\n"
    "v_smfmac_f32_16x16x64_bf16 v[240:243], v[200:203], v[192:199], v204\n"
    "s_waitcnt lgkmcnt(4)\n"
    "ds_read_b128 a[164:167], v4 offset:9216\n"
    "v_mfma_f32_16x16x32_bf16 a[16:19], a[200:203], v[192:195], a[16:19]\n"
    "s_cselect_b32 s79, s55, s79\n"
    "v_exp_f32 v102, v102\n"
    "v_mfma_f32_16x16x32_bf16 a[24:27], a[204:207], v[192:195], a[24:27]\n"
    "ds_read_b128 a[168:171], v4 offset:10240\n"
    "v_exp_f32 v103, v103\n"
    "v_mfma_f32_16x16x32_bf16 a[32:35], a[208:211], v[192:195], a[32:35]\n"
    "s_waitcnt lgkmcnt(3)\n"
    "v_exp_f32 v104, v104\n"
    "v_mfma_f32_16x16x32_bf16 a[40:43], a[212:215], v[192:195], a[40:43]\n"
    "ds_read_b128 a[172:175], v4 offset:11264\n"
    "v_exp_f32 v105, v105\n"
    "v_mfma_f32_16x16x32_bf16 a[48:51], a[216:219], v[192:195], a[48:51]\n"
    "buffer_load_dwordx4 v0, s[40:43], s83 offen offset:2048 sc1 lds\n"
    "v_mfma_f32_16x16x32_bf16 a[56:59], a[220:223], v[192:195], a[56:59]\n"
    "ds_read_b128 a[176:179], v4 offset:12288\n"
    "v_cvt_pk_bf16_f32 v192, v96, v97\n"
    "v_mfma_f32_16x16x32_bf16 a[4:7], a[192:195], v[196:199], a[4:7]\n"
    "v_exp_f32 v106, v106\n"
    "v_mfma_f32_16x16x32_bf16 a[12:15], a[196:199], v[196:199], a[12:15]\n"
    "ds_read_b128 a[180:183], v4 offset:13312\n"
    "v_cvt_pk_bf16_f32 v193, v98, v99\n"
    "v_mfma_f32_16x16x32_bf16 a[20:23], a[200:203], v[196:199], a[20:23]\n"
    "v_exp_f32 v107, v107\n"
    "v_mfma_f32_16x16x32_bf16 a[28:31], a[204:207], v[196:199], a[28:31]\n"
    "ds_read_b128 a[184:187], v4 offset:14336\n"
    "v_cvt_pk_bf16_f32 v194, v100, v101\n"
    "v_mfma_f32_16x16x32_bf16 a[36:39], a[208:211], v[196:199], a[36:39]\n"
    "s_sub_u32 s63, s63, 1\n"
    "v_exp_f32 v108, v108\n"
    "v_mfma_f32_16x16x32_bf16 a[44:47], a[212:215], v[196:199], a[44:47]\n"
    "ds_read_b128 a[188:191], v4 offset:15360\n"
    "v_cvt_pk_bf16_f32 v195, v102, v103\n"
    "v_mfma_f32_16x16x32_bf16 a[52:55], a[216:219], v[196:199], a[52:55]\n"
    "s_waitcnt lgkmcnt(4)\n"
    "v_exp_f32 v109, v109\n"
    "v_mfma_f32_16x16x32_bf16 a[60:63], a[220:223], v[196:199], a[60:63]\n"
    "buffer_load_dwordx4 v0, s[40:43], s83 offen offset:3072 sc1 lds\n"
    "s_cbranch_scc0 ATT_B1_%=\n"
    "s_cmp_eq_u32 s65, 0\n"
    "s_cbranch_scc1 ATT_MID_%=\n"
    "ATT_BLAST_%=:\n"
    "v_mfma_f32_16x16x32_bf16 v[96:99], a[160:163], v[216:219], v[80:83]\n"
    "s_mov_b32 s83, s84\n"
    "ds_read_b128 a[192:195], v5 offset:0\n"
    "v_cvt_pk_bf16_f32 v196, v104, v105\n"
    "v_mfma_f32_16x16x32_bf16 v[96:99], a[164:167], v[40:43], v[96:99]\n"
    "s_max_u32 m0, s64, s79\n"
    "v_exp_f32 v110, v110\n"
    "v_mfma_f32_16x16x32_bf16 v[96:99], a[168:171], v[56:59], v[96:99]\n"
    "ds_read_b128 a[196:199], v5 offset:1024\n"
    "v_cvt_pk_bf16_f32 v197, v106, v107\n"
    "v_mfma_f32_16x16x32_bf16 v[96:99], a[172:175], v[72:75], v[96:99]\n"
    "s_waitcnt lgkmcnt(2)\n"
    "v_exp_f32 v111, v111\n"
    "v_mfma_f32_16x16x32_bf16 v[100:103], a[176:179], v[216:219], v[84:87]\n"
    "s_addk_i32 s64, 0x4000\n"
    "ds_read_b128 a[200:203], v5 offset:2048\n"
    "v_cvt_pk_bf16_f32 v198, v108, v109\n"
    "v_mfma_f32_16x16x32_bf16 v[100:103], a[180:183], v[40:43], v[100:103]\n"
    "buffer_load_dwordx4 v0, s[40:43], s83 offen offset:0 sc1 lds\n"
    "v_mfma_f32_16x16x32_bf16 v[100:103], a[184:187], v[56:59], v[100:103]\n"
    "s_and_b32 s64, s64, 0x1ffff\n"
    "ds_read_b128 a[204:207], v5 offset:3072\n"
    "v_cvt_pk_bf16_f32 v199, v110, v111\n"
    "v_mfma_f32_16x16x32_bf16 v[100:103], a[188:191], v[72:75], v[100:103]\n"
    "s_add_u32 s80, s84, 0x4000\n"
    "v_exp_f32 v96, v96\n"
    "v_mfma_f32_16x16x32_bf16 v[104:107], a[160:163], v[220:223], v[88:91]\n"
    "s_cmp_eq_u32 s80, s85\n"
    "ds_read_b128 a[208:211], v5 offset:4096\n"
    "v_mfma_f32_16x16x32_bf16 v[104:107], a[164:167], v[44:47], v[104:107]\n"
    "s_cselect_b32 s84, s27, s80\n"
    "v_exp_f32 v97, v97\n"
    "v_mfma_f32_16x16x32_bf16 v[104:107], a[168:171], v[60:63], v[104:107]\n"
    "s_cmp_eq_u32 s80, s26\n"
    "ds_read_b128 a[212:215], v5 offset:5120\n"
    "v_mfma_f32_16x16x32_bf16 v[104:107], a[172:175], v[76:79], v[104:107]\n"
    "s_cselect_b32 s84, s88, s84\n"
    "v_exp_f32 v98, v98\n"
    "v_mfma_f32_16x16x32_bf16 v[108:111], a[176:179], v[220:223], v[92:95]\n"
    "s_cmp_eq_u32 s80, s86\n"
    "ds_read_b128 a[216:219], v5 offset:6144\n"
    "v_exp_f32 v99, v99\n"
    "v_mfma_f32_16x16x32_bf16 v[108:111], a[180:183], v[44:47], v[108:111]\n"
    "buffer_load_dwordx4 v0, s[40:43], s83 offen offset:1024 sc1 lds\n"
    "v_mfma_f32_16x16x32_bf16 v[108:111], a[184:187], v[60:63], v[108:111]\n"
    "s_cselect_b32 s84, s78, s84\n"
    "ds_read_b128 a[220:223], v5 offset:7168\n"
    "v_add_u32 v5, 0x4000, v5\n"
    "v_mfma_f32_16x16x32_bf16 v[108:111], a[188:191], v[76:79], v[108:111]\n"
    "s_waitcnt lgkmcnt(6)\n"
    "v_exp_f32 v100, v100\n"
    "v_mfma_f32_16x16x32_bf16 a[0:3], a[192:195], v[192:195], a[0:3]\n"
    "s_cmp_eq_u32 s80, s87\n"
    "v_and_b32 v5, 0x1ffff, v5\n"
    "v_mfma_f32_16x16x32_bf16 a[8:11], a[196:199], v[192:195], a[8:11]\n"
    "s_waitcnt lgkmcnt(3)\n"
    "v_exp_f32 v101, v101\n"
    "v_smfmac_f32_16x16x64_bf16 v[240:243], v[200:203], v[192:199], v204\n"
    "buffer_load_dwordx4 v0, s[40:43], s83 offen offset:2048 sc1 lds\n"
    "v_mfma_f32_16x16x32_bf16 a[16:19], a[200:203], v[192:195], a[16:19]\n"
    "s_cselect_b32 s84, 0xc0000000, s84\n"
    "v_exp_f32 v102, v102\n"
    "v_mfma_f32_16x16x32_bf16 a[24:27], a[204:207], v[192:195], a[24:27]\n"
    "s_cselect_b32 s79, s55, s79\n"
    "v_mfma_f32_16x16x32_bf16 a[32:35], a[208:211], v[192:195], a[32:35]\n"
    "s_waitcnt lgkmcnt(0)\n"
    "v_exp_f32 v103, v103\n"
    "v_mfma_f32_16x16x32_bf16 a[40:43], a[212:215], v[192:195], a[40:43]\n"
    "buffer_load_dwordx4 v0, s[40:43], s83 offen offset:3072 sc1 lds\n"
    "v_mfma_f32_16x16x32_bf16 a[48:51], a[216:219], v[192:195], a[48:51]\n"
    "v_exp_f32 v104, v104\n"
    "v_mfma_f32_16x16x32_bf16 a[56:59], a[220:223], v[192:195], a[56:59]\n"
    "v_cvt_pk_bf16_f32 v192, v96, v97\n"
    "v_mfma_f32_16x16x32_bf16 a[4:7], a[192:195], v[196:199], a[4:7]\n"
    "v_exp_f32 v105, v105\n"
    "v_mfma_f32_16x16x32_bf16 a[12:15], a[196:199], v[196:199], a[12:15]\n"
    "v_cvt_pk_bf16_f32 v193, v98, v99\n"
    "v_mfma_f32_16x16x32_bf16 a[20:23], a[200:203], v[196:199], a[20:23]\n"
    "s_waitcnt vmcnt(8)\n"
    "v_exp_f32 v106, v106\n"
    "v_mfma_f32_16x16x32_bf16 a[28:31], a[204:207], v[196:199], a[28:31]\n"
    "s_barrier\n"
    "v_cvt_pk_bf16_f32 v194, v100, v101\n"
    "v_mfma_f32_16x16x32_bf16 a[36:39], a[208:211], v[196:199], a[36:39]\n"
    "v_exp_f32 v107, v107\n"
    "v_mfma_f32_16x16x32_bf16 a[44:47], a[212:215], v[196:199], a[44:47]\n"
    "v_cvt_pk_bf16_f32 v195, v102, v103\n"
    "v_mfma_f32_16x16x32_bf16 a[52:55], a[216:219], v[196:199], a[52:55]\n"
    "v_exp_f32 v108, v108\n"
    "v_mfma_f32_16x16x32_bf16 a[60:63], a[220:223], v[196:199], a[60:63]\n"
    "v_exp_f32 v109, v109\n"
    "ATT_A_ENTRY_%=:\n"
    "s_lshl_b32 vcc_lo, s57, 2\n"
    "s_or_b32 s89, s89, 2\n"
    "ATT_A_NEXT_%=:\n"
    "s_min_u32 s63, s65, vcc_lo\n"
    "s_sub_u32 vcc_lo, vcc_lo, s63\n"
    "s_sub_u32 s65, s65, s63\n"
    "s_sub_u32 s63, s63, 1\n"
    "s_cbranch_scc1 ATT_AT_ENTRY_%=\n"
    "ATT_A_%=:\n"
    "v_mfma_f32_16x16x32_bf16 v[96:99], a[160:163], v[208:211], v[224:227]\n"
    "ds_read_b128 a[192:195], v5 offset:0\n"
    "v_cvt_pk_bf16_f32 v196, v104, v105\n"
    "v_mfma_f32_16x16x32_bf16 v[96:99], a[164:167], v[32:35], v[96:99]\n"
    "v_exp_f32 v110, v110\n"
    "v_mfma_f32_16x16x32_bf16 v[96:99], a[168:171], v[48:51], v[96:99]\n"
    "ds_read_b128 a[196:199], v5 offset:1024\n"
    "v_cvt_pk_bf16_f32 v197, v106, v107\n"
    "v_mfma_f32_16x16x32_bf16 v[96:99], a[172:175], v[64:67], v[96:99]\n"
    "v_exp_f32 v111, v111\n"
    "v_mfma_f32_16x16x32_bf16 v[100:103], a[176:179], v[208:211], v[228:231]\n"
    "ds_read_b128 a[200:203], v5 offset:2048\n"
    "v_cvt_pk_bf16_f32 v198, v108, v109\n"
    "v_mfma_f32_16x16x32_bf16 v[100:103], a[180:183], v[32:35], v[100:103]\n"
    "v_mfma_f32_16x16x32_bf16 v[100:103], a[184:187], v[48:51], v[100:103]\n"
    "ds_read_b128 a[204:207], v5 offset:3072\n"
    "v_cvt_pk_bf16_f32 v199, v110, v111\n"
    "v_mfma_f32_16x16x32_bf16 v[100:103], a[188:191], v[64:67], v[100:103]\n"
    "v_exp_f32 v96, v96\n"
    "v_mfma_f32_16x16x32_bf16 v[104:107], a[160:163], v[212:215], v[232:235]\n"
    "ds_read_b128 a[208:211], v5 offset:4096\n"
    "v_add_u32 v4, 0x4000, v4\n"
    "v_mfma_f32_16x16x32_bf16 v[104:107], a[164:167], v[36:39], v[104:107]\n"
    "v_exp_f32 v97, v97\n"
    "v_mfma_f32_16x16x32_bf16 v[104:107], a[168:171], v[52:55], v[104:107]\n"
    "ds_read_b128 a[212:215], v5 offset:5120\n"
    "v_and_b32 v4, 0x1ffff, v4\n"
    "v_mfma_f32_16x16x32_bf16 v[104:107], a[172:175], v[68:71], v[104:107]\n"
    "v_exp_f32 v98, v98\n"
    "v_mfma_f32_16x16x32_bf16 v[108:111], a[176:179], v[212:215], v[236:239]\n"
    "ds_read_b128 a[216:219], v5 offset:6144\n"
    "v_mfma_f32_16x16x32_bf16 v[108:111], a[180:183], v[36:39], v[108:111]\n"
    "v_exp_f32 v99, v99\n"
    "v_mfma_f32_16x16x32_bf16 v[108:111], a[184:187], v[52:55], v[108:111]\n"
    "ds_read_b128 a[220:223], v5 offset:7168\n"
    "v_add_u32 v5, 0x4000, v5\n"
    "v_mfma_f32_16x16x32_bf16 v[108:111], a[188:191], v[68:71], v[108:111]\n"
    "s_waitcnt lgkmcnt(6)\n"
    "v_exp_f32 v100, v100\n"
    "v_mfma_f32_16x16x32_bf16 a[0:3], a[192:195], v[192:195], a[0:3]\n"
    "ds_read_b128 a[160:163], v4 offset:8192\n"
    "v_and_b32 v5, 0x1ffff, v5\n"
    "v_mfma_f32_16x16x32_bf16 a[8:11], a[196:199], v[192:195], a[8:11]\n"
    "v_exp_f32 v101, v101\n"
    "v_smfmac_f32_16x16x64_bf16 v[240:243], v[200:203], v[192:199], v204\n"
    "s_waitcnt lgkmcnt(4)\n"
    "ds_read_b128 a[164:167], v4 offset:9216\n"
    "v_mfma_f32_16x16x32_bf16 a[16:19], a[200:203], v[192:195], a[16:19]\n"
    "v_exp_f32 v102, v102\n"
    "v_mfma_f32_16x16x32_bf16 a[24:27], a[204:207], v[192:195], a[24:27]\n"
    "ds_read_b128 a[168:171], v4 offset:10240\n"
    "v_mfma_f32_16x16x32_bf16 a[32:35], a[208:211], v[192:195], a[32:35]\n"
    "s_waitcnt lgkmcnt(3)\n"
    "v_exp_f32 v103, v103\n"
    "v_mfma_f32_16x16x32_bf16 a[40:43], a[212:215], v[192:195], a[40:43]\n"
    "ds_read_b128 a[172:175], v4 offset:11264\n"
    "v_mfma_f32_16x16x32_bf16 a[48:51], a[216:219], v[192:195], a[48:51]\n"
    "v_exp_f32 v104, v104\n"
    "v_mfma_f32_16x16x32_bf16 a[56:59], a[220:223], v[192:195], a[56:59]\n"
    "ds_read_b128 a[176:179], v4 offset:12288\n"
    "v_cvt_pk_bf16_f32 v192, v96, v97\n"
    "v_mfma_f32_16x16x32_bf16 a[4:7], a[192:195], v[196:199], a[4:7]\n"
    "v_exp_f32 v105, v105\n"
    "v_mfma_f32_16x16x32_bf16 a[12:15], a[196:199], v[196:199], a[12:15]\n"
    "ds_read_b128 a[180:183], v4 offset:13312\n"
    "v_cvt_pk_bf16_f32 v193, v98, v99\n"
    "v_mfma_f32_16x16x32_bf16 a[20:23], a[200:203], v[196:199], a[20:23]\n"
    "v_exp_f32 v106, v106\n"
    "v_mfma_f32_16x16x32_bf16 a[28:31], a[204:207], v[196:199], a[28:31]\n"
    "ds_read_b128 a[184:187], v4 offset:14336\n"
    "v_cvt_pk_bf16_f32 v194, v100, v101\n"
    "v_mfma_f32_16x16x32_bf16 a[36:39], a[208:211], v[196:199], a[36:39]\n"
    "v_exp_f32 v107, v107\n"
    "v_mfma_f32_16x16x32_bf16 a[44:47], a[212:215], v[196:199], a[44:47]\n"
    "ds_read_b128 a[188:191], v4 offset:15360\n"
    "v_cvt_pk_bf16_f32 v195, v102, v103\n"
    "v_mfma_f32_16x16x32_bf16 a[52:55], a[216:219], v[196:199], a[52:55]\n"
    "s_waitcnt lgkmcnt(4)\n"
    "v_exp_f32 v108, v108\n"
    "v_mfma_f32_16x16x32_bf16 a[60:63], a[220:223], v[196:199], a[60:63]\n"
    "v_exp_f32 v109, v109\n"
    "v_mfma_f32_16x16x32_bf16 v[96:99], a[160:163], v[216:219], v[80:83]\n"
    "s_mov_b32 s83, s84\n"
    "v_cvt_pk_bf16_f32 v196, v104, v105\n"
    "v_mfma_f32_16x16x32_bf16 v[96:99], a[164:167], v[40:43], v[96:99]\n"
    "s_max_u32 m0, s64, s79\n"
    "v_exp_f32 v110, v110\n"
    "v_mfma_f32_16x16x32_bf16 v[96:99], a[168:171], v[56:59], v[96:99]\n"
    "v_cvt_pk_bf16_f32 v197, v106, v107\n"
    "v_mfma_f32_16x16x32_bf16 v[96:99], a[172:175], v[72:75], v[96:99]\n"
    "s_waitcnt lgkmcnt(0)\n"
    "v_exp_f32 v111, v111\n"
    "v_mfma_f32_16x16x32_bf16 v[100:103], a[176:179], v[216:219], v[84:87]\n"
    "s_addk_i32 s64, 0x4000\n"
    "v_cvt_pk_bf16_f32 v198, v108, v109\n"
    "v_mfma_f32_16x16x32_bf16 v[100:103], a[180:183], v[40:43], v[100:103]\n"
    "buffer_load_dwordx4 v0, s[40:43], s83 offen offset:0 sc1 lds\n"
    "v_mfma_f32_16x16x32_bf16 v[100:103], a[184:187], v[56:59], v[100:103]\n"
    "s_and_b32 s64, s64, 0x1ffff\n"
    "v_cvt_pk_bf16_f32 v199, v110, v111\n"
    "v_mfma_f32_16x16x32_bf16 v[100:103], a[188:191], v[72:75], v[100:103]\n"
    "s_add_u32 s80, s84, 0x4000\n"
    "v_exp_f32 v96, v96\n"
    "v_mfma_f32_16x16x32_bf16 v[104:107], a[160:163], v[220:223], v[88:91]\n"
    "s_cmp_eq_u32 s80, s85\n"
    "v_mfma_f32_16x16x32_bf16 v[104:107], a[164:167], v[44:47], v[104:107]\n"
    "s_cselect_b32 s84, s27, s80\n"
    "v_exp_f32 v97, v97\n"
    "v_mfma_f32_16x16x32_bf16 v[104:107], a[168:171], v[60:63], v[104:107]\n"
    "s_cmp_eq_u32 s80, s26\n"
    "v_mfma_f32_16x16x32_bf16 v[104:107], a[172:175], v[76:79], v[104:107]\n"
    "s_cselect_b32 s84, s88, s84\n"
    "v_exp_f32 v98, v98\n"
    "v_mfma_f32_16x16x32_bf16 v[108:111], a[176:179], v[220:223], v[92:95]\n"
    "buffer_load_dwordx4 v0, s[40:43], s83 offen offset:1024 sc1 lds\n"
    "v_mfma_f32_16x16x32_bf16 v[108:111], a[180:183], v[44:47], v[108:111]\n"
    "s_cmp_eq_u32 s80, s86\n"
    "v_exp_f32 v99, v99\n"
    "v_mfma_f32_16x16x32_bf16 v[108:111], a[184:187], v[60:63], v[108:111]\n"
    "s_cselect_b32 s84, s78, s84\n"
    "v_exp_f32 v100, v100\n"
    "v_mfma_f32_16x16x32_bf16 v[108:111], a[188:191], v[76:79], v[108:111]\n"
    "v_exp_f32 v101, v101\n"
    "v_mfma_f32_16x16x32_bf16 a[64:67], a[192:195], v[192:195], a[64:67]\n"
    "s_cmp_eq_u32 s80, s87\n"
    "v_mfma_f32_16x16x32_bf16 a[72:75], a[196:199], v[192:195], a[72:75]\n"
    "v_exp_f32 v102, v102\n"
    "v_smfmac_f32_16x16x64_bf16 v[248:251], v[200:203], v[192:199], v204\n"
    "buffer_load_dwordx4 v0, s[40:43], s83 offen offset:2048 sc1 lds\n"
    "v_mfma_f32_16x16x32_bf16 a[80:83], a[200:203], v[192:195], a[80:83]\n"
    "s_cselect_b32 s84, 0xc0000000, s84\n"
    "v_exp_f32 v103, v103\n"
    "v_mfma_f32_16x16x32_bf16 a[88:91], a[204:207], v[192:195], a[88:91]\n"
    "s_cselect_b32 s79, s55, s79\n"
    "v_mfma_f32_16x16x32_bf16 a[96:99], a[208:211], v[192:195], a[96:99]\n"
    "v_exp_f32 v104, v104\n"
    "v_mfma_f32_16x16x32_bf16 a[104:107], a[212:215], v[192:195], a[104:107]\n"
    "buffer_load_dwordx4 v0, s[40:43], s83 offen offset:3072 sc1 lds\n"
    "v_mfma_f32_16x16x32_bf16 a[112:115], a[216:219], v[192:195], a[112:115]\n"
    "v_exp_f32 v105, v105\n"
    "v_mfma_f32_16x16x32_bf16 a[120:123], a[220:223], v[192:195], a[120:123]\n"
    "v_cvt_pk_bf16_f32 v192, v96, v97\n"
    "v_mfma_f32_16x16x32_bf16 a[68:71], a[192:195], v[196:199], a[68:71]\n"
    "v_exp_f32 v106, v106\n"
    "v_mfma_f32_16x16x32_bf16 a[76:79], a[196:199], v[196:199], a[76:79]\n"
    "v_cvt_pk_bf16_f32 v193, v98, v99\n"
    "v_mfma_f32_16x16x32_bf16 a[84:87], a[200:203], v[196:199], a[84:87]\n"
    "s_waitcnt vmcnt(8)\n"
    "v_exp_f32 v107, v107\n"
    "v_mfma_f32_16x16x32_bf16 a[92:95], a[204:207], v[196:199], a[92:95]\n"
    "s_barrier\n"
    "v_cvt_pk_bf16_f32 v194, v100, v101\n"
    "v_mfma_f32_16x16x32_bf16 a[100:103], a[208:211], v[196:199], a[100:103]\n"
    "s_sub_u32 s63, s63, 1\n"
    "v_exp_f32 v108, v108\n"
    "v_mfma_f32_16x16x32_bf16 a[108:111], a[212:215], v[196:199], a[108:111]\n"
    "v_cvt_pk_bf16_f32 v195, v102, v103\n"
    "v_mfma_f32_16x16x32_bf16 a[116:119], a[216:219], v[196:199], a[116:119]\n"
    "v_exp_f32 v109, v109\n"
    "v_mfma_f32_16x16x32_bf16 a[124:127], a[220:223], v[196:199], a[124:127]\n"
    "s_cbranch_scc0 ATT_A_%=\n"
    "s_cmp_eq_u32 s65, 0\n"
    "s_cbranch_scc1 ATT_MID_%=\n"
    "ATT_AT_ENTRY_%=:\n"
    "s_mov_b32 s63, 0\n"
    "T_A_%=:\n"
    "s_cmp_lg_u32 s63, s58\n"
    "s_cbranch_scc1 T_H_%=\n"
    "s_cmp_eq_u32 s58, 3\n"
    "s_cbranch_scc0 T_NOHM_%=\n"
    "v_cndmask_b32 v80, v2, v80, s[92:93]\n"
    "v_cndmask_b32 v81, v2, v81, s[94:95]\n"
    "v_cndmask_b32 v82, v2, v82, s[96:97]\n"
    "v_cndmask_b32 v83, v2, v83, s[98:99]\n"
    "v_cndmask_b32 v92, v2, v92, s[92:93]\n"
    "v_cndmask_b32 v93, v2, v93, s[94:95]\n"
    "v_cndmask_b32 v94, v2, v94, s[96:97]\n"
    "v_cndmask_b32 v95, v2, v95, s[98:99]\n"
    "v_mov_b32 v84, v2\n"
    "v_mov_b32 v85, v2\n"
    "v_mov_b32 v86, v2\n"
    "v_mov_b32 v87, v2\n"
    "T_NOHM_%=:\n"
    "v_cndmask_b32 v224, v2, v224, s[92:93]\n"
    "v_cndmask_b32 v225, v2, v225, s[94:95]\n"
    "v_cndmask_b32 v226, v2, v226, s[96:97]\n"
    "v_cndmask_b32 v227, v2, v227, s[98:99]\n"
    "v_cndmask_b32 v236, v2, v236, s[92:93]\n"
    "v_cndmask_b32 v237, v2, v237, s[94:95]\n"
    "v_cndmask_b32 v238, v2, v238, s[96:97]\n"
    "v_cndmask_b32 v239, v2, v239, s[98:99]\n"
    "v_mov_b32 v228, v2\n"
    "v_mov_b32 v229, v2\n"
    "v_mov_b32 v230, v2\n"
    "v_mov_b32 v231, v2\n"
    "s_nop 0\n"
    "T_H_%=:\n"
    "v_mfma_f32_16x16x32_bf16 v[96:99], a[160:163], v[208:211], v[224:227]\n"
    "s_mov_b32 s83, s84\n"
    "ds_read_b128 a[192:195], v5 offset:0\n"
    "v_cvt_pk_bf16_f32 v196, v104, v105\n"
    "v_mfma_f32_16x16x32_bf16 v[96:99], a[164:167], v[32:35], v[96:99]\n"
    "s_max_u32 m0, s64, s79\n"
    "v_exp_f32 v110, v110\n"
    "v_mfma_f32_16x16x32_bf16 v[96:99], a[168:171], v[48:51], v[96:99]\n"
    "ds_read_b128 a[196:199], v5 offset:1024\n"
    "v_cvt_pk_bf16_f32 v197, v106, v107\n"
    "v_mfma_f32_16x16x32_bf16 v[96:99], a[172:175], v[64:67], v[96:99]\n"
    "v_exp_f32 v111, v111\n"
    "v_mfma_f32_16x16x32_bf16 v[100:103], a[176:179], v[208:211], v[228:231]\n"
    "s_addk_i32 s64, 0x4000\n"
    "ds_read_b128 a[200:203], v5 offset:2048\n"
    "v_cvt_pk_bf16_f32 v198, v108, v109\n"
    "v_mfma_f32_16x16x32_bf16 v[100:103], a[180:183], v[32:35], v[100:103]\n"
    "buffer_load_dwordx4 v0, s[40:43], s83 offen offset:0 sc1 lds\n"
    "v_mfma_f32_16x16x32_bf16 v[100:103], a[184:187], v[48:51], v[100:103]\n"
    "s_and_b32 s64, s64, 0x1ffff\n"
    "ds_read_b128 a[204:207], v5 offset:3072\n"
    "v_cvt_pk_bf16_f32 v199, v110, v111\n"
    "v_mfma_f32_16x16x32_bf16 v[100:103], a[188:191], v[64:67], v[100:103]\n"
    "s_add_u32 s80, s84, 0x4000\n"
    "v_exp_f32 v96, v96\n"
    "v_mfma_f32_16x16x32_bf16 v[104:107], a[160:163], v[212:215], v[232:235]\n"
    "s_cmp_eq_u32 s80, s85\n"
    "ds_read_b128 a[208:211], v5 offset:4096\n"
    "v_add_u32 v4, 0x4000, v4\n"
    "v_mfma_f32_16x16x32_bf16 v[104:107], a[164:167], v[36:39], v[104:107]\n"
    "s_cselect_b32 s84, s27, s80\n"
    "v_exp_f32 v97, v97\n"
    "v_mfma_f32_16x16x32_bf16 v[104:107], a[168:171], v[52:55], v[104:107]\n"
    "s_cmp_eq_u32 s80, s26\n"
    "ds_read_b128 a[212:215], v5 offset:5120\n"
    "v_and_b32 v4, 0x1ffff, v4\n"
    "v_mfma_f32_16x16x32_bf16 v[104:107], a[172:175], v[68:71], v[104:107]\n"
    "s_cselect_b32 s84, s88, s84\n"
    "v_exp_f32 v98, v98\n"
    "v_mfma_f32_16x16x32_bf16 v[108:111], a[176:179], v[212:215], v[236:239]\n"
    "s_cmp_eq_u32 s80, s86\n"
    "ds_read_b128 a[216:219], v5 offset:6144\n"
    "v_exp_f32 v99, v99\n"
    "v_mfma_f32_16x16x32_bf16 v[108:111], a[180:183], v[36:39], v[108:111]\n"
    "buffer_load_dwordx4 v0, s[40:43], s83 offen offset:1024 sc1 lds\n"
    "v_mfma_f32_16x16x32_bf16 v[108:111], a[184:187], v[52:55], v[108:111]\n"
    "s_cselect_b32 s84, s78, s84\n"
    "ds_read_b128 a[220:223], v5 offset:7168\n"
    "v_add_u32 v5, 0x4000, v5\n"
    "v_mfma_f32_16x16x32_bf16 v[108:111], a[188:191], v[68:71], v[108:111]\n"
    "s_waitcnt lgkmcnt(6)\n"
    "v_exp_f32 v100, v100\n"
    "v_mfma_f32_16x16x32_bf16 a[0:3], a[192:195], v[192:195], a[0:3]\n"
    "s_cmp_eq_u32 s80, s87\n"
    "ds_read_b128 a[160:163], v4 offset:8192\n"
    "v_and_b32 v5, 0x1ffff, v5\n"
    "v_mfma_f32_16x16x32_bf16 a[8:11], a[196:199], v[192:195], a[8:11]\n"
    "s_cselect_b32 s84, 0xc0000000, s84\n"
    "v_exp_f32 v101, v101\n"
    "v_smfmac_f32_16x16x64_bf16 v[240:243], v[200:203], v[192:199], v204\n"
    "s_waitcnt lgkmcnt(4)\n"
    "ds_read_b128 a[164:167], v4 offset:9216\n"
    "v_mfma_f32_16x16x32_bf16 a[16:19], a[200:203], v[192:195], a[16:19]\n"
    "s_cselect_b32 s79, s55, s79\n"
    "v_exp_f32 v102, v102\n"
    "v_mfma_f32_16x16x32_bf16 a[24:27], a[204:207], v[192:195], a[24:27]\n"
    "ds_read_b128 a[168:171], v4 offset:10240\n"
    "v_exp_f32 v103, v103\n"
    "v_mfma_f32_16x16x32_bf16 a[32:35], a[208:211], v[192:195], a[32:35]\n"
    "s_waitcnt lgkmcnt(3)\n"
    "v_exp_f32 v104, v104\n"
    "v_mfma_f32_16x16x32_bf16 a[40:43], a[212:215], v[192:195], a[40:43]\n"
    "ds_read_b128 a[172:175], v4 offset:11264\n"
    "v_exp_f32 v105, v105\n"
    "v_mfma_f32_16x16x32_bf16 a[48:51], a[216:219], v[192:195], a[48:51]\n"
    "buffer_load_dwordx4 v0, s[40:43], s83 offen offset:2048 sc1 lds\n"
    "v_mfma_f32_16x16x32_bf16 a[56:59], a[220:223], v[192:195], a[56:59]\n"
    "ds_read_b128 a[176:179], v4 offset:12288\n"
    "v_cvt_pk_bf16_f32 v192, v96, v97\n"
    "v_mfma_f32_16x16x32_bf16 a[4:7], a[192:195], v[196:199], a[4:7]\n"
    "v_exp_f32 v106, v106\n"
    "v_mfma_f32_16x16x32_bf16 a[12:15], a[196:199], v[196:199], a[12:15]\n"
    "ds_read_b128 a[180:183], v4 offset:13312\n"
    "v_cvt_pk_bf16_f32 v193, v98, v99\n"
    "v_mfma_f32_16x16x32_bf16 a[20:23], a[200:203], v[196:199], a[20:23]\n"
    "v_exp_f32 v107, v107\n"
    "v_mfma_f32_16x16x32_bf16 a[28:31], a[204:207], v[196:199], a[28:31]\n"
    "ds_read_b128 a[184:187], v4 offset:14336\n"
    "v_cvt_pk_bf16_f32 v194, v100, v101\n"
    "v_mfma_f32_16x16x32_bf16 a[36:39], a[208:211], v[196:199], a[36:39]\n"
    "s_cmp_eq_u32 s63, s58\n"
    "v_exp_f32 v108, v108\n"
    "v_mfma_f32_16x16x32_bf16 a[44:47], a[212:215], v[196:199], a[44:47]\n"
    "ds_read_b128 a[188:191], v4 offset:15360\n"
    "v_cvt_pk_bf16_f32 v195, v102, v103\n"
    "v_mfma_f32_16x16x32_bf16 a[52:55], a[216:219], v[196:199], a[52:55]\n"
    "s_waitcnt lgkmcnt(4)\n"
    "v_exp_f32 v109, v109\n"
    "v_mfma_f32_16x16x32_bf16 a[60:63], a[220:223], v[196:199], a[60:63]\n"
    "buffer_load_dwordx4 v0, s[40:43], s83 offen offset:3072 sc1 lds\n"
    "s_cbranch_scc1 T_LL_%=\n"
    "v_mfma_f32_16x16x32_bf16 v[96:99], a[160:163], v[216:219], v[80:83]\n"
    "s_mov_b32 s83, s84\n"
    "v_cvt_pk_bf16_f32 v196, v104, v105\n"
    "v_mfma_f32_16x16x32_bf16 v[96:99], a[164:167], v[40:43], v[96:99]\n"
    "s_max_u32 m0, s64, s79\n"
    "v_exp_f32 v110, v110\n"
    "v_mfma_f32_16x16x32_bf16 v[96:99], a[168:171], v[56:59], v[96:99]\n"
    "v_cvt_pk_bf16_f32 v197, v106, v107\n"
    "v_mfma_f32_16x16x32_bf16 v[96:99], a[172:175], v[72:75], v[96:99]\n"
    "s_waitcnt lgkmcnt(0)\n"
    "v_exp_f32 v111, v111\n"
    "v_mfma_f32_16x16x32_bf16 v[100:103], a[176:179], v[216:219], v[84:87]\n"
    "s_addk_i32 s64, 0x4000\n"
    "v_cvt_pk_bf16_f32 v198, v108, v109\n"
    "v_mfma_f32_16x16x32_bf16 v[100:103], a[180:183], v[40:43], v[100:103]\n"
    "buffer_load_dwordx4 v0, s[40:43], s83 offen offset:0 sc1 lds\n"
    "v_mfma_f32_16x16x32_bf16 v[100:103], a[184:187], v[56:59], v[100:103]\n"
    "s_and_b32 s64, s64, 0x1ffff\n"
    "v_cvt_pk_bf16_f32 v199, v110, v111\n"
    "v_mfma_f32_16x16x32_bf16 v[100:103], a[188:191], v[72:75], v[100:103]\n"
    "s_add_u32 s80, s84, 0x4000\n"
    "v_exp_f32 v96, v96\n"
    "v_mfma_f32_16x16x32_bf16 v[104:107], a[160:163], v[220:223], v[88:91]\n"
    "s_cmp_eq_u32 s80, s85\n"
    "v_mfma_f32_16x16x32_bf16 v[104:107], a[164:167], v[44:47], v[104:107]\n"
    "s_cselect_b32 s84, s27, s80\n"
    "v_exp_f32 v97, v97\n"
    "v_mfma_f32_16x16x32_bf16 v[104:107], a[168:171], v[60:63], v[104:107]\n"
    "s_cmp_eq_u32 s80, s26\n"
    "v_mfma_f32_16x16x32_bf16 v[104:107], a[172:175], v[76:79], v[104:107]\n"
    "s_cselect_b32 s84, s88, s84\n"
    "v_exp_f32 v98, v98\n"
    "v_mfma_f32_16x16x32_bf16 v[108:111], a[176:179], v[220:223], v[92:95]\n"
    "buffer_load_dwordx4 v0, s[40:43], s83 offen offset:1024 sc1 lds\n"
    "v_mfma_f32_16x16x32_bf16 v[108:111], a[180:183], v[44:47], v[108:111]\n"
    "s_cmp_eq_u32 s80, s86\n"
    "v_exp_f32 v99, v99\n"
    "v_mfma_f32_16x16x32_bf16 v[108:111], a[184:187], v[60:63], v[108:111]\n"
    "s_cselect_b32 s84, s78, s84\n"
    "v_exp_f32 v100, v100\n"
    "v_mfma_f32_16x16x32_bf16 v[108:111], a[188:191], v[76:79], v[108:111]\n"
    "v_exp_f32 v101, v101\n"
    "v_mfma_f32_16x16x32_bf16 a[64:67], a[192:195], v[192:195], a[64:67]\n"
    "s_cmp_eq_u32 s80, s87\n"
    "v_mfma_f32_16x16x32_bf16 a[72:75], a[196:199], v[192:195], a[72:75]\n"
    "v_exp_f32 v102, v102\n"
    "v_smfmac_f32_16x16x64_bf16 v[248:251], v[200:203], v[192:199], v204\n"
    "buffer_load_dwordx4 v0, s[40:43], s83 offen offset:2048 sc1 lds\n"
    "v_mfma_f32_16x16x32_bf16 a[80:83], a[200:203], v[192:195], a[80:83]\n"
    "s_cselect_b32 s84, 0xc0000000, s84\n"
    "v_exp_f32 v103, v103\n"
    "v_mfma_f32_16x16x32_bf16 a[88:91], a[204:207], v[192:195], a[88:91]\n"
    "s_cselect_b32 s79, s55, s79\n"
    "v_mfma_f32_16x16x32_bf16 a[96:99], a[208:211], v[192:195], a[96:99]\n"
    "v_exp_f32 v104, v104\n"
    "v_mfma_f32_16x16x32_bf16 a[104:107], a[212:215], v[192:195], a[104:107]\n"
    "buffer_load_dwordx4 v0, s[40:43], s83 offen offset:3072 sc1 lds\n"
    "v_mfma_f32_16x16x32_bf16 a[112:115], a[216:219], v[192:195], a[112:115]\n"
    "v_exp_f32 v105, v105\n"
    "v_mfma_f32_16x16x32_bf16 a[120:123], a[220:223], v[192:195], a[120:123]\n"
    "v_cvt_pk_bf16_f32 v192, v96, v97\n"
    "v_mfma_f32_16x16x32_bf16 a[68:71], a[192:195], v[196:199], a[68:71]\n"
    "v_exp_f32 v106, v106\n"
    "v_mfma_f32_16x16x32_bf16 a[76:79], a[196:199], v[196:199], a[76:79]\n"
    "v_cvt_pk_bf16_f32 v193, v98, v99\n"
    "v_mfma_f32_16x16x32_bf16 a[84:87], a[200:203], v[196:199], a[84:87]\n"
    "v_exp_f32 v107, v107\n"
    "v_mfma_f32_16x16x32_bf16 a[92:95], a[204:207], v[196:199], a[92:95]\n"
    "v_cvt_pk_bf16_f32 v194, v100, v101\n"
    "v_mfma_f32_16x16x32_bf16 a[100:103], a[208:211], v[196:199], a[100:103]\n"
    "s_add_u32 s63, s63, 1\n"
    "v_exp_f32 v108, v108\n"
    "v_mfma_f32_16x16x32_bf16 a[108:111], a[212:215], v[196:199], a[108:111]\n"
    "s_cmp_lg_u32 s63, 2\n"
    "v_cvt_pk_bf16_f32 v195, v102, v103\n"
    "v_mfma_f32_16x16x32_bf16 a[116:119], a[216:219], v[196:199], a[116:119]\n"
    "v_exp_f32 v109, v109\n"
    "v_mfma_f32_16x16x32_bf16 a[124:127], a[220:223], v[196:199], a[124:127]\n"
    "s_cbranch_scc1 T_A_%=\n"
    "s_waitcnt vmcnt(4)\n"
    "s_barrier\n"
    "s_branch T_A_%=\n"
    "T_LL_%=:\n"
    "v_mfma_f32_16x16x32_bf16 v[96:99], a[160:163], v[216:219], v[80:83]\n"
    "s_mov_b32 s83, s84\n"
    "v_cvt_pk_bf16_f32 v196, v104, v105\n"
    "v_mfma_f32_16x16x32_bf16 v[96:99], a[164:167], v[40:43], v[96:99]\n"
    "s_max_u32 m0, s64, s79\n"
    "v_exp_f32 v110, v110\n"
    "v_mfma_f32_16x16x32_bf16 v[96:99], a[168:171], v[56:59], v[96:99]\n"
    "s_lshl_b32 s62, s28, 8\n"
    "v_cvt_pk_bf16_f32 v197, v106, v107\n"
    "v_mfma_f32_16x16x32_bf16 v[96:99], a[172:175], v[72:75], v[96:99]\n"
    "s_waitcnt lgkmcnt(0)\n"
    "v_exp_f32 v111, v111\n"
    "v_mfma_f32_16x16x32_bf16 v[100:103], a[176:179], v[216:219], v[84:87]\n"
    "s_addk_i32 s64, 0x4000\n"
    "v_cvt_pk_bf16_f32 v198, v108, v109\n"
    "v_mfma_f32_16x16x32_bf16 v[100:103], a[180:183], v[40:43], v[100:103]\n"
    "buffer_load_dwordx4 v0, s[40:43], s83 offen offset:0 sc1 lds\n"
    "v_mfma_f32_16x16x32_bf16 v[100:103], a[184:187], v[56:59], v[100:103]\n"
    "s_and_b32 s64, s64, 0x1ffff\n"
    "v_cvt_pk_bf16_f32 v199, v110, v111\n"
    "v_mfma_f32_16x16x32_bf16 v[100:103], a[188:191], v[72:75], v[100:103]\n"
    "s_add_u32 s80, s84, 0x4000\n"
    "v_exp_f32 v96, v96\n"
    "v_mfma_f32_16x16x32_bf16 v[104:107], a[160:163], v[220:223], v[88:91]\n"
    "s_cmp_eq_u32 s80, s85\n"
    "v_add_u32 v4, 0x4000, v4\n"
    "v_mfma_f32_16x16x32_bf16 v[104:107], a[164:167], v[44:47], v[104:107]\n"
    "s_cselect_b32 s84, s27, s80\n"
    "v_exp_f32 v97, v97\n"
    "v_mfma_f32_16x16x32_bf16 v[104:107], a[168:171], v[60:63], v[104:107]\n"
    "s_cmp_eq_u32 s80, s26\n"
    "v_and_b32 v4, 0x1ffff, v4\n"
    "v_mfma_f32_16x16x32_bf16 v[104:107], a[172:175], v[76:79], v[104:107]\n"
    "s_cselect_b32 s84, s88, s84\n"
    "v_exp_f32 v98, v98\n"
    "v_mfma_f32_16x16x32_bf16 v[108:111], a[176:179], v[220:223], v[92:95]\n"
    "buffer_load_dwordx4 v0, s[40:43], s83 offen offset:1024 sc1 lds\n"
    "v_mfma_f32_16x16x32_bf16 v[108:111], a[180:183], v[44:47], v[108:111]\n"
    "s_cmp_eq_u32 s80, s86\n"
    "v_exp_f32 v99, v99\n"
    "v_mfma_f32_16x16x32_bf16 v[108:111], a[184:187], v[60:63], v[108:111]\n"
    "s_cselect_b32 s84, s78, s84\n"
    "v_exp_f32 v100, v100\n"
    "v_mfma_f32_16x16x32_bf16 v[108:111], a[188:191], v[76:79], v[108:111]\n"
    "v_exp_f32 v101, v101\n"
    "v_mfma_f32_16x16x32_bf16 a[64:67], a[192:195], v[192:195], a[64:67]\n"
    "s_cmp_eq_u32 s80, s87\n"
    "ds_read_b128 a[160:163], v4 offset:8192\n"
    "v_mfma_f32_16x16x32_bf16 a[72:75], a[196:199], v[192:195], a[72:75]\n"
    "s_cselect_b32 s84, 0xc0000000, s84\n"
    "v_exp_f32 v102, v102\n"
    "v_smfmac_f32_16x16x64_bf16 v[248:251], v[200:203], v[192:199], v204\n"
    "ds_read_b128 a[164:167], v4 offset:9216\n"
    "v_mfma_f32_16x16x32_bf16 a[80:83], a[200:203], v[192:195], a[80:83]\n"
    "s_cselect_b32 s79, s55, s79\n"
    "v_exp_f32 v103, v103\n"
    "v_mfma_f32_16x16x32_bf16 a[88:91], a[204:207], v[192:195], a[88:91]\n"
    "s_addk_i32 s62, 0x3800\n"
    "ds_read_b128 a[168:171], v4 offset:10240\n"
    "v_mfma_f32_16x16x32_bf16 a[96:99], a[208:211], v[192:195], a[96:99]\n"
    "v_exp_f32 v104, v104\n"
    "v_mfma_f32_16x16x32_bf16 a[104:107], a[212:215], v[192:195], a[104:107]\n"
    "s_cmp_eq_u32 s58, 3\n"
    "ds_read_b128 a[172:175], v4 offset:11264\n"
    "v_exp_f32 v105, v105\n"
    "v_mfma_f32_16x16x32_bf16 a[112:115], a[216:219], v[192:195], a[112:115]\n"
    "buffer_load_dwordx4 v0, s[40:43], s83 offen offset:2048 sc1 lds\n"
    "v_mfma_f32_16x16x32_bf16 a[120:123], a[220:223], v[192:195], a[120:123]\n"
    "s_cselect_b32 s62, s62, s31\n"
    "ds_read_b128 a[176:179], v4 offset:12288\n"
    "v_cvt_pk_bf16_f32 v192, v96, v97\n"
    "v_mfma_f32_16x16x32_bf16 a[68:71], a[192:195], v[196:199], a[68:71]\n"
    "s_cselect_b32 s60, 63, s66\n"
    "v_exp_f32 v106, v106\n"
    "v_mfma_f32_16x16x32_bf16 a[76:79], a[196:199], v[196:199], a[76:79]\n"
    "s_add_u32 s63, s63, 1\n"
    "ds_read_b128 a[180:183], v4 offset:13312\n"
    "v_cvt_pk_bf16_f32 v193, v98, v99\n"
    "v_mfma_f32_16x16x32_bf16 a[84:87], a[200:203], v[196:199], a[84:87]\n"
    "s_cmp_eq_u32 s63, 1\n"
    "v_exp_f32 v107, v107\n"
    "v_mfma_f32_16x16x32_bf16 a[92:95], a[204:207], v[196:199], a[92:95]\n"
    "ds_read_b128 a[184:187], v4 offset:14336\n"
    "v_cvt_pk_bf16_f32 v194, v100, v101\n"
    "v_mfma_f32_16x16x32_bf16 a[100:103], a[208:211], v[196:199], a[100:103]\n"
    "v_exp_f32 v108, v108\n"
    "v_mfma_f32_16x16x32_bf16 a[108:111], a[212:215], v[196:199], a[108:111]\n"
    "ds_read_b128 a[188:191], v4 offset:15360\n"
    "v_cvt_pk_bf16_f32 v195, v102, v103\n"
    "v_mfma_f32_16x16x32_bf16 a[116:119], a[216:219], v[196:199], a[116:119]\n"
    "s_waitcnt lgkmcnt(4)\n"
    "v_exp_f32 v109, v109\n"
    "v_mfma_f32_16x16x32_bf16 a[124:127], a[220:223], v[196:199], a[124:127]\n"
    "buffer_load_dwordx4 v0, s[40:43], s83 offen offset:3072 sc1 lds\n"
    "s_cbranch_scc1 T_B_%=\n"
    "s_cmp_lg_u32 s63, 2\n"
    "s_cbranch_scc1 T_LEPI_%=\n"
    "T_BAR_%=:\n"
    "s_waitcnt vmcnt(12)\n"
    "s_barrier\n"
    "T_LEPI_%=:\n"
    "v_min_u32 v10, s60, v12\n"
    "v_lshlrev_b32 v10, 2, v10\n"
    "buffer_load_dword v1, v10, s[40:43], s62 offen offset:0 sc1\n"
    "s_nop 3\n"
    "v_add_f32 v252, v250, v251\n"
    "v_add_f32 v248, v248, v249\n"
    "v_rcp_f32 v30, v248\n"
    "v_rcp_f32 v31, v252\n"
    "s_lshl_b32 s61, s75, 5\n"
    "s_add_u32 s61, s61, s56\n"
    "s_lshl_b32 s61, s61, 12\n"
    "s_add_u32 s61, s61, 0x29c0000\n"
    "v_accvgpr_read_b32 v112, a64\n"
    "v_accvgpr_read_b32 v113, a65\n"
    "v_accvgpr_read_b32 v114, a66\n"
    "v_accvgpr_read_b32 v115, a67\n"
    "v_accvgpr_read_b32 v116, a72\n"
    "v_accvgpr_read_b32 v117, a73\n"
    "v_accvgpr_read_b32 v118, a74\n"
    "v_accvgpr_read_b32 v119, a75\n"
    "v_mul_f32 v112, v112, v30\n"
    "v_mul_f32 v113, v113, v30\n"
    "buffer_load_dwordx2 a[240:241], v24, s[44:47], s35 offen offset:0\n"
    "v_mul_f32 v114, v114, v30\n"
    "v_mul_f32 v115, v115, v30\n"
    "v_mul_f32 v116, v116, v30\n"
    "v_mul_f32 v117, v117, v30\n"
    "v_mul_f32 v118, v118, v30\n"
    "v_mul_f32 v119, v119, v30\n"
    "v_cvt_pk_bf16_f32 v128, v112, v113\n"
    "v_cvt_pk_bf16_f32 v129, v114, v115\n"
    "v_cvt_pk_bf16_f32 v130, v116, v117\n"
    "v_cvt_pk_bf16_f32 v131, v118, v119\n"
    "buffer_store_dwordx4 v[128:131], v0, s[40:43], s61 offen offset:0 sc1\n"
    "v_accvgpr_read_b32 v120, a80\n"
    "v_accvgpr_read_b32 v121, a81\n"
    "v_accvgpr_read_b32 v122, a82\n"
    "v_accvgpr_read_b32 v123, a83\n"
    "v_accvgpr_read_b32 v124, a88\n"
    "v_accvgpr_read_b32 v125, a89\n"
    "v_accvgpr_read_b32 v126, a90\n"
    "v_accvgpr_read_b32 v127, a91\n"
    "v_mul_f32 v120, v120, v30\n"
    "v_mul_f32 v121, v121, v30\n"
    "buffer_load_dwordx2 a[242:243], v24, s[44:47], s35 offen offset:32\n"
    "v_mul_f32 v122, v122, v30\n"
    "v_mul_f32 v123, v123, v30\n"
    "v_mul_f32 v124, v124, v30\n"
    "v_mul_f32 v125, v125, v30\n"
    "v_mul_f32 v126, v126, v30\n"
    "v_mul_f32 v127, v127, v30\n"
    "v_cvt_pk_bf16_f32 v132, v120, v121\n"
    "v_cvt_pk_bf16_f32 v133, v122, v123\n"
    "v_cvt_pk_bf16_f32 v134, v124, v125\n"
    "v_cvt_pk_bf16_f32 v135, v126, v127\n"
    "buffer_store_dwordx4 v[132:135], v0, s[40:43], s61 offen offset:1024 sc1\n"
    "v_accvgpr_read_b32 v112, a96\n"
    "v_accvgpr_read_b32 v113, a97\n"
    "v_accvgpr_read_b32 v114, a98\n"
    "v_accvgpr_read_b32 v115, a99\n"
    "v_accvgpr_read_b32 v116, a104\n"
    "v_accvgpr_read_b32 v117, a105\n"
    "v_accvgpr_read_b32 v118, a106\n"
    "v_accvgpr_read_b32 v119, a107\n"
    "v_mul_f32 v112, v112, v30\n"
    "v_mul_f32 v113, v113, v30\n"
    "buffer_load_dwordx2 a[244:245], v24, s[44:47], s35 offen offset:64\n"
    "v_mul_f32 v114, v114, v30\n"
    "v_mul_f32 v115, v115, v30\n"
    "v_mul_f32 v116, v116, v30\n"
    "v_mul_f32 v117, v117, v30\n"
    "v_mul_f32 v118, v118, v30\n"
    "v_mul_f32 v119, v119, v30\n"
    "v_cvt_pk_bf16_f32 v136, v112, v113\n"
    "v_cvt_pk_bf16_f32 v137, v114, v115\n"
    "v_cvt_pk_bf16_f32 v138, v116, v117\n"
    "v_cvt_pk_bf16_f32 v139, v118, v119\n"
    "buffer_store_dwordx4 v[136:139], v0, s[40:43], s61 offen offset:2048 sc1\n"
    "v_accvgpr_read_b32 v120, a112\n"
    "v_accvgpr_read_b32 v121, a113\n"
    "v_accvgpr_read_b32 v122, a114\n"
    "v_accvgpr_read_b32 v123, a115\n"
    "v_accvgpr_read_b32 v124, a120\n"
    "v_accvgpr_read_b32 v125, a121\n"
    "v_accvgpr_read_b32 v126, a122\n"
    "v_accvgpr_read_b32 v127, a123\n"
    "v_mul_f32 v120, v120, v30\n"
    "v_mul_f32 v121, v121, v30\n"
    "buffer_load_dwordx2 a[246:247], v24, s[44:47], s35 offen offset:96\n"
    "v_mul_f32 v122, v122, v30\n"
    "v_mul_f32 v123, v123, v30\n"
    "v_mul_f32 v124, v124, v30\n"
    "v_mul_f32 v125, v125, v30\n"
    "v_mul_f32 v126, v126, v30\n"
    "v_mul_f32 v127, v127, v30\n"
    "v_cvt_pk_bf16_f32 v140, v120, v121\n"
    "v_cvt_pk_bf16_f32 v141, v122, v123\n"
    "v_cvt_pk_bf16_f32 v142, v124, v125\n"
    "v_cvt_pk_bf16_f32 v143, v126, v127\n"
    "buffer_store_dwordx4 v[140:143], v0, s[40:43], s61 offen offset:3072 sc1\n"
    "s_add_u32 s61, s61, 0x20000\n"
    "v_accvgpr_read_b32 v112, a68\n"
    "v_accvgpr_read_b32 v113, a69\n"
    "v_accvgpr_read_b32 v114, a70\n"
    "v_accvgpr_read_b32 v115, a71\n"
    "v_accvgpr_read_b32 v116, a76\n"
    "v_accvgpr_read_b32 v117, a77\n"
    "v_accvgpr_read_b32 v118, a78\n"
    "v_accvgpr_read_b32 v119, a79\n"
    "v_mul_f32 v112, v112, v31\n"
    "v_mul_f32 v113, v113, v31\n"
    "buffer_load_dwordx2 a[248:249], v24, s[44:47], s35 offen offset:128\n"
    "v_mul_f32 v114, v114, v31\n"
    "v_mul_f32 v115, v115, v31\n"
    "v_mul_f32 v116, v116, v31\n"
    "v_mul_f32 v117, v117, v31\n"
    "v_mul_f32 v118, v118, v31\n"
    "v_mul_f32 v119, v119, v31\n"
    "v_cvt_pk_bf16_f32 v144, v112, v113\n"
    "v_cvt_pk_bf16_f32 v145, v114, v115\n"
    "v_cvt_pk_bf16_f32 v146, v116, v117\n"
    "v_cvt_pk_bf16_f32 v147, v118, v119\n"
    "buffer_store_dwordx4 v[144:147], v0, s[40:43], s61 offen offset:0 sc1\n"
    "v_accvgpr_read_b32 v120, a84\n"
    "v_accvgpr_read_b32 v121, a85\n"
    "v_accvgpr_read_b32 v122, a86\n"
    "v_accvgpr_read_b32 v123, a87\n"
    "v_accvgpr_read_b32 v124, a92\n"
    "v_accvgpr_read_b32 v125, a93\n"
    "v_accvgpr_read_b32 v126, a94\n"
    "v_accvgpr_read_b32 v127, a95\n"
    "v_mul_f32 v120, v120, v31\n"
    "v_mul_f32 v121, v121, v31\n"
    "v_mul_f32 v122, v122, v31\n"
    "v_mul_f32 v123, v123, v31\n"
    "v_mul_f32 v124, v124, v31\n"
    "v_mul_f32 v125, v125, v31\n"
    "v_mul_f32 v126, v126, v31\n"
    "v_mul_f32 v127, v127, v31\n"
    "v_cvt_pk_bf16_f32 v148, v120, v121\n"
    "v_cvt_pk_bf16_f32 v149, v122, v123\n"
    "v_cvt_pk_bf16_f32 v150, v124, v125\n"
    "v_cvt_pk_bf16_f32 v151, v126, v127\n"
    "buffer_store_dwordx4 v[148:151], v0, s[40:43], s61 offen offset:1024 sc1\n"
    "v_accvgpr_read_b32 v112, a100\n"
    "v_accvgpr_read_b32 v113, a101\n"
    "v_accvgpr_read_b32 v114, a102\n"
    "v_accvgpr_read_b32 v115, a103\n"
    "v_accvgpr_read_b32 v116, a108\n"
    "v_accvgpr_read_b32 v117, a109\n"
    "v_accvgpr_read_b32 v118, a110\n"
    "v_accvgpr_read_b32 v119, a111\n"
    "v_mul_f32 v112, v112, v31\n"
    "v_mul_f32 v113, v113, v31\n"
    "v_mul_f32 v114, v114, v31\n"
    "v_mul_f32 v115, v115, v31\n"
    "v_mul_f32 v116, v116, v31\n"
    "v_mul_f32 v117, v117, v31\n"
    "v_mul_f32 v118, v118, v31\n"
    "v_mul_f32 v119, v119, v31\n"
    "v_cvt_pk_bf16_f32 v152, v112, v113\n"
    "v_cvt_pk_bf16_f32 v153, v114, v115\n"
    "v_cvt_pk_bf16_f32 v154, v116, v117\n"
    "v_cvt_pk_bf16_f32 v155, v118, v119\n"
    "buffer_store_dwordx4 v[152:155], v0, s[40:43], s61 offen offset:2048 sc1\n"
    "v_accvgpr_read_b32 v120, a116\n"
    "v_accvgpr_read_b32 v121, a117\n"
    "v_accvgpr_read_b32 v122, a118\n"
    "v_accvgpr_read_b32 v123, a119\n"
    "v_accvgpr_read_b32 v124, a124\n"
    "v_accvgpr_read_b32 v125, a125\n"
    "v_accvgpr_read_b32 v126, a126\n"
    "v_accvgpr_read_b32 v127, a127\n"
    "v_mul_f32 v120, v120, v31\n"
    "v_mul_f32 v121, v121, v31\n"
    "v_mul_f32 v122, v122, v31\n"
    "v_mul_f32 v123, v123, v31\n"
    "v_mul_f32 v124, v124, v31\n"
    "v_mul_f32 v125, v125, v31\n"
    "v_mul_f32 v126, v126, v31\n"
    "v_mul_f32 v127, v127, v31\n"
    "v_cvt_pk_bf16_f32 v156, v120, v121\n"
    "v_cvt_pk_bf16_f32 v157, v122, v123\n"
    "v_cvt_pk_bf16_f32 v158, v124, v125\n"
    "v_cvt_pk_bf16_f32 v159, v126, v127\n"
    "buffer_store_dwordx4 v[156:159], v0, s[40:43], s61 offen offset:3072 sc1\n"
    "s_waitcnt vmcnt(13)\n"
    "v_cmp_ne_u32 vcc, v1, v13\n"
    "s_nop 1\n"
    "s_cbranch_vccnz HF_SPIN_%=\n"
    "HF_OK_%=:\n"
    "s_getpc_b64 s[38:39]\n"
    "PCC_%=:\n"
    "s_lshr_b32 s59, %[bid], 3\n"
    "s_lshl2_add_u32 s59, s59, %[wid]\n"
    "s_mulk_i32 s59, 256\n"
    "v_lshrrev_b32 v27, 5, v12\n"
    "v_lshlrev_b32 v27, 7, v27\n"
    "s_add_u32 s59, s59, ATT_DRAIN_%=-PCC_%=\n"
    "s_min_u32 s59, s59, MAXPASS_%=-PCC_%=-256\n"
    "s_add_u32 s38, s38, s59\n"
    "s_addc_u32 s39, s39, 0\n"
    "global_load_dword v27, v27, s[38:39]\n"
    "s_cmp_eq_u32 s63, 4\n"
    "s_cbranch_scc1 ATT_DRAIN_%=\n"
    "T_B_%=:\n"
    "v_mfma_f32_16x16x32_bf16 v[96:99], a[160:163], v[216:219], v[80:83]\n"
    "s_mov_b32 s83, s84\n"
    "ds_read_b128 a[192:195], v5 offset:0\n"
    "v_cvt_pk_bf16_f32 v196, v104, v105\n"
    "v_mfma_f32_16x16x32_bf16 v[96:99], a[164:167], v[40:43], v[96:99]\n"
    "s_max_u32 m0, s64, s79\n"
    "v_exp_f32 v110, v110\n"
    "v_mfma_f32_16x16x32_bf16 v[96:99], a[168:171], v[56:59], v[96:99]\n"
    "ds_read_b128 a[196:199], v5 offset:1024\n"
    "v_cvt_pk_bf16_f32 v197, v106, v107\n"
    "v_mfma_f32_16x16x32_bf16 v[96:99], a[172:175], v[72:75], v[96:99]\n"
    "s_waitcnt lgkmcnt(2)\n"
    "v_exp_f32 v111, v111\n"
    "v_mfma_f32_16x16x32_bf16 v[100:103], a[176:179], v[216:219], v[84:87]\n"
    "s_addk_i32 s64, 0x4000\n"
    "ds_read_b128 a[200:203], v5 offset:2048\n"
    "v_cvt_pk_bf16_f32 v198, v108, v109\n"
    "v_mfma_f32_16x16x32_bf16 v[100:103], a[180:183], v[40:43], v[100:103]\n"
    "buffer_load_dwordx4 v0, s[40:43], s83 offen offset:0 sc1 lds\n"
    "v_mfma_f32_16x16x32_bf16 v[100:103], a[184:187], v[56:59], v[100:103]\n"
    "s_and_b32 s64, s64, 0x1ffff\n"
    "ds_read_b128 a[204:207], v5 offset:3072\n"
    "v_cvt_pk_bf16_f32 v199, v110, v111\n"
    "v_mfma_f32_16x16x32_bf16 v[100:103], a[188:191], v[72:75], v[100:103]\n"
    "s_add_u32 s80, s84, 0x4000\n"
    "v_exp_f32 v96, v96\n"
    "v_mfma_f32_16x16x32_bf16 v[104:107], a[160:163], v[220:223], v[88:91]\n"
    "s_cmp_eq_u32 s80, s85\n"
    "ds_read_b128 a[208:211], v5 offset:4096\n"
    "v_add_u32 v4, 0x4000, v4\n"
    "v_mfma_f32_16x16x32_bf16 v[104:107], a[164:167], v[44:47], v[104:107]\n"
    "s_cselect_b32 s84, s27, s80\n"
    "v_exp_f32 v97, v97\n"
    "v_mfma_f32_16x16x32_bf16 v[104:107], a[168:171], v[60:63], v[104:107]\n"
    "s_cmp_eq_u32 s80, s26\n"
    "ds_read_b128 a[212:215], v5 offset:5120\n"
    "v_and_b32 v4, 0x1ffff, v4\n"
    "v_mfma_f32_16x16x32_bf16 v[104:107], a[172:175], v[76:79], v[104:107]\n"
    "s_cselect_b32 s84, s88, s84\n"
    "v_exp_f32 v98, v98\n"
    "v_mfma_f32_16x16x32_bf16 v[108:111], a[176:179], v[220:223], v[92:95]\n"
    "s_cmp_eq_u32 s80, s86\n"
    "ds_read_b128 a[216:219], v5 offset:6144\n"
    "v_exp_f32 v99, v99\n"
    "v_mfma_f32_16x16x32_bf16 v[108:111], a[180:183], v[44:47], v[108:111]\n"
    "buffer_load_dwordx4 v0, s[40:43], s83 offen offset:1024 sc1 lds\n"
    "v_mfma_f32_16x16x32_bf16 v[108:111], a[184:187], v[60:63], v[108:111]\n"
    "s_cselect_b32 s84, s78, s84\n"
    "ds_read_b128 a[220:223], v5 offset:7168\n"
    "v_add_u32 v5, 0x4000, v5\n"
    "v_mfma_f32_16x16x32_bf16 v[108:111], a[188:191], v[76:79], v[108:111]\n"
    "s_waitcnt lgkmcnt(6)\n"
    "v_exp_f32 v100, v100\n"
    "v_mfma_f32_16x16x32_bf16 a[0:3], a[192:195], v[192:195], a[0:3]\n"
    "s_cmp_eq_u32 s80, s87\n"
    "ds_read_b128 a[160:163], v4 offset:8192\n"
    "v_and_b32 v5, 0x1ffff, v5\n"
    "v_mfma_f32_16x16x32_bf16 a[8:11], a[196:199], v[192:195], a[8:11]\n"
    "s_cselect_b32 s84, 0xc0000000, s84\n"
    "v_exp_f32 v101, v101\n"
    "v_smfmac_f32_16x16x64_bf16 v[240:243], v[200:203], v[192:199], v204\n"
    "s_waitcnt lgkmcnt(4)\n"
    "ds_read_b128 a[164:167], v4 offset:9216\n"
    "v_mfma_f32_16x16x32_bf16 a[16:19], a[200:203], v[192:195], a[16:19]\n"
    "s_cselect_b32 s79, s55, s79\n"
    "v_exp_f32 v102, v102\n"
    "v_mfma_f32_16x16x32_bf16 a[24:27], a[204:207], v[192:195], a[24:27]\n"
    "ds_read_b128 a[168:171], v4 offset:10240\n"
    "v_exp_f32 v103, v103\n"
    "v_mfma_f32_16x16x32_bf16 a[32:35], a[208:211], v[192:195], a[32:35]\n"
    "s_waitcnt lgkmcnt(3)\n"
    "v_exp_f32 v104, v104\n"
    "v_mfma_f32_16x16x32_bf16 a[40:43], a[212:215], v[192:195], a[40:43]\n"
    "ds_read_b128 a[172:175], v4 offset:11264\n"
    "v_exp_f32 v105, v105\n"
    "v_mfma_f32_16x16x32_bf16 a[48:51], a[216:219], v[192:195], a[48:51]\n"
    "buffer_load_dwordx4 v0, s[40:43], s83 offen offset:2048 sc1 lds\n"
    "v_mfma_f32_16x16x32_bf16 a[56:59], a[220:223], v[192:195], a[56:59]\n"
    "ds_read_b128 a[176:179], v4 offset:12288\n"
    "v_cvt_pk_bf16_f32 v192, v96, v97\n"
    "v_mfma_f32_16x16x32_bf16 a[4:7], a[192:195], v[196:199], a[4:7]\n"
    "v_exp_f32 v106, v106\n"
    "v_mfma_f32_16x16x32_bf16 a[12:15], a[196:199], v[196:199], a[12:15]\n"
    "ds_read_b128 a[180:183], v4 offset:13312\n"
    "v_cvt_pk_bf16_f32 v193, v98, v99\n"
    "v_mfma_f32_16x16x32_bf16 a[20:23], a[200:203], v[196:199], a[20:23]\n"
    "v_exp_f32 v107, v107\n"
    "v_mfma_f32_16x16x32_bf16 a[28:31], a[204:207], v[196:199], a[28:31]\n"
    "ds_read_b128 a[184:187], v4 offset:14336\n"
    "v_cvt_pk_bf16_f32 v194, v100, v101\n"
    "v_mfma_f32_16x16x32_bf16 a[36:39], a[208:211], v[196:199], a[36:39]\n"
    "s_cmp_eq_u32 s63, 3\n"
    "v_exp_f32 v108, v108\n"
    "v_mfma_f32_16x16x32_bf16 a[44:47], a[212:215], v[196:199], a[44:47]\n"
    "ds_read_b128 a[188:191], v4 offset:15360\n"
    "v_cvt_pk_bf16_f32 v195, v102, v103\n"
    "v_mfma_f32_16x16x32_bf16 a[52:55], a[216:219], v[196:199], a[52:55]\n"
    "s_waitcnt lgkmcnt(4)\n"
    "v_exp_f32 v109, v109\n"
    "v_mfma_f32_16x16x32_bf16 a[60:63], a[220:223], v[196:199], a[60:63]\n"
    "buffer_load_dwordx4 v0, s[40:43], s83 offen offset:3072 sc1 lds\n"
    "s_cbranch_scc1 T_BL_%=\n"
    "v_mfma_f32_16x16x32_bf16 v[96:99], a[160:163], v[216:219], v[80:83]\n"
    "ds_read_b128 a[192:195], v5 offset:0\n"
    "v_cvt_pk_bf16_f32 v196, v104, v105\n"
    "v_mfma_f32_16x16x32_bf16 v[96:99], a[164:167], v[40:43], v[96:99]\n"
    "v_exp_f32 v110, v110\n"
    "v_mfma_f32_16x16x32_bf16 v[96:99], a[168:171], v[56:59], v[96:99]\n"
    "ds_read_b128 a[196:199], v5 offset:1024\n"
    "v_cvt_pk_bf16_f32 v197, v106, v107\n"
    "v_mfma_f32_16x16x32_bf16 v[96:99], a[172:175], v[72:75], v[96:99]\n"
    "s_waitcnt lgkmcnt(2)\n"
    "v_exp_f32 v111, v111\n"
    "v_mfma_f32_16x16x32_bf16 v[100:103], a[176:179], v[216:219], v[84:87]\n"
    "ds_read_b128 a[200:203], v5 offset:2048\n"
    "v_cvt_pk_bf16_f32 v198, v108, v109\n"
    "v_mfma_f32_16x16x32_bf16 v[100:103], a[180:183], v[40:43], v[100:103]\n"
    "v_mfma_f32_16x16x32_bf16 v[100:103], a[184:187], v[56:59], v[100:103]\n"
    "ds_read_b128 a[204:207], v5 offset:3072\n"
    "v_cvt_pk_bf16_f32 v199, v110, v111\n"
    "v_mfma_f32_16x16x32_bf16 v[100:103], a[188:191], v[72:75], v[100:103]\n"
    "v_exp_f32 v96, v96\n"
    "v_mfma_f32_16x16x32_bf16 v[104:107], a[160:163], v[220:223], v[88:91]\n"
    "ds_read_b128 a[208:211], v5 offset:4096\n"
    "v_add_u32 v4, 0x4000, v4\n"
    "v_mfma_f32_16x16x32_bf16 v[104:107], a[164:167], v[44:47], v[104:107]\n"
    "v_exp_f32 v97, v97\n"
    "v_mfma_f32_16x16x32_bf16 v[104:107], a[168:171], v[60:63], v[104:107]\n"
    "ds_read_b128 a[212:215], v5 offset:5120\n"
    "v_and_b32 v4, 0x1ffff, v4\n"
    "v_mfma_f32_16x16x32_bf16 v[104:107], a[172:175], v[76:79], v[104:107]\n"
    "v_exp_f32 v98, v98\n"
    "v_mfma_f32_16x16x32_bf16 v[108:111], a[176:179], v[220:223], v[92:95]\n"
    "ds_read_b128 a[216:219], v5 offset:6144\n"
    "v_mfma_f32_16x16x32_bf16 v[108:111], a[180:183], v[44:47], v[108:111]\n"
    "v_exp_f32 v99, v99\n"
    "v_mfma_f32_16x16x32_bf16 v[108:111], a[184:187], v[60:63], v[108:111]\n"
    "ds_read_b128 a[220:223], v5 offset:7168\n"
    "v_add_u32 v5, 0x4000, v5\n"
    "v_mfma_f32_16x16x32_bf16 v[108:111], a[188:191], v[76:79], v[108:111]\n"
    "s_waitcnt lgkmcnt(6)\n"
    "v_exp_f32 v100, v100\n"
    "v_mfma_f32_16x16x32_bf16 a[0:3], a[192:195], v[192:195], a[0:3]\n"
    "ds_read_b128 a[160:163], v4 offset:8192\n"
    "v_and_b32 v5, 0x1ffff, v5\n"
    "v_mfma_f32_16x16x32_bf16 a[8:11], a[196:199], v[192:195], a[8:11]\n"
    "v_exp_f32 v101, v101\n"
    "v_smfmac_f32_16x16x64_bf16 v[240:243], v[200:203], v[192:199], v204\n"
    "s_waitcnt lgkmcnt(4)\n"
    "ds_read_b128 a[164:167], v4 offset:9216\n"
    "v_mfma_f32_16x16x32_bf16 a[16:19], a[200:203], v[192:195], a[16:19]\n"
    "v_exp_f32 v102, v102\n"
    "v_mfma_f32_16x16x32_bf16 a[24:27], a[204:207], v[192:195], a[24:27]\n"
    "ds_read_b128 a[168:171], v4 offset:10240\n"
    "v_mfma_f32_16x16x32_bf16 a[32:35], a[208:211], v[192:195], a[32:35]\n"
    "s_waitcnt lgkmcnt(3)\n"
    "v_exp_f32 v103, v103\n"
    "v_mfma_f32_16x16x32_bf16 a[40:43], a[212:215], v[192:195], a[40:43]\n"
    "ds_read_b128 a[172:175], v4 offset:11264\n"
    "v_mfma_f32_16x16x32_bf16 a[48:51], a[216:219], v[192:195], a[48:51]\n"
    "v_exp_f32 v104, v104\n"
    "v_mfma_f32_16x16x32_bf16 a[56:59], a[220:223], v[192:195], a[56:59]\n"
    "ds_read_b128 a[176:179], v4 offset:12288\n"
    "v_cvt_pk_bf16_f32 v192, v96, v97\n"
    "v_mfma_f32_16x16x32_bf16 a[4:7], a[192:195], v[196:199], a[4:7]\n"
    "v_exp_f32 v105, v105\n"
    "v_mfma_f32_16x16x32_bf16 a[12:15], a[196:199], v[196:199], a[12:15]\n"
    "ds_read_b128 a[180:183], v4 offset:13312\n"
    "v_cvt_pk_bf16_f32 v193, v98, v99\n"
    "v_mfma_f32_16x16x32_bf16 a[20:23], a[200:203], v[196:199], a[20:23]\n"
    "v_exp_f32 v106, v106\n"
    "v_mfma_f32_16x16x32_bf16 a[28:31], a[204:207], v[196:199], a[28:31]\n"
    "s_add_u32 s63, s63, 1\n"
    "ds_read_b128 a[184:187], v4 offset:14336\n"
    "v_cvt_pk_bf16_f32 v194, v100, v101\n"
    "v_mfma_f32_16x16x32_bf16 a[36:39], a[208:211], v[196:199], a[36:39]\n"
    "s_cmp_eq_u32 s63, 2\n"
    "v_exp_f32 v107, v107\n"
    "v_mfma_f32_16x16x32_bf16 a[44:47], a[212:215], v[196:199], a[44:47]\n"
    "ds_read_b128 a[188:191], v4 offset:15360\n"
    "v_cvt_pk_bf16_f32 v195, v102, v103\n"
    "v_mfma_f32_16x16x32_bf16 a[52:55], a[216:219], v[196:199], a[52:55]\n"
    "s_waitcnt lgkmcnt(4)\n"
    "v_exp_f32 v108, v108\n"
    "v_mfma_f32_16x16x32_bf16 a[60:63], a[220:223], v[196:199], a[60:63]\n"
    "v_exp_f32 v109, v109\n"
    "s_cbranch_scc1 T_BAR_%=\n"
    "s_branch T_B_%=\n"
    "T_BL_%=:\n"
    "v_cndmask_b32 v80, v2, v80, s[92:93]\n"
    "v_cndmask_b32 v81, v2, v81, s[94:95]\n"
    "v_cndmask_b32 v82, v2, v82, s[96:97]\n"
    "v_cndmask_b32 v83, v2, v83, s[98:99]\n"
    "v_cndmask_b32 v92, v2, v92, s[92:93]\n"
    "v_cndmask_b32 v93, v2, v93, s[94:95]\n"
    "v_cndmask_b32 v94, v2, v94, s[96:97]\n"
    "v_cndmask_b32 v95, v2, v95, s[98:99]\n"
    "v_mov_b32 v84, v2\n"
    "v_mov_b32 v85, v2\n"
    "v_mov_b32 v86, v2\n"
    "v_mov_b32 v87, v2\n"
    "s_nop 0\n"
    "v_mfma_f32_16x16x32_bf16 v[96:99], a[160:163], v[216:219], v[80:83]\n"
    "ds_read_b128 a[192:195], v5 offset:0\n"
    "v_cvt_pk_bf16_f32 v196, v104, v105\n"
    "v_mfma_f32_16x16x32_bf16 v[96:99], a[164:167], v[40:43], v[96:99]\n"
    "v_exp_f32 v110, v110\n"
    "v_mfma_f32_16x16x32_bf16 v[96:99], a[168:171], v[56:59], v[96:99]\n"
    "ds_read_b128 a[196:199], v5 offset:1024\n"
    "v_cvt_pk_bf16_f32 v197, v106, v107\n"
    "v_mfma_f32_16x16x32_bf16 v[96:99], a[172:175], v[72:75], v[96:99]\n"
    "s_waitcnt lgkmcnt(2)\n"
    "v_exp_f32 v111, v111\n"
    "v_mfma_f32_16x16x32_bf16 v[100:103], a[176:179], v[216:219], v[84:87]\n"
    "ds_read_b128 a[200:203], v5 offset:2048\n"
    "v_cvt_pk_bf16_f32 v198, v108, v109\n"
    "v_mfma_f32_16x16x32_bf16 v[100:103], a[180:183], v[40:43], v[100:103]\n"
    "v_mfma_f32_16x16x32_bf16 v[100:103], a[184:187], v[56:59], v[100:103]\n"
    "ds_read_b128 a[204:207], v5 offset:3072\n"
    "v_cvt_pk_bf16_f32 v199, v110, v111\n"
    "v_mfma_f32_16x16x32_bf16 v[100:103], a[188:191], v[72:75], v[100:103]\n"
    "v_exp_f32 v96, v96\n"
    "v_mfma_f32_16x16x32_bf16 v[104:107], a[160:163], v[220:223], v[88:91]\n"
    "ds_read_b128 a[208:211], v5 offset:4096\n"
    "v_mfma_f32_16x16x32_bf16 v[104:107], a[164:167], v[44:47], v[104:107]\n"
    "v_exp_f32 v97, v97\n"
    "v_mfma_f32_16x16x32_bf16 v[104:107], a[168:171], v[60:63], v[104:107]\n"
    "ds_read_b128 a[212:215], v5 offset:5120\n"
    "v_mfma_f32_16x16x32_bf16 v[104:107], a[172:175], v[76:79], v[104:107]\n"
    "v_exp_f32 v98, v98\n"
    "v_mfma_f32_16x16x32_bf16 v[108:111], a[176:179], v[220:223], v[92:95]\n"
    "ds_read_b128 a[216:219], v5 offset:6144\n"
    "v_mfma_f32_16x16x32_bf16 v[108:111], a[180:183], v[44:47], v[108:111]\n"
    "v_exp_f32 v99, v99\n"
    "v_mfma_f32_16x16x32_bf16 v[108:111], a[184:187], v[60:63], v[108:111]\n"
    "ds_read_b128 a[220:223], v5 offset:7168\n"
    "v_add_u32 v5, 0x4000, v5\n"
    "v_mfma_f32_16x16x32_bf16 v[108:111], a[188:191], v[76:79], v[108:111]\n"
    "s_waitcnt lgkmcnt(6)\n"
    "v_exp_f32 v100, v100\n"
    "v_mfma_f32_16x16x32_bf16 a[0:3], a[192:195], v[192:195], a[0:3]\n"
    "v_and_b32 v5, 0x1ffff, v5\n"
    "v_mfma_f32_16x16x32_bf16 a[8:11], a[196:199], v[192:195], a[8:11]\n"
    "v_exp_f32 v101, v101\n"
    "v_smfmac_f32_16x16x64_bf16 v[240:243], v[200:203], v[192:199], v204\n"
    "s_waitcnt lgkmcnt(3)\n"
    "v_mfma_f32_16x16x32_bf16 a[16:19], a[200:203], v[192:195], a[16:19]\n"
    "v_exp_f32 v102, v102\n"
    "v_mfma_f32_16x16x32_bf16 a[24:27], a[204:207], v[192:195], a[24:27]\n"
    "v_mfma_f32_16x16x32_bf16 a[32:35], a[208:211], v[192:195], a[32:35]\n"
    "s_waitcnt lgkmcnt(0)\n"
    "v_exp_f32 v103, v103\n"
    "v_mfma_f32_16x16x32_bf16 a[40:43], a[212:215], v[192:195], a[40:43]\n"
    "v_mfma_f32_16x16x32_bf16 a[48:51], a[216:219], v[192:195], a[48:51]\n"
    "v_exp_f32 v104, v104\n"
    "v_mfma_f32_16x16x32_bf16 a[56:59], a[220:223], v[192:195], a[56:59]\n"
    "v_cvt_pk_bf16_f32 v192, v96, v97\n"
    "v_mfma_f32_16x16x32_bf16 a[4:7], a[192:195], v[196:199], a[4:7]\n"
    "v_exp_f32 v105, v105\n"
    "v_mfma_f32_16x16x32_bf16 a[12:15], a[196:199], v[196:199], a[12:15]\n"
    "v_cvt_pk_bf16_f32 v193, v98, v99\n"
    "v_mfma_f32_16x16x32_bf16 a[20:23], a[200:203], v[196:199], a[20:23]\n"
    "v_exp_f32 v106, v106\n"
    "v_mfma_f32_16x16x32_bf16 a[28:31], a[204:207], v[196:199], a[28:31]\n"
    "v_cvt_pk_bf16_f32 v194, v100, v101\n"
    "v_mfma_f32_16x16x32_bf16 a[36:39], a[208:211], v[196:199], a[36:39]\n"
    "v_exp_f32 v107, v107\n"
    "v_mfma_f32_16x16x32_bf16 a[44:47], a[212:215], v[196:199], a[44:47]\n"
    "v_cvt_pk_bf16_f32 v195, v102, v103\n"
    "v_mfma_f32_16x16x32_bf16 a[52:55], a[216:219], v[196:199], a[52:55]\n"
    "v_exp_f32 v108, v108\n"
    "v_mfma_f32_16x16x32_bf16 a[60:63], a[220:223], v[196:199], a[60:63]\n"
    "v_exp_f32 v109, v109\n"
    "ATT_DRAIN_%=:\n"
    "ds_read_b128 a[192:195], v5 offset:0\n"
    "ds_read_b128 a[196:199], v5 offset:1024\n"
    "ds_read_b128 a[200:203], v5 offset:2048\n"
    "ds_read_b128 a[204:207], v5 offset:3072\n"
    "ds_read_b128 a[208:211], v5 offset:4096\n"
    "ds_read_b128 a[212:215], v5 offset:5120\n"
    "ds_read_b128 a[216:219], v5 offset:6144\n"
    "ds_read_b128 a[220:223], v5 offset:7168\n"
    "v_cvt_pk_bf16_f32 v196, v104, v105\n"
    "v_exp_f32 v110, v110\n"
    "v_cvt_pk_bf16_f32 v197, v106, v107\n"
    "v_exp_f32 v111, v111\n"
    "v_cvt_pk_bf16_f32 v198, v108, v109\n"
    "v_cvt_pk_bf16_f32 v199, v110, v111\n"
    "s_bitcmp1_b32 s74, 31\n"
    "s_mov_b32 s88, 55296\n"
    "s_cselect_b32 s88, 0xffff2800, s88\n"
    "s_waitcnt lgkmcnt(0)\n"
    "v_mfma_f32_16x16x32_bf16 a[0:3], a[192:195], v[192:195], a[0:3]\n"
    "v_mfma_f32_16x16x32_bf16 a[8:11], a[196:199], v[192:195], a[8:11]\n"
    "v_smfmac_f32_16x16x64_bf16 v[240:243], v[200:203], v[192:199], v204\n"
    "v_mfma_f32_16x16x32_bf16 a[16:19], a[200:203], v[192:195], a[16:19]\n"
    "v_mfma_f32_16x16x32_bf16 a[24:27], a[204:207], v[192:195], a[24:27]\n"
    "v_mfma_f32_16x16x32_bf16 a[32:35], a[208:211], v[192:195], a[32:35]\n"
    "v_mfma_f32_16x16x32_bf16 a[40:43], a[212:215], v[192:195], a[40:43]\n"
    "v_mfma_f32_16x16x32_bf16 a[48:51], a[216:219], v[192:195], a[48:51]\n"
    "v_mfma_f32_16x16x32_bf16 a[56:59], a[220:223], v[192:195], a[56:59]\n"
    "v_mfma_f32_16x16x32_bf16 a[4:7], a[192:195], v[196:199], a[4:7]\n"
    "s_nop 0\n"
    "v_mfma_f32_16x16x32_bf16 a[12:15], a[196:199], v[196:199], a[12:15]\n"
    "s_nop 0\n"
    "v_mfma_f32_16x16x32_bf16 a[20:23], a[200:203], v[196:199], a[20:23]\n"
    "s_nop 0\n"
    "v_mfma_f32_16x16x32_bf16 a[28:31], a[204:207], v[196:199], a[28:31]\n"
    "s_nop 0\n"
    "v_mfma_f32_16x16x32_bf16 a[36:39], a[208:211], v[196:199], a[36:39]\n"
    "v_add_f32 v244, v242, v243\n"
    "v_add_f32 v240, v240, v241\n"
    "v_mfma_f32_16x16x32_bf16 a[44:47], a[212:215], v[196:199], a[44:47]\n"
    "v_rcp_f32 v28, v240\n"
    "v_mfma_f32_16x16x32_bf16 a[52:55], a[216:219], v[196:199], a[52:55]\n"
    "v_accvgpr_read_b32 v96, a0\n"
    "v_accvgpr_read_b32 v97, a1\n"
    "v_mfma_f32_16x16x32_bf16 a[60:63], a[220:223], v[196:199], a[60:63]\n"
    "v_accvgpr_read_b32 v98, a2\n"
    "v_accvgpr_read_b32 v99, a3\n"
    "s_barrier\n"
    "s_add_u32 s59, s69, 0\n"
    "s_mul_i32 s60, s59, 40\n"
    "s_add_u32 s60, s60, s68\n"
    "s_add_u32 m0, s82, 0x8000\n"
    "v_accvgpr_read_b32 v100, a8\n"
    "v_accvgpr_read_b32 v101, a9\n"
    "v_accvgpr_read_b32 v102, a10\n"
    "v_accvgpr_read_b32 v103, a11\n"
    "v_mul_f32 v96, v96, v28\n"
    "v_mul_f32 v97, v97, v28\n"
    "v_mul_f32 v98, v98, v28\n"
    "buffer_load_dwordx4 v0, s[40:43], s60 offen offset:0 sc1 lds\n"
    "v_mul_f32 v99, v99, v28\n"
    "v_mul_f32 v100, v100, v28\n"
    "v_mul_f32 v101, v101, v28\n"
    "v_mul_f32 v102, v102, v28\n"
    "v_mul_f32 v103, v103, v28\n"
    "v_cvt_pk_bf16_f32 v224, v96, v97\n"
    "v_cvt_pk_bf16_f32 v225, v98, v99\n"
    "buffer_load_dwordx4 v0, s[40:43], s60 offen offset:1024 sc1 lds\n"
    "v_cvt_pk_bf16_f32 v226, v100, v101\n"
    "v_cvt_pk_bf16_f32 v227, v102, v103\n"
    "v_accvgpr_read_b32 v104, a16\n"
    "v_accvgpr_read_b32 v105, a17\n"
    "v_accvgpr_read_b32 v106, a18\n"
    "v_accvgpr_read_b32 v107, a19\n"
    "v_accvgpr_read_b32 v108, a24\n"
    "buffer_load_dwordx4 v0, s[40:43], s60 offen offset:2048 sc1 lds\n"
    "s_lshl_b32 s61, s76, 5\n"
    "s_add_u32 s61, s61, s56\n"
    "s_lshl_b32 s61, s61, 12\n"
    "s_add_u32 s61, s61, 0x29c0000\n"
    "v_accvgpr_read_b32 v109, a25\n"
    "v_accvgpr_read_b32 v110, a26\n"
    "v_accvgpr_read_b32 v111, a27\n"
    "v_mul_f32 v104, v104, v28\n"
    "v_mul_f32 v105, v105, v28\n"
    "v_mul_f32 v106, v106, v28\n"
    "v_mul_f32 v107, v107, v28\n"
    "buffer_store_dwordx4 v[224:227], v0, s[40:43], s61 offen offset:0 sc1\n"
    "v_mul_f32 v108, v108, v28\n"
    "v_mul_f32 v109, v109, v28\n"
    "v_mul_f32 v110, v110, v28\n"
    "v_mul_f32 v111, v111, v28\n"
    "v_cvt_pk_bf16_f32 v228, v104, v105\n"
    "v_cvt_pk_bf16_f32 v229, v106, v107\n"
    "v_cvt_pk_bf16_f32 v230, v108, v109\n"
    "buffer_load_dwordx4 v0, s[40:43], s60 offen offset:3072 sc1 lds\n"
    "s_addk_i32 s60, 0x1000\n"
    "s_addk_i32 m0, 0x1000\n"
    "v_cvt_pk_bf16_f32 v231, v110, v111\n"
    "v_accvgpr_read_b32 v96, a32\n"
    "v_accvgpr_read_b32 v97, a33\n"
    "v_accvgpr_read_b32 v98, a34\n"
    "v_accvgpr_read_b32 v99, a35\n"
    "v_accvgpr_read_b32 v100, a40\n"
    "v_accvgpr_read_b32 v101, a41\n"
    "buffer_load_dwordx4 v0, s[40:43], s60 offen offset:0 sc1 lds\n"
    "v_accvgpr_read_b32 v102, a42\n"
    "v_accvgpr_read_b32 v103, a43\n"
    "v_mul_f32 v96, v96, v28\n"
    "v_mul_f32 v97, v97, v28\n"
    "v_mul_f32 v98, v98, v28\n"
    "v_mul_f32 v99, v99, v28\n"
    "v_mul_f32 v100, v100, v28\n"
    "buffer_store_dwordx4 v[228:231], v0, s[40:43], s61 offen offset:1024 sc1\n"
    "s_add_u32 s59, s69, 0x800\n"
    "s_mul_i32 s60, s59, 40\n"
    "s_add_u32 s60, s60, s68\n"
    "s_add_u32 m0, s82, 0xd000\n"
    "v_mul_f32 v101, v101, v28\n"
    "v_mul_f32 v102, v102, v28\n"
    "v_mul_f32 v103, v103, v28\n"
    "v_cvt_pk_bf16_f32 v232, v96, v97\n"
    "v_cvt_pk_bf16_f32 v233, v98, v99\n"
    "v_cvt_pk_bf16_f32 v234, v100, v101\n"
    "v_cvt_pk_bf16_f32 v235, v102, v103\n"
    "buffer_load_dwordx4 v0, s[40:43], s60 offen offset:0 sc1 lds\n"
    "v_accvgpr_read_b32 v104, a48\n"
    "v_accvgpr_read_b32 v105, a49\n"
    "v_accvgpr_read_b32 v106, a50\n"
    "v_accvgpr_read_b32 v107, a51\n"
    "v_accvgpr_read_b32 v108, a56\n"
    "v_accvgpr_read_b32 v109, a57\n"
    "v_accvgpr_read_b32 v110, a58\n"
    "buffer_store_dwordx4 v[232:235], v0, s[40:43], s61 offen offset:2048 sc1\n"
    "v_accvgpr_read_b32 v111, a59\n"
    "v_mul_f32 v104, v104, v28\n"
    "v_mul_f32 v105, v105, v28\n"
    "v_mul_f32 v106, v106, v28\n"
    "v_mul_f32 v107, v107, v28\n"
    "v_mul_f32 v108, v108, v28\n"
    "v_mul_f32 v109, v109, v28\n"
    "buffer_load_dwordx4 v0, s[40:43], s60 offen offset:1024 sc1 lds\n"
    "v_mul_f32 v110, v110, v28\n"
    "v_mul_f32 v111, v111, v28\n"
    "v_cvt_pk_bf16_f32 v236, v104, v105\n"
    "v_cvt_pk_bf16_f32 v237, v106, v107\n"
    "v_cvt_pk_bf16_f32 v238, v108, v109\n"
    "v_cvt_pk_bf16_f32 v239, v110, v111\n"
    "v_rcp_f32 v29, v244\n"
    "buffer_load_dwordx4 v0, s[40:43], s60 offen offset:2048 sc1 lds\n"
    "v_accvgpr_read_b32 v96, a4\n"
    "v_accvgpr_read_b32 v97, a5\n"
    "v_accvgpr_read_b32 v98, a6\n"
    "v_accvgpr_read_b32 v99, a7\n"
    "v_accvgpr_read_b32 v100, a12\n"
    "v_accvgpr_read_b32 v101, a13\n"
    "v_accvgpr_read_b32 v102, a14\n"
    "buffer_store_dwordx4 v[236:239], v0, s[40:43], s61 offen offset:3072 sc1\n"
    "v_accvgpr_read_b32 v103, a15\n"
    "v_mul_f32 v96, v96, v29\n"
    "v_mul_f32 v97, v97, v29\n"
    "v_mul_f32 v98, v98, v29\n"
    "v_mul_f32 v99, v99, v29\n"
    "v_mul_f32 v100, v100, v29\n"
    "v_mul_f32 v101, v101, v29\n"
    "buffer_load_dwordx4 v0, s[40:43], s60 offen offset:3072 sc1 lds\n"
    "s_addk_i32 s60, 0x1000\n"
    "s_addk_i32 m0, 0x1000\n"
    "v_mul_f32 v102, v102, v29\n"
    "v_mul_f32 v103, v103, v29\n"
    "v_cvt_pk_bf16_f32 v192, v96, v97\n"
    "v_cvt_pk_bf16_f32 v193, v98, v99\n"
    "v_cvt_pk_bf16_f32 v194, v100, v101\n"
    "v_cvt_pk_bf16_f32 v195, v102, v103\n"
    "v_accvgpr_read_b32 v104, a20\n"
    "buffer_load_dwordx4 v0, s[40:43], s60 offen offset:0 sc1 lds\n"
    "s_add_u32 s61, s61, 0x20000\n"
    "v_accvgpr_read_b32 v105, a21\n"
    "v_accvgpr_read_b32 v106, a22\n"
    "v_accvgpr_read_b32 v107, a23\n"
    "v_accvgpr_read_b32 v108, a28\n"
    "v_accvgpr_read_b32 v109, a29\n"
    "v_accvgpr_read_b32 v110, a30\n"
    "v_accvgpr_read_b32 v111, a31\n"
    "buffer_store_dwordx4 v[192:195], v0, s[40:43], s61 offen offset:0 sc1\n"
    "v_mul_f32 v104, v104, v29\n"
    "v_mul_f32 v105, v105, v29\n"
    "v_mul_f32 v106, v106, v29\n"
    "v_mul_f32 v107, v107, v29\n"
    "v_mul_f32 v108, v108, v29\n"
    "v_mul_f32 v109, v109, v29\n"
    "v_mul_f32 v110, v110, v29\n"
    "v_mul_f32 v111, v111, v29\n"
    "v_cvt_pk_bf16_f32 v196, v104, v105\n"
    "v_cvt_pk_bf16_f32 v197, v106, v107\n"
    "v_cvt_pk_bf16_f32 v198, v108, v109\n"
    "v_cvt_pk_bf16_f32 v199, v110, v111\n"
    "v_accvgpr_read_b32 v96, a36\n"
    "v_accvgpr_read_b32 v97, a37\n"
    "v_accvgpr_read_b32 v98, a38\n"
    "v_accvgpr_read_b32 v99, a39\n"
    "v_accvgpr_read_b32 v100, a44\n"
    "v_accvgpr_read_b32 v101, a45\n"
    "v_accvgpr_read_b32 v102, a46\n"
    "v_accvgpr_read_b32 v103, a47\n"
    "v_mul_f32 v96, v96, v29\n"
    "buffer_store_dwordx4 v[196:199], v0, s[40:43], s61 offen offset:1024 sc1\n"
    "v_mul_f32 v97, v97, v29\n"
    "v_mul_f32 v98, v98, v29\n"
    "v_mul_f32 v99, v99, v29\n"
    "v_mul_f32 v100, v100, v29\n"
    "v_mul_f32 v101, v101, v29\n"
    "v_mul_f32 v102, v102, v29\n"
    "v_mul_f32 v103, v103, v29\n"
    "v_cvt_pk_bf16_f32 v200, v96, v97\n"
    "v_cvt_pk_bf16_f32 v201, v98, v99\n"
    "v_cvt_pk_bf16_f32 v202, v100, v101\n"
    "v_cvt_pk_bf16_f32 v203, v102, v103\n"
    "v_accvgpr_read_b32 v104, a52\n"
    "v_accvgpr_read_b32 v105, a53\n"
    "v_accvgpr_read_b32 v106, a54\n"
    "v_accvgpr_read_b32 v107, a55\n"
    "v_accvgpr_read_b32 v108, a60\n"
    "v_accvgpr_read_b32 v109, a61\n"
    "v_accvgpr_read_b32 v110, a62\n"
    "v_accvgpr_read_b32 v111, a63\n"
    "v_mul_f32 v104, v104, v29\n"
    "v_mul_f32 v105, v105, v29\n"
    "buffer_store_dwordx4 v[200:203], v0, s[40:43], s61 offen offset:2048 sc1\n"
    "v_mul_f32 v106, v106, v29\n"
    "v_mul_f32 v107, v107, v29\n"
    "v_mul_f32 v108, v108, v29\n"
    "v_mul_f32 v109, v109, v29\n"
    "v_mul_f32 v110, v110, v29\n"
    "v_mul_f32 v111, v111, v29\n"
    "v_cvt_pk_bf16_f32 v204, v104, v105\n"
    "v_cvt_pk_bf16_f32 v205, v106, v107\n"
    "v_cvt_pk_bf16_f32 v206, v108, v109\n"
    "v_cvt_pk_bf16_f32 v207, v110, v111\n"
    "s_nop 0\n"
    "buffer_store_dwordx4 v[204:207], v0, s[40:43], s61 offen offset:3072 sc1\n"
    "s_bitcmp1_b32 %[bid], 0\n"
    "s_cbranch_scc1 OWN_H_%=\n"
    "s_add_u32 s60, s68, 0xfff74000\n"
    "s_add_u32 m0, s82, 0x1c000\n"
    "ds_write_b128 v20, v[128:131] offset:0\n"
    "ds_write_b128 v20, v[132:135] offset:1024\n"
    "buffer_load_dwordx4 v0, s[40:43], s60 offen offset:0 sc1 lds\n"
    "ds_write_b128 v20, v[136:139] offset:16384\n"
    "ds_write_b128 v20, v[140:143] offset:17408\n"
    "buffer_load_dwordx4 v0, s[40:43], s60 offen offset:1024 sc1 lds\n"
    "ds_write_b128 v21, v[144:147] offset:0\n"
    "ds_write_b128 v21, v[148:151] offset:1024\n"
    "buffer_load_dwordx4 v0, s[40:43], s60 offen offset:2048 sc1 lds\n"
    "ds_write_b128 v21, v[152:155] offset:16384\n"
    "ds_write_b128 v21, v[156:159] offset:17408\n"
    "buffer_load_dwordx4 v0, s[40:43], s60 offen offset:3072 sc1 lds\n"
    "s_addk_i32 s60, 0x1000\n"
    "s_addk_i32 m0, 0x1000\n"
    "s_nop 0\n"
    "buffer_load_dwordx4 v0, s[40:43], s60 offen offset:0 sc1 lds\n"
    "s_branch OWN_DONE_%=\n"
    "OWN_H_%=:\n"
    "s_add_u32 s60, s68, 0xfff74000\n"
    "s_add_u32 m0, s82, 0x1c000\n"
    "ds_write_b128 v20, v[224:227] offset:0\n"
    "ds_write_b128 v20, v[228:231] offset:1024\n"
    "buffer_load_dwordx4 v0, s[40:43], s60 offen offset:0 sc1 lds\n"
    "ds_write_b128 v20, v[232:235] offset:16384\n"
    "ds_write_b128 v20, v[236:239] offset:17408\n"
    "buffer_load_dwordx4 v0, s[40:43], s60 offen offset:1024 sc1 lds\n"
    "ds_write_b128 v21, v[192:195] offset:0\n"
    "ds_write_b128 v21, v[196:199] offset:1024\n"
    "buffer_load_dwordx4 v0, s[40:43], s60 offen offset:2048 sc1 lds\n"
    "ds_write_b128 v21, v[200:203] offset:16384\n"
    "ds_write_b128 v21, v[204:207] offset:17408\n"
    "buffer_load_dwordx4 v0, s[40:43], s60 offen offset:3072 sc1 lds\n"
    "s_addk_i32 s60, 0x1000\n"
    "s_addk_i32 m0, 0x1000\n"
    "s_nop 0\n"
    "buffer_load_dwordx4 v0, s[40:43], s60 offen offset:0 sc1 lds\n"
    "OWN_DONE_%=:\n"
    "s_add_u32 s60, s71, 0\n"
    "s_add_u32 m0, s70, 0x14000\n"
    "v_min3_f32 v125, v240, v244, v248\n"
    "v_add_f32 v126, v240, v244\n"
    "v_add_f32 v127, v248, v252\n"
    "v_min_f32 v125, v125, v252\n"
    "buffer_load_dwordx4 v0, s[40:43], s60 offen offset:0 sc1 lds\n"
    "v_add_f32 v124, v126, v127\n"
    "s_mov_b32 s59, 0x6f800000\n"
    "v_cmp_nle_f32 vcc, v124, s59\n"
    "s_mov_b32 s59, 0x17800000\n"
    "buffer_load_dwordx4 v0, s[40:43], s60 offen offset:1024 sc1 lds\n"
    "s_add_u32 s60, s60, s88\n"
    "s_add_u32 m0, m0, s81\n"
    "v_cmp_nge_f32 s[38:39], v125, s59\n"
    "s_nop 1\n"
    "s_or_b64 vcc, vcc, s[38:39]\n"
    "s_or_b32 s59, vcc_lo, vcc_hi\n"
    "buffer_load_dwordx4 v0, s[40:43], s60 offen offset:0 sc1 lds\n"
    "s_cmp_eq_u32 s90, 0\n"
    "s_cselect_b32 s59, s59, 0\n"
    "v_mov_b32 v124, s59\n"
    "s_lshl2_add_u32 s59, s58, 0x12000\n"
    "buffer_load_dwordx4 v0, s[40:43], s60 offen offset:1024 sc1 lds\n"
    "v_mov_b32 v125, s59\n"
    "v_mov_b32 v126, 0x12000\n"
    "ds_write_b32 v125, v124\n"
    "s_waitcnt vmcnt(21)\n"
    "s_waitcnt lgkmcnt(0)\n"
    "s_barrier\n"
    "ds_read_b128 v[120:123], v126\n"
    "ds_read_b128 a[144:147], v22 offset:0\n"
    "ds_read_b128 a[180:183], v22 offset:10240\n"
    "ds_read_b128 a[128:131], v23 offset:0\n"
    "ds_read_b128 a[164:167], v23 offset:1024\n"
    "ds_read_b128 a[132:135], v23 offset:2048\n"
    "ds_read_b128 a[168:171], v23 offset:3072\n"
    "ds_read_b128 a[136:139], v23 offset:4096\n"
    "ds_read_b128 a[172:175], v23 offset:5120\n"
    "ds_read_b128 a[140:143], v23 offset:6144\n"
    "ds_read_b128 a[176:179], v23 offset:7168\n"
    "ds_read_b128 a[148:151], v22 offset:1024\n"
    "ds_read_b128 a[184:187], v22 offset:11264\n"
    "v_and_b32 v10, 31, v12\n"
    "v_lshlrev_b32 v10, 2, v10\n"
    "s_waitcnt lgkmcnt(12)\n"
    "v_or_b32 v120, v120, v121\n"
    "v_or_b32 v122, v122, v123\n"
    "v_or_b32 v120, v120, v122\n"
    "v_cmp_ne_u32 vcc, 0, v120\n"
    "s_waitcnt lgkmcnt(3)\n"
    "s_nop 0\n"
    "s_cbranch_vccnz MAXPASS_%=\n"
    "s_lshl_b32 s62, s57, 5\n"
    "s_add_u32 s62, s62, s56\n"
    "s_lshl2_add_u32 s62, s62, s58\n"
    "s_lshl2_add_u32 s62, s62, 0x4000\n"
    "v_mfma_f32_16x16x32_bf16 v[96:99], a[144:147], a[128:131], 0\n"
    "s_add_u32 s60, s68, 0xfff88000\n"
    "ds_read_b128 a[152:155], v22 offset:2048\n"
    "v_mfma_f32_16x16x32_bf16 v[96:99], a[180:183], a[164:167], v[96:99]\n"
    "s_add_u32 m0, s82, 0x21000\n"
    "ds_read_b128 a[188:191], v22 offset:12288\n"
    "v_mfma_f32_16x16x32_bf16 v[116:119], a[144:147], a[132:135], 0\n"
    "buffer_load_dwordx4 v0, s[40:43], s60 offen offset:0 sc1 lds\n"
    "v_mfma_f32_16x16x32_bf16 v[116:119], a[180:183], a[168:171], v[116:119]\n"
    "buffer_load_dwordx4 v0, s[40:43], s60 offen offset:1024 sc1 lds\n"
    "v_mfma_f32_16x16x32_bf16 v[136:139], a[144:147], a[136:139], 0\n"
    "ds_read_b128 a[156:159], v22 offset:3072\n"
    "v_mfma_f32_16x16x32_bf16 v[136:139], a[180:183], a[172:175], v[136:139]\n"
    "ds_read_b128 a[192:195], v22 offset:13312\n"
    "v_mfma_f32_16x16x32_bf16 v[156:159], a[144:147], a[140:143], 0\n"
    "s_waitcnt lgkmcnt(4)\n"
    "v_mfma_f32_16x16x32_bf16 v[156:159], a[180:183], a[176:179], v[156:159]\n"
    "buffer_load_dwordx4 v0, s[40:43], s60 offen offset:2048 sc1 lds\n"
    "v_mfma_f32_16x16x32_bf16 v[100:103], a[148:151], a[128:131], 0\n"
    "ds_read_b128 a[160:163], v22 offset:4096\n"
    "v_mfma_f32_16x16x32_bf16 v[100:103], a[184:187], a[164:167], v[100:103]\n"
    "ds_read_b128 a[196:199], v22 offset:14336\n"
    "v_mfma_f32_16x16x32_bf16 v[120:123], a[148:151], a[132:135], 0\n"
    "buffer_load_dwordx4 v0, s[40:43], s60 offen offset:3072 sc1 lds\n"
    "v_mfma_f32_16x16x32_bf16 v[120:123], a[184:187], a[168:171], v[120:123]\n"
    "s_addk_i32 s60, 0x1000\n"
    "s_addk_i32 m0, 0x1000\n"
    "v_mfma_f32_16x16x32_bf16 v[140:143], a[148:151], a[136:139], 0\n"
    "buffer_load_dwordx4 v0, s[40:43], s60 offen offset:0 sc1 lds\n"
    "v_mfma_f32_16x16x32_bf16 v[140:143], a[184:187], a[172:175], v[140:143]\n"
    "s_add_u32 s60, s71, 0x800\n"
    "s_add_u32 m0, s70, 0x18000\n"
    "v_mfma_f32_16x16x32_bf16 v[160:163], a[148:151], a[140:143], 0\n"
    "buffer_load_dwordx4 v0, s[40:43], s60 offen offset:0 sc1 lds\n"
    "v_mfma_f32_16x16x32_bf16 v[160:163], a[184:187], a[176:179], v[160:163]\n"
    "s_waitcnt lgkmcnt(4)\n"
    "v_mfma_f32_16x16x32_bf16 v[104:107], a[152:155], a[128:131], 0\n"
    "buffer_load_dwordx4 v0, s[40:43], s60 offen offset:1024 sc1 lds\n"
    "v_mfma_f32_16x16x32_bf16 v[104:107], a[188:191], a[164:167], v[104:107]\n"
    "s_waitcnt lgkmcnt(0)\n"
    "v_mfma_f32_16x16x32_bf16 v[124:127], a[152:155], a[132:135], 0\n"
    "s_waitcnt vmcnt(20)\n"
    "v_mfma_f32_16x16x32_bf16 v[124:127], a[188:191], a[168:171], v[124:127]\n"
    "s_barrier\n"
    "v_mfma_f32_16x16x32_bf16 v[144:147], a[152:155], a[136:139], 0\n"
    "s_add_u32 s60, s60, s88\n"
    "ds_read_b128 a[16:19], v22 offset:20480\n"
    "s_add_u32 m0, m0, s81\n"
    "v_mfma_f32_16x16x32_bf16 v[144:147], a[188:191], a[172:175], v[144:147]\n"
    "buffer_load_dwordx4 v0, s[40:43], s60 offen offset:0 sc1 lds\n"
    "v_mfma_f32_16x16x32_bf16 v[164:167], a[152:155], a[140:143], 0\n"
    "ds_read_b128 a[52:55], v22 offset:30720\n"
    "v_mfma_f32_16x16x32_bf16 v[164:167], a[188:191], a[176:179], v[164:167]\n"
    "ds_read_b128 a[0:3], v23 offset:16384\n"
    "v_mfma_f32_16x16x32_bf16 v[108:111], a[156:159], a[128:131], 0\n"
    "ds_read_b128 a[36:39], v23 offset:17408\n"
    "v_mfma_f32_16x16x32_bf16 v[108:111], a[192:195], a[164:167], v[108:111]\n"
    "buffer_load_dwordx4 v0, s[40:43], s60 offen offset:1024 sc1 lds\n"
    "v_mfma_f32_16x16x32_bf16 v[128:131], a[156:159], a[132:135], 0\n"
    "s_add_u32 s60, s68, 0xfff9c000\n"
    "ds_read_b128 a[4:7], v23 offset:18432\n"
    "v_mfma_f32_16x16x32_bf16 v[128:131], a[192:195], a[168:171], v[128:131]\n"
    "s_add_u32 m0, s82, 0x8000\n"
    "ds_read_b128 a[40:43], v23 offset:19456\n"
    "v_mfma_f32_16x16x32_bf16 v[148:151], a[156:159], a[136:139], 0\n"
    "ds_read_b128 a[8:11], v23 offset:20480\n"
    "v_mfma_f32_16x16x32_bf16 v[148:151], a[192:195], a[172:175], v[148:151]\n"
    "buffer_load_dwordx4 v0, s[40:43], s60 offen offset:0 sc1 lds\n"
    "v_mfma_f32_16x16x32_bf16 v[168:171], a[156:159], a[140:143], 0\n"
    "ds_read_b128 a[44:47], v23 offset:21504\n"
    "v_mfma_f32_16x16x32_bf16 v[168:171], a[192:195], a[176:179], v[168:171]\n"
    "ds_read_b128 a[12:15], v23 offset:22528\n"
    "v_mfma_f32_16x16x32_bf16 v[112:115], a[160:163], a[128:131], 0\n"
    "ds_read_b128 a[48:51], v23 offset:23552\n"
    "v_mfma_f32_16x16x32_bf16 v[112:115], a[196:199], a[164:167], v[112:115]\n"
    "buffer_load_dwordx4 v0, s[40:43], s60 offen offset:1024 sc1 lds\n"
    "v_mfma_f32_16x16x32_bf16 v[132:135], a[160:163], a[132:135], 0\n"
    "ds_read_b128 a[20:23], v22 offset:21504\n"
    "v_mfma_f32_16x16x32_bf16 v[132:135], a[196:199], a[168:171], v[132:135]\n"
    "ds_read_b128 a[56:59], v22 offset:31744\n"
    "v_mfma_f32_16x16x32_bf16 v[152:155], a[160:163], a[136:139], 0\n"
    "buffer_load_dwordx4 v0, s[40:43], s60 offen offset:2048 sc1 lds\n"
    "v_mfma_f32_16x16x32_bf16 v[152:155], a[196:199], a[172:175], v[152:155]\n"
    "buffer_load_dwordx4 v0, s[40:43], s60 offen offset:3072 sc1 lds\n"
    "s_addk_i32 s60, 0x1000\n"
    "s_addk_i32 m0, 0x1000\n"
    "v_mfma_f32_16x16x32_bf16 v[172:175], a[160:163], a[140:143], 0\n"
    "buffer_load_dwordx4 v0, s[40:43], s60 offen offset:0 sc1 lds\n"
    "v_mfma_f32_16x16x32_bf16 v[172:175], a[196:199], a[176:179], v[172:175]\n"
    "s_waitcnt lgkmcnt(3)\n"
    "v_mfma_f32_16x16x32_bf16 v[96:99], a[16:19], a[0:3], v[96:99]\n"
    "s_add_u32 s60, s71, 0x1000\n"
    "ds_read_b128 a[24:27], v22 offset:22528\n"
    "v_mfma_f32_16x16x32_bf16 v[96:99], a[52:55], a[36:39], v[96:99]\n"
    "s_add_u32 m0, s70, 0\n"
    "ds_read_b128 a[60:63], v22 offset:32768\n"
    "v_mfma_f32_16x16x32_bf16 v[116:119], a[16:19], a[4:7], v[116:119]\n"
    "v_mfma_f32_16x16x32_bf16 v[116:119], a[52:55], a[40:43], v[116:119]\n"
    "buffer_load_dwordx4 v0, s[40:43], s60 offen offset:0 sc1 lds\n"
    "v_mfma_f32_16x16x32_bf16 v[136:139], a[16:19], a[8:11], v[136:139]\n"
    "ds_read_b128 a[28:31], v22 offset:23552\n"
    "v_mfma_f32_16x16x32_bf16 v[136:139], a[52:55], a[44:47], v[136:139]\n"
    "ds_read_b128 a[64:67], v22 offset:33792\n"
    "v_mfma_f32_16x16x32_bf16 v[156:159], a[16:19], a[12:15], v[156:159]\n"
    "s_waitcnt lgkmcnt(4)\n"
    "v_mfma_f32_16x16x32_bf16 v[156:159], a[52:55], a[48:51], v[156:159]\n"
    "buffer_load_dwordx4 v0, s[40:43], s60 offen offset:1024 sc1 lds\n"
    "v_mfma_f32_16x16x32_bf16 v[100:103], a[20:23], a[0:3], v[100:103]\n"
    "s_add_u32 s60, s60, s88\n"
    "ds_read_b128 a[32:35], v22 offset:24576\n"
    "v_mfma_f32_16x16x32_bf16 v[100:103], a[56:59], a[36:39], v[100:103]\n"
    "s_add_u32 m0, m0, s81\n"
    "ds_read_b128 a[68:71], v22 offset:34816\n"
    "v_mfma_f32_16x16x32_bf16 v[120:123], a[20:23], a[4:7], v[120:123]\n"
    "v_mfma_f32_16x16x32_bf16 v[120:123], a[56:59], a[40:43], v[120:123]\n"
    "v_mfma_f32_16x16x32_bf16 v[140:143], a[20:23], a[8:11], v[140:143]\n"
    "buffer_load_dwordx4 v0, s[40:43], s60 offen offset:0 sc1 lds\n"
    "v_mfma_f32_16x16x32_bf16 v[140:143], a[56:59], a[44:47], v[140:143]\n"
    "v_mfma_f32_16x16x32_bf16 v[160:163], a[20:23], a[12:15], v[160:163]\n"
    "v_mfma_f32_16x16x32_bf16 v[160:163], a[56:59], a[48:51], v[160:163]\n"
    "s_waitcnt lgkmcnt(4)\n"
    "v_mfma_f32_16x16x32_bf16 v[104:107], a[24:27], a[0:3], v[104:107]\n"
    "buffer_load_dwordx4 v0, s[40:43], s60 offen offset:1024 sc1 lds\n"
    "v_mfma_f32_16x16x32_bf16 v[104:107], a[60:63], a[36:39], v[104:107]\n"
    "s_waitcnt lgkmcnt(0)\n"
    "v_mfma_f32_16x16x32_bf16 v[124:127], a[24:27], a[4:7], v[124:127]\n"
    "s_waitcnt vmcnt(18)\n"
    "v_xor_b32 v23, 0x14000, v23\n"
    "v_xor_b32 v22, 0x14000, v22\n"
    "v_mfma_f32_16x16x32_bf16 v[124:127], a[60:63], a[40:43], v[124:127]\n"
    "s_barrier\n"
    "v_mfma_f32_16x16x32_bf16 v[144:147], a[24:27], a[8:11], v[144:147]\n"
    "s_add_u32 s60, s68, 0xfffb0000\n"
    "ds_read_b128 a[144:147], v22 offset:0\n"
    "s_add_u32 m0, s82, 0xd000\n"
    "v_mfma_f32_16x16x32_bf16 v[144:147], a[60:63], a[44:47], v[144:147]\n"
    "buffer_load_dwordx4 v0, s[40:43], s60 offen offset:0 sc1 lds\n"
    "v_mfma_f32_16x16x32_bf16 v[164:167], a[24:27], a[12:15], v[164:167]\n"
    "ds_read_b128 a[180:183], v22 offset:10240\n"
    "v_mfma_f32_16x16x32_bf16 v[164:167], a[60:63], a[48:51], v[164:167]\n"
    "ds_read_b128 a[128:131], v23 offset:0\n"
    "v_mfma_f32_16x16x32_bf16 v[108:111], a[28:31], a[0:3], v[108:111]\n"
    "ds_read_b128 a[164:167], v23 offset:1024\n"
    "v_mfma_f32_16x16x32_bf16 v[108:111], a[64:67], a[36:39], v[108:111]\n"
    "buffer_load_dwordx4 v0, s[40:43], s60 offen offset:1024 sc1 lds\n"
    "v_mfma_f32_16x16x32_bf16 v[128:131], a[28:31], a[4:7], v[128:131]\n"
    "ds_read_b128 a[132:135], v23 offset:2048\n"
    "v_mfma_f32_16x16x32_bf16 v[128:131], a[64:67], a[40:43], v[128:131]\n"
    "ds_read_b128 a[168:171], v23 offset:3072\n"
    "v_mfma_f32_16x16x32_bf16 v[148:151], a[28:31], a[8:11], v[148:151]\n"
    "ds_read_b128 a[136:139], v23 offset:4096\n"
    "v_mfma_f32_16x16x32_bf16 v[148:151], a[64:67], a[44:47], v[148:151]\n"
    "buffer_load_dwordx4 v0, s[40:43], s60 offen offset:2048 sc1 lds\n"
    "v_mfma_f32_16x16x32_bf16 v[168:171], a[28:31], a[12:15], v[168:171]\n"
    "ds_read_b128 a[172:175], v23 offset:5120\n"
    "v_mfma_f32_16x16x32_bf16 v[168:171], a[64:67], a[48:51], v[168:171]\n"
    "ds_read_b128 a[140:143], v23 offset:6144\n"
    "v_mfma_f32_16x16x32_bf16 v[112:115], a[32:35], a[0:3], v[112:115]\n"
    "ds_read_b128 a[176:179], v23 offset:7168\n"
    "v_mfma_f32_16x16x32_bf16 v[112:115], a[68:71], a[36:39], v[112:115]\n"
    "buffer_store_dword v13, off, s[40:43], s62 offset:0 sc1\n"
    "v_mfma_f32_16x16x32_bf16 v[132:135], a[32:35], a[4:7], v[132:135]\n"
    "ds_read_b128 a[148:151], v22 offset:1024\n"
    "v_mfma_f32_16x16x32_bf16 v[132:135], a[68:71], a[40:43], v[132:135]\n"
    "ds_read_b128 a[184:187], v22 offset:11264\n"
    "v_mfma_f32_16x16x32_bf16 v[152:155], a[32:35], a[8:11], v[152:155]\n"
    "buffer_load_dwordx4 v0, s[40:43], s60 offen offset:3072 sc1 lds\n"
    "s_addk_i32 s60, 0x1000\n"
    "s_addk_i32 m0, 0x1000\n"
    "v_mfma_f32_16x16x32_bf16 v[152:155], a[68:71], a[44:47], v[152:155]\n"
    "buffer_load_dwordx4 v0, s[40:43], s60 offen offset:0 sc1 lds\n"
    "v_mfma_f32_16x16x32_bf16 v[172:175], a[32:35], a[12:15], v[172:175]\n"
    "s_add_u32 s60, s71, 0x1800\n"
    "v_mfma_f32_16x16x32_bf16 v[172:175], a[68:71], a[48:51], v[172:175]\n"
    "s_waitcnt lgkmcnt(3)\n"
    "v_mfma_f32_16x16x32_bf16 v[176:179], a[144:147], a[128:131], 0\n"
    "s_add_u32 m0, s70, 0x4000\n"
    "ds_read_b128 a[152:155], v22 offset:2048\n"
    "v_mfma_f32_16x16x32_bf16 v[176:179], a[180:183], a[164:167], v[176:179]\n"
    "ds_read_b128 a[188:191], v22 offset:12288\n"
    "v_mfma_f32_16x16x32_bf16 v[196:199], a[144:147], a[132:135], 0\n"
    "v_mfma_f32_16x16x32_bf16 v[196:199], a[180:183], a[168:171], v[196:199]\n"
    "buffer_load_dwordx4 v0, s[40:43], s60 offen offset:0 sc1 lds\n"
    "v_mfma_f32_16x16x32_bf16 v[216:219], a[144:147], a[136:139], 0\n"
    "ds_read_b128 a[156:159], v22 offset:3072\n"
    "v_mfma_f32_16x16x32_bf16 v[216:219], a[180:183], a[172:175], v[216:219]\n"
    "ds_read_b128 a[192:195], v22 offset:13312\n"
    "v_mfma_f32_16x16x32_bf16 v[236:239], a[144:147], a[140:143], 0\n"
    "s_waitcnt lgkmcnt(4)\n"
    "v_mfma_f32_16x16x32_bf16 v[236:239], a[180:183], a[176:179], v[236:239]\n"
    "buffer_load_dwordx4 v0, s[40:43], s60 offen offset:1024 sc1 lds\n"
    "v_mfma_f32_16x16x32_bf16 v[180:183], a[148:151], a[128:131], 0\n"
    "s_add_u32 s60, s60, s88\n"
    "ds_read_b128 a[160:163], v22 offset:4096\n"
    "v_mfma_f32_16x16x32_bf16 v[180:183], a[184:187], a[164:167], v[180:183]\n"
    "s_add_u32 m0, m0, s81\n"
    "ds_read_b128 a[196:199], v22 offset:14336\n"
    "v_mfma_f32_16x16x32_bf16 v[200:203], a[148:151], a[132:135], 0\n"
    "v_mfma_f32_16x16x32_bf16 v[200:203], a[184:187], a[168:171], v[200:203]\n"
    "v_mfma_f32_16x16x32_bf16 v[220:223], a[148:151], a[136:139], 0\n"
    "buffer_load_dwordx4 v0, s[40:43], s60 offen offset:0 sc1 lds\n"
    "v_mfma_f32_16x16x32_bf16 v[220:223], a[184:187], a[172:175], v[220:223]\n"
    "v_mfma_f32_16x16x32_bf16 v[240:243], a[148:151], a[140:143], 0\n"
    "v_mfma_f32_16x16x32_bf16 v[240:243], a[184:187], a[176:179], v[240:243]\n"
    "s_waitcnt lgkmcnt(4)\n"
    "v_mfma_f32_16x16x32_bf16 v[184:187], a[152:155], a[128:131], 0\n"
    "buffer_load_dwordx4 v0, s[40:43], s60 offen offset:1024 sc1 lds\n"
    "v_mfma_f32_16x16x32_bf16 v[184:187], a[188:191], a[164:167], v[184:187]\n"
    "s_waitcnt lgkmcnt(0)\n"
    "v_mfma_f32_16x16x32_bf16 v[204:207], a[152:155], a[132:135], 0\n"
    "s_waitcnt vmcnt(19)\n"
    "v_mfma_f32_16x16x32_bf16 v[204:207], a[188:191], a[168:171], v[204:207]\n"
    "s_barrier\n"
    "v_mfma_f32_16x16x32_bf16 v[224:227], a[152:155], a[136:139], 0\n"
    "s_add_u32 s60, s68, 0xfffc4000\n"
    "ds_read_b128 a[16:19], v22 offset:20480\n"
    "s_add_u32 m0, s82, 0x1c000\n"
    "v_mfma_f32_16x16x32_bf16 v[224:227], a[188:191], a[172:175], v[224:227]\n"
    "buffer_load_dwordx4 v0, s[40:43], s60 offen offset:0 sc1 lds\n"
    "v_mfma_f32_16x16x32_bf16 v[244:247], a[152:155], a[140:143], 0\n"
    "ds_read_b128 a[52:55], v22 offset:30720\n"
    "v_mfma_f32_16x16x32_bf16 v[244:247], a[188:191], a[176:179], v[244:247]\n"
    "ds_read_b128 a[0:3], v23 offset:16384\n"
    "v_mfma_f32_16x16x32_bf16 v[188:191], a[156:159], a[128:131], 0\n"
    "ds_read_b128 a[36:39], v23 offset:17408\n"
    "v_mfma_f32_16x16x32_bf16 v[188:191], a[192:195], a[164:167], v[188:191]\n"
    "buffer_load_dwordx4 v0, s[40:43], s60 offen offset:1024 sc1 lds\n"
    "v_mfma_f32_16x16x32_bf16 v[208:211], a[156:159], a[132:135], 0\n"
    "ds_read_b128 a[4:7], v23 offset:18432\n"
    "v_mfma_f32_16x16x32_bf16 v[208:211], a[192:195], a[168:171], v[208:211]\n"
    "ds_read_b128 a[40:43], v23 offset:19456\n"
    "v_mfma_f32_16x16x32_bf16 v[228:231], a[156:159], a[136:139], 0\n"
    "ds_read_b128 a[8:11], v23 offset:20480\n"
    "v_mfma_f32_16x16x32_bf16 v[228:231], a[192:195], a[172:175], v[228:231]\n"
    "buffer_load_dwordx4 v0, s[40:43], s60 offen offset:2048 sc1 lds\n"
    "v_mfma_f32_16x16x32_bf16 v[248:251], a[156:159], a[140:143], 0\n"
    "ds_read_b128 a[44:47], v23 offset:21504\n"
    "v_mfma_f32_16x16x32_bf16 v[248:251], a[192:195], a[176:179], v[248:251]\n"
    "ds_read_b128 a[12:15], v23 offset:22528\n"
    "v_mfma_f32_16x16x32_bf16 v[192:195], a[160:163], a[128:131], 0\n"
    "ds_read_b128 a[48:51], v23 offset:23552\n"
    "v_mfma_f32_16x16x32_bf16 v[192:195], a[196:199], a[164:167], v[192:195]\n"
    "buffer_load_dwordx4 v0, s[40:43], s60 offen offset:3072 sc1 lds\n"
    "v_mfma_f32_16x16x32_bf16 v[212:215], a[160:163], a[132:135], 0\n"
    "s_addk_i32 s60, 0x1000\n"
    "ds_read_b128 a[20:23], v22 offset:21504\n"
    "v_mfma_f32_16x16x32_bf16 v[212:215], a[196:199], a[168:171], v[212:215]\n"
    "s_addk_i32 m0, 0x1000\n"
    "ds_read_b128 a[56:59], v22 offset:31744\n"
    "v_mfma_f32_16x16x32_bf16 v[232:235], a[160:163], a[136:139], 0\n"
    "v_mfma_f32_16x16x32_bf16 v[232:235], a[196:199], a[172:175], v[232:235]\n"
    "buffer_load_dwordx4 v0, s[40:43], s60 offen offset:0 sc1 lds\n"
    "v_mfma_f32_16x16x32_bf16 v[252:255], a[160:163], a[140:143], 0\n"
    "s_add_u32 s60, s71, 0x2000\n"
    "v_mfma_f32_16x16x32_bf16 v[252:255], a[196:199], a[176:179], v[252:255]\n"
    "s_waitcnt lgkmcnt(3)\n"
    "v_mfma_f32_16x16x32_bf16 v[176:179], a[16:19], a[0:3], v[176:179]\n"
    "s_add_u32 m0, s70, 0x14000\n"
    "ds_read_b128 a[24:27], v22 offset:22528\n"
    "v_mfma_f32_16x16x32_bf16 v[176:179], a[52:55], a[36:39], v[176:179]\n"
    "ds_read_b128 a[60:63], v22 offset:32768\n"
    "v_mfma_f32_16x16x32_bf16 v[196:199], a[16:19], a[4:7], v[196:199]\n"
    "v_mfma_f32_16x16x32_bf16 v[196:199], a[52:55], a[40:43], v[196:199]\n"
    "buffer_load_dwordx4 v0, s[40:43], s60 offen offset:0 sc1 lds\n"
    "v_mfma_f32_16x16x32_bf16 v[216:219], a[16:19], a[8:11], v[216:219]\n"
    "ds_read_b128 a[28:31], v22 offset:23552\n"
    "v_mfma_f32_16x16x32_bf16 v[216:219], a[52:55], a[44:47], v[216:219]\n"
    "ds_read_b128 a[64:67], v22 offset:33792\n"
    "v_mfma_f32_16x16x32_bf16 v[236:239], a[16:19], a[12:15], v[236:239]\n"
    "s_waitcnt lgkmcnt(4)\n"
    "v_mfma_f32_16x16x32_bf16 v[236:239], a[52:55], a[48:51], v[236:239]\n"
    "buffer_load_dwordx4 v0, s[40:43], s60 offen offset:1024 sc1 lds\n"
    "v_mfma_f32_16x16x32_bf16 v[180:183], a[20:23], a[0:3], v[180:183]\n"
    "s_add_u32 s60, s60, s88\n"
    "ds_read_b128 a[32:35], v22 offset:24576\n"
    "v_mfma_f32_16x16x32_bf16 v[180:183], a[56:59], a[36:39], v[180:183]\n"
    "s_add_u32 m0, m0, s81\n"
    "ds_read_b128 a[68:71], v22 offset:34816\n"
    "v_mfma_f32_16x16x32_bf16 v[200:203], a[20:23], a[4:7], v[200:203]\n"
    "v_mfma_f32_16x16x32_bf16 v[200:203], a[56:59], a[40:43], v[200:203]\n"
    "v_mfma_f32_16x16x32_bf16 v[220:223], a[20:23], a[8:11], v[220:223]\n"
    "buffer_load_dwordx4 v0, s[40:43], s60 offen offset:0 sc1 lds\n"
    "v_mfma_f32_16x16x32_bf16 v[220:223], a[56:59], a[44:47], v[220:223]\n"
    "v_mfma_f32_16x16x32_bf16 v[240:243], a[20:23], a[12:15], v[240:243]\n"
    "v_mfma_f32_16x16x32_bf16 v[240:243], a[56:59], a[48:51], v[240:243]\n"
    "s_waitcnt lgkmcnt(4)\n"
    "v_mfma_f32_16x16x32_bf16 v[184:187], a[24:27], a[0:3], v[184:187]\n"
    "buffer_load_dwordx4 v0, s[40:43], s60 offen offset:1024 sc1 lds\n"
    "v_mfma_f32_16x16x32_bf16 v[184:187], a[60:63], a[36:39], v[184:187]\n"
    "s_waitcnt lgkmcnt(0)\n"
    "v_mfma_f32_16x16x32_bf16 v[204:207], a[24:27], a[4:7], v[204:207]\n"
    "s_waitcnt vmcnt(19)\n"
    "v_xor_b32 v23, 0x14000, v23\n"
    "v_xor_b32 v22, 0x14000, v22\n"
    "v_mfma_f32_16x16x32_bf16 v[204:207], a[60:63], a[40:43], v[204:207]\n"
    "s_barrier\n"
    "v_mfma_f32_16x16x32_bf16 v[224:227], a[24:27], a[8:11], v[224:227]\n"
    "s_add_u32 s60, s68, 0xfffd8000\n"
    "ds_read_b128 a[144:147], v22 offset:0\n"
    "s_add_u32 m0, s82, 0x21000\n"
    "v_mfma_f32_16x16x32_bf16 v[224:227], a[60:63], a[44:47], v[224:227]\n"
    "buffer_load_dwordx4 v0, s[40:43], s60 offen offset:0 sc1 lds\n"
    "v_mfma_f32_16x16x32_bf16 v[244:247], a[24:27], a[12:15], v[244:247]\n"
    "ds_read_b128 a[180:183], v22 offset:10240\n"
    "v_mfma_f32_16x16x32_bf16 v[244:247], a[60:63], a[48:51], v[244:247]\n"
    "ds_read_b128 a[128:131], v23 offset:0\n"
    "v_mfma_f32_16x16x32_bf16 v[188:191], a[28:31], a[0:3], v[188:191]\n"
    "ds_read_b128 a[164:167], v23 offset:1024\n"
    "v_mfma_f32_16x16x32_bf16 v[188:191], a[64:67], a[36:39], v[188:191]\n"
    "buffer_load_dwordx4 v0, s[40:43], s60 offen offset:1024 sc1 lds\n"
    "v_mfma_f32_16x16x32_bf16 v[208:211], a[28:31], a[4:7], v[208:211]\n"
    "ds_read_b128 a[132:135], v23 offset:2048\n"
    "v_mfma_f32_16x16x32_bf16 v[208:211], a[64:67], a[40:43], v[208:211]\n"
    "ds_read_b128 a[168:171], v23 offset:3072\n"
    "v_mfma_f32_16x16x32_bf16 v[228:231], a[28:31], a[8:11], v[228:231]\n"
    "ds_read_b128 a[136:139], v23 offset:4096\n"
    "v_mfma_f32_16x16x32_bf16 v[228:231], a[64:67], a[44:47], v[228:231]\n"
    "buffer_load_dwordx4 v0, s[40:43], s60 offen offset:2048 sc1 lds\n"
    "v_mfma_f32_16x16x32_bf16 v[248:251], a[28:31], a[12:15], v[248:251]\n"
    "ds_read_b128 a[172:175], v23 offset:5120\n"
    "v_mfma_f32_16x16x32_bf16 v[248:251], a[64:67], a[48:51], v[248:251]\n"
    "ds_read_b128 a[140:143], v23 offset:6144\n"
    "v_mfma_f32_16x16x32_bf16 v[192:195], a[32:35], a[0:3], v[192:195]\n"
    "ds_read_b128 a[176:179], v23 offset:7168\n"
    "v_mfma_f32_16x16x32_bf16 v[192:195], a[68:71], a[36:39], v[192:195]\n"
    "buffer_load_dwordx4 v0, s[40:43], s60 offen offset:3072 sc1 lds\n"
    "v_mfma_f32_16x16x32_bf16 v[212:215], a[32:35], a[4:7], v[212:215]\n"
    "s_addk_i32 s60, 0x1000\n"
    "ds_read_b128 a[148:151], v22 offset:1024\n"
    "v_mfma_f32_16x16x32_bf16 v[212:215], a[68:71], a[40:43], v[212:215]\n"
    "s_addk_i32 m0, 0x1000\n"
    "ds_read_b128 a[184:187], v22 offset:11264\n"
    "v_mfma_f32_16x16x32_bf16 v[232:235], a[32:35], a[8:11], v[232:235]\n"
    "v_mfma_f32_16x16x32_bf16 v[232:235], a[68:71], a[44:47], v[232:235]\n"
    "buffer_load_dwordx4 v0, s[40:43], s60 offen offset:0 sc1 lds\n"
    "v_mfma_f32_16x16x32_bf16 v[252:255], a[32:35], a[12:15], v[252:255]\n"
    "s_add_u32 s60, s71, 0x2800\n"
    "v_mfma_f32_16x16x32_bf16 v[252:255], a[68:71], a[48:51], v[252:255]\n"
    "s_waitcnt lgkmcnt(3)\n"
    "v_mfma_f32_16x16x32_bf16 v[176:179], a[144:147], a[128:131], v[176:179]\n"
    "s_add_u32 m0, s70, 0x18000\n"
    "ds_read_b128 a[152:155], v22 offset:2048\n"
    "v_mfma_f32_16x16x32_bf16 v[176:179], a[180:183], a[164:167], v[176:179]\n"
    "ds_read_b128 a[188:191], v22 offset:12288\n"
    "v_mfma_f32_16x16x32_bf16 v[196:199], a[144:147], a[132:135], v[196:199]\n"
    "v_mfma_f32_16x16x32_bf16 v[196:199], a[180:183], a[168:171], v[196:199]\n"
    "buffer_load_dwordx4 v0, s[40:43], s60 offen offset:0 sc1 lds\n"
    "v_mfma_f32_16x16x32_bf16 v[216:219], a[144:147], a[136:139], v[216:219]\n"
    "ds_read_b128 a[156:159], v22 offset:3072\n"
    "v_mfma_f32_16x16x32_bf16 v[216:219], a[180:183], a[172:175], v[216:219]\n"
    "ds_read_b128 a[192:195], v22 offset:13312\n"
    "v_mfma_f32_16x16x32_bf16 v[236:239], a[144:147], a[140:143], v[236:239]\n"
    "s_waitcnt lgkmcnt(4)\n"
    "v_mfma_f32_16x16x32_bf16 v[236:239], a[180:183], a[176:179], v[236:239]\n"
    "buffer_load_dwordx4 v0, s[40:43], s60 offen offset:1024 sc1 lds\n"
    "v_mfma_f32_16x16x32_bf16 v[180:183], a[148:151], a[128:131], v[180:183]\n"
    "s_add_u32 s60, s60, s88\n"
    "ds_read_b128 a[160:163], v22 offset:4096\n"
    "v_mfma_f32_16x16x32_bf16 v[180:183], a[184:187], a[164:167], v[180:183]\n"
    "s_add_u32 m0, m0, s81\n"
    "ds_read_b128 a[196:199], v22 offset:14336\n"
    "v_mfma_f32_16x16x32_bf16 v[200:203], a[148:151], a[132:135], v[200:203]\n"
    "v_mfma_f32_16x16x32_bf16 v[200:203], a[184:187], a[168:171], v[200:203]\n"
    "v_mfma_f32_16x16x32_bf16 v[220:223], a[148:151], a[136:139], v[220:223]\n"
    "buffer_load_dwordx4 v0, s[40:43], s60 offen offset:0 sc1 lds\n"
    "v_mfma_f32_16x16x32_bf16 v[220:223], a[184:187], a[172:175], v[220:223]\n"
    "v_mfma_f32_16x16x32_bf16 v[240:243], a[148:151], a[140:143], v[240:243]\n"
    "v_mfma_f32_16x16x32_bf16 v[240:243], a[184:187], a[176:179], v[240:243]\n"
    "s_waitcnt lgkmcnt(4)\n"
    "v_mfma_f32_16x16x32_bf16 v[184:187], a[152:155], a[128:131], v[184:187]\n"
    "buffer_load_dwordx4 v0, s[40:43], s60 offen offset:1024 sc1 lds\n"
    "v_mfma_f32_16x16x32_bf16 v[184:187], a[188:191], a[164:167], v[184:187]\n"
    "s_waitcnt lgkmcnt(0)\n"
    "v_mfma_f32_16x16x32_bf16 v[204:207], a[152:155], a[132:135], v[204:207]\n"
    "s_waitcnt vmcnt(18)\n"
    "v_mfma_f32_16x16x32_bf16 v[204:207], a[188:191], a[168:171], v[204:207]\n"
    "s_barrier\n"
    "v_mfma_f32_16x16x32_bf16 v[224:227], a[152:155], a[136:139], v[224:227]\n"
    "s_add_u32 s60, s68, 0xfffec000\n"
    "ds_read_b128 a[16:19], v22 offset:20480\n"
    "s_add_u32 m0, s82, 0x8000\n"
    "v_mfma_f32_16x16x32_bf16 v[224:227], a[188:191], a[172:175], v[224:227]\n"
    "buffer_load_dwordx4 v0, s[40:43], s60 offen offset:0 sc1 lds\n"
    "v_mfma_f32_16x16x32_bf16 v[244:247], a[152:155], a[140:143], v[244:247]\n"
    "ds_read_b128 a[52:55], v22 offset:30720\n"
    "v_mfma_f32_16x16x32_bf16 v[244:247], a[188:191], a[176:179], v[244:247]\n"
    "ds_read_b128 a[0:3], v23 offset:16384\n"
    "v_mfma_f32_16x16x32_bf16 v[188:191], a[156:159], a[128:131], v[188:191]\n"
    "ds_read_b128 a[36:39], v23 offset:17408\n"
    "v_mfma_f32_16x16x32_bf16 v[188:191], a[192:195], a[164:167], v[188:191]\n"
    "buffer_load_dwordx4 v0, s[40:43], s60 offen offset:1024 sc1 lds\n"
    "v_mfma_f32_16x16x32_bf16 v[208:211], a[156:159], a[132:135], v[208:211]\n"
    "ds_read_b128 a[4:7], v23 offset:18432\n"
    "v_mfma_f32_16x16x32_bf16 v[208:211], a[192:195], a[168:171], v[208:211]\n"
    "ds_read_b128 a[40:43], v23 offset:19456\n"
    "v_mfma_f32_16x16x32_bf16 v[228:231], a[156:159], a[136:139], v[228:231]\n"
    "ds_read_b128 a[8:11], v23 offset:20480\n"
    "v_mfma_f32_16x16x32_bf16 v[228:231], a[192:195], a[172:175], v[228:231]\n"
    "buffer_load_dwordx4 v0, s[40:43], s60 offen offset:2048 sc1 lds\n"
    "v_mfma_f32_16x16x32_bf16 v[248:251], a[156:159], a[140:143], v[248:251]\n"
    "ds_read_b128 a[44:47], v23 offset:21504\n"
    "v_mfma_f32_16x16x32_bf16 v[248:251], a[192:195], a[176:179], v[248:251]\n"
    "ds_read_b128 a[12:15], v23 offset:22528\n"
    "v_mfma_f32_16x16x32_bf16 v[192:195], a[160:163], a[128:131], v[192:195]\n"
    "ds_read_b128 a[48:51], v23 offset:23552\n"
    "v_mfma_f32_16x16x32_bf16 v[192:195], a[196:199], a[164:167], v[192:195]\n"
    "buffer_load_dword v1, v10, s[40:43], s32 offen offset:0 sc1\n"
    "v_mfma_f32_16x16x32_bf16 v[212:215], a[160:163], a[132:135], v[212:215]\n"
    "ds_read_b128 a[20:23], v22 offset:21504\n"
    "v_mfma_f32_16x16x32_bf16 v[212:215], a[196:199], a[168:171], v[212:215]\n"
    "ds_read_b128 a[56:59], v22 offset:31744\n"
    "v_mfma_f32_16x16x32_bf16 v[232:235], a[160:163], a[136:139], v[232:235]\n"
    "buffer_load_dwordx4 v0, s[40:43], s60 offen offset:3072 sc1 lds\n"
    "s_addk_i32 s60, 0x1000\n"
    "s_addk_i32 m0, 0x1000\n"
    "v_mfma_f32_16x16x32_bf16 v[232:235], a[196:199], a[172:175], v[232:235]\n"
    "buffer_load_dwordx4 v0, s[40:43], s60 offen offset:0 sc1 lds\n"
    "v_mfma_f32_16x16x32_bf16 v[252:255], a[160:163], a[140:143], v[252:255]\n"
    "s_add_u32 s60, s72, 0x3000\n"
    "v_mfma_f32_16x16x32_bf16 v[252:255], a[196:199], a[176:179], v[252:255]\n"
    "s_waitcnt lgkmcnt(3)\n"
    "v_mfma_f32_16x16x32_bf16 v[176:179], a[16:19], a[0:3], v[176:179]\n"
    "s_add_u32 m0, s70, 0\n"
    "ds_read_b128 a[24:27], v22 offset:22528\n"
    "v_mfma_f32_16x16x32_bf16 v[176:179], a[52:55], a[36:39], v[176:179]\n"
    "ds_read_b128 a[60:63], v22 offset:32768\n"
    "v_mfma_f32_16x16x32_bf16 v[196:199], a[16:19], a[4:7], v[196:199]\n"
    "v_mfma_f32_16x16x32_bf16 v[196:199], a[52:55], a[40:43], v[196:199]\n"
    "buffer_load_dwordx4 v0, s[40:43], s60 offen offset:0 sc1 lds\n"
    "v_mfma_f32_16x16x32_bf16 v[216:219], a[16:19], a[8:11], v[216:219]\n"
    "ds_read_b128 a[28:31], v22 offset:23552\n"
    "v_mfma_f32_16x16x32_bf16 v[216:219], a[52:55], a[44:47], v[216:219]\n"
    "ds_read_b128 a[64:67], v22 offset:33792\n"
    "v_mfma_f32_16x16x32_bf16 v[236:239], a[16:19], a[12:15], v[236:239]\n"
    "s_waitcnt lgkmcnt(4)\n"
    "v_mfma_f32_16x16x32_bf16 v[236:239], a[52:55], a[48:51], v[236:239]\n"
    "buffer_load_dwordx4 v0, s[40:43], s60 offen offset:1024 sc1 lds\n"
    "v_mfma_f32_16x16x32_bf16 v[180:183], a[20:23], a[0:3], v[180:183]\n"
    "s_add_u32 s60, s60, s88\n"
    "ds_read_b128 a[32:35], v22 offset:24576\n"
    "v_mfma_f32_16x16x32_bf16 v[180:183], a[56:59], a[36:39], v[180:183]\n"
    "s_add_u32 m0, m0, s81\n"
    "ds_read_b128 a[68:71], v22 offset:34816\n"
    "v_mfma_f32_16x16x32_bf16 v[200:203], a[20:23], a[4:7], v[200:203]\n"
    "v_mfma_f32_16x16x32_bf16 v[200:203], a[56:59], a[40:43], v[200:203]\n"
    "v_mfma_f32_16x16x32_bf16 v[220:223], a[20:23], a[8:11], v[220:223]\n"
    "buffer_load_dwordx4 v0, s[40:43], s60 offen offset:0 sc1 lds\n"
    "v_mfma_f32_16x16x32_bf16 v[220:223], a[56:59], a[44:47], v[220:223]\n"
    "v_mfma_f32_16x16x32_bf16 v[240:243], a[20:23], a[12:15], v[240:243]\n"
    "v_mfma_f32_16x16x32_bf16 v[240:243], a[56:59], a[48:51], v[240:243]\n"
    "s_waitcnt lgkmcnt(4)\n"
    "v_mfma_f32_16x16x32_bf16 v[184:187], a[24:27], a[0:3], v[184:187]\n"
    "buffer_load_dwordx4 v0, s[40:43], s60 offen offset:1024 sc1 lds\n"
    "v_mfma_f32_16x16x32_bf16 v[184:187], a[60:63], a[36:39], v[184:187]\n"
    "s_waitcnt lgkmcnt(0)\n"
    "v_mfma_f32_16x16x32_bf16 v[204:207], a[24:27], a[4:7], v[204:207]\n"
    "s_waitcnt vmcnt(6)\n"
    "v_xor_b32 v23, 0x14000, v23\n"
    "v_xor_b32 v22, 0x14000, v22\n"
    "v_mfma_f32_16x16x32_bf16 v[204:207], a[60:63], a[40:43], v[204:207]\n"
    "s_barrier\n"
    "v_cmp_ne_u32 vcc, v1, v13\n"
    "s_nop 1\n"
    "s_cbranch_vccnz CF_SPIN_%=\n"
    "CF_OK_%=:\n"
    "v_mfma_f32_16x16x32_bf16 v[224:227], a[24:27], a[8:11], v[224:227]\n"
    "s_cmp_le_u32 s69, 0\n"
    "ds_read_b128 a[144:147], v22 offset:0\n"
    "s_cselect_b32 s59, 2, 0\n"
    "s_lshl_b32 s59, s59, 11\n"
    "s_mul_i32 s60, s59, 40\n"
    "s_add_u32 s60, s60, s68\n"
    "s_add_u32 m0, s82, 0xd000\n"
    "v_mfma_f32_16x16x32_bf16 v[224:227], a[60:63], a[44:47], v[224:227]\n"
    "v_min_u32 v10, 2, v12\n"
    "v_lshlrev_b32 v10, 2, v10\n"
    "buffer_load_dwordx4 v0, s[40:43], s60 offen offset:0 sc1 lds\n"
    "v_mfma_f32_16x16x32_bf16 v[244:247], a[24:27], a[12:15], v[244:247]\n"
    "ds_read_b128 a[180:183], v22 offset:10240\n"
    "v_mfma_f32_16x16x32_bf16 v[244:247], a[60:63], a[48:51], v[244:247]\n"
    "ds_read_b128 a[128:131], v23 offset:0\n"
    "v_mfma_f32_16x16x32_bf16 v[188:191], a[28:31], a[0:3], v[188:191]\n"
    "ds_read_b128 a[164:167], v23 offset:1024\n"
    "v_mfma_f32_16x16x32_bf16 v[188:191], a[64:67], a[36:39], v[188:191]\n"
    "buffer_load_dwordx4 v0, s[40:43], s60 offen offset:1024 sc1 lds\n"
    "v_mfma_f32_16x16x32_bf16 v[208:211], a[28:31], a[4:7], v[208:211]\n"
    "ds_read_b128 a[132:135], v23 offset:2048\n"
    "v_mfma_f32_16x16x32_bf16 v[208:211], a[64:67], a[40:43], v[208:211]\n"
    "ds_read_b128 a[168:171], v23 offset:3072\n"
    "v_mfma_f32_16x16x32_bf16 v[228:231], a[28:31], a[8:11], v[228:231]\n"
    "ds_read_b128 a[136:139], v23 offset:4096\n"
    "v_mfma_f32_16x16x32_bf16 v[228:231], a[64:67], a[44:47], v[228:231]\n"
    "buffer_load_dwordx4 v0, s[40:43], s60 offen offset:2048 sc1 lds\n"
    "v_mfma_f32_16x16x32_bf16 v[248:251], a[28:31], a[12:15], v[248:251]\n"
    "ds_read_b128 a[172:175], v23 offset:5120\n"
    "v_mfma_f32_16x16x32_bf16 v[248:251], a[64:67], a[48:51], v[248:251]\n"
    "ds_read_b128 a[140:143], v23 offset:6144\n"
    "v_mfma_f32_16x16x32_bf16 v[192:195], a[32:35], a[0:3], v[192:195]\n"
    "ds_read_b128 a[176:179], v23 offset:7168\n"
    "v_mfma_f32_16x16x32_bf16 v[192:195], a[68:71], a[36:39], v[192:195]\n"
    "buffer_load_dwordx4 v0, s[40:43], s60 offen offset:3072 sc1 lds\n"
    "v_mfma_f32_16x16x32_bf16 v[212:215], a[32:35], a[4:7], v[212:215]\n"
    "s_addk_i32 s60, 0x1000\n"
    "ds_read_b128 a[148:151], v22 offset:1024\n"
    "v_mfma_f32_16x16x32_bf16 v[212:215], a[68:71], a[40:43], v[212:215]\n"
    "s_addk_i32 m0, 0x1000\n"
    "ds_read_b128 a[184:187], v22 offset:11264\n"
    "v_mfma_f32_16x16x32_bf16 v[232:235], a[32:35], a[8:11], v[232:235]\n"
    "v_mfma_f32_16x16x32_bf16 v[232:235], a[68:71], a[44:47], v[232:235]\n"
    "buffer_load_dwordx4 v0, s[40:43], s60 offen offset:0 sc1 lds\n"
    "v_mfma_f32_16x16x32_bf16 v[252:255], a[32:35], a[12:15], v[252:255]\n"
    "s_add_u32 s60, s73, s59\n"
    "v_mfma_f32_16x16x32_bf16 v[252:255], a[68:71], a[48:51], v[252:255]\n"
    "s_waitcnt lgkmcnt(3)\n"
    "v_mfma_f32_16x16x32_bf16 v[176:179], a[144:147], a[128:131], v[176:179]\n"
    "s_add_u32 m0, s70, 0x4000\n"
    "ds_read_b128 a[152:155], v22 offset:2048\n"
    "v_mfma_f32_16x16x32_bf16 v[176:179], a[180:183], a[164:167], v[176:179]\n"
    "ds_read_b128 a[188:191], v22 offset:12288\n"
    "v_mfma_f32_16x16x32_bf16 v[196:199], a[144:147], a[132:135], v[196:199]\n"
    "v_mfma_f32_16x16x32_bf16 v[196:199], a[180:183], a[168:171], v[196:199]\n"
    "buffer_load_dwordx4 v0, s[40:43], s60 offen offset:0 sc1 lds\n"
    "v_mfma_f32_16x16x32_bf16 v[216:219], a[144:147], a[136:139], v[216:219]\n"
    "ds_read_b128 a[156:159], v22 offset:3072\n"
    "v_mfma_f32_16x16x32_bf16 v[216:219], a[180:183], a[172:175], v[216:219]\n"
    "ds_read_b128 a[192:195], v22 offset:13312\n"
    "v_mfma_f32_16x16x32_bf16 v[236:239], a[144:147], a[140:143], v[236:239]\n"
    "s_waitcnt lgkmcnt(4)\n"
    "v_mfma_f32_16x16x32_bf16 v[236:239], a[180:183], a[176:179], v[236:239]\n"
    "buffer_load_dwordx4 v0, s[40:43], s60 offen offset:1024 sc1 lds\n"
    "v_mfma_f32_16x16x32_bf16 v[180:183], a[148:151], a[128:131], v[180:183]\n"
    "s_add_u32 s60, s60, s74\n"
    "ds_read_b128 a[160:163], v22 offset:4096\n"
    "v_mfma_f32_16x16x32_bf16 v[180:183], a[184:187], a[164:167], v[180:183]\n"
    "s_add_u32 m0, m0, s81\n"
    "ds_read_b128 a[196:199], v22 offset:14336\n"
    "v_mfma_f32_16x16x32_bf16 v[200:203], a[148:151], a[132:135], v[200:203]\n"
    "v_mfma_f32_16x16x32_bf16 v[200:203], a[184:187], a[168:171], v[200:203]\n"
    "v_mfma_f32_16x16x32_bf16 v[220:223], a[148:151], a[136:139], v[220:223]\n"
    "buffer_load_dwordx4 v0, s[40:43], s60 offen offset:0 sc1 lds\n"
    "v_mfma_f32_16x16x32_bf16 v[220:223], a[184:187], a[172:175], v[220:223]\n"
    "v_mfma_f32_16x16x32_bf16 v[240:243], a[148:151], a[140:143], v[240:243]\n"
    "v_mfma_f32_16x16x32_bf16 v[240:243], a[184:187], a[176:179], v[240:243]\n"
    "s_waitcnt lgkmcnt(4)\n"
    "v_mfma_f32_16x16x32_bf16 v[184:187], a[152:155], a[128:131], v[184:187]\n"
    "buffer_load_dwordx4 v0, s[40:43], s60 offen offset:1024 sc1 lds\n"
    "v_mfma_f32_16x16x32_bf16 v[184:187], a[188:191], a[164:167], v[184:187]\n"
    "s_waitcnt lgkmcnt(0)\n"
    "v_mfma_f32_16x16x32_bf16 v[204:207], a[152:155], a[132:135], v[204:207]\n"
    "s_waitcnt vmcnt(19)\n"
    "v_mfma_f32_16x16x32_bf16 v[204:207], a[188:191], a[168:171], v[204:207]\n"
    "s_barrier\n"
    "v_mfma_f32_16x16x32_bf16 v[224:227], a[152:155], a[136:139], v[224:227]\n"
    "s_addk_i32 s59, 0x800\n"
    "ds_read_b128 a[16:19], v22 offset:20480\n"
    "s_mul_i32 s60, s59, 40\n"
    "s_add_u32 s60, s60, s68\n"
    "s_add_u32 m0, s82, 0x1c000\n"
    "v_mfma_f32_16x16x32_bf16 v[224:227], a[188:191], a[172:175], v[224:227]\n"
    "buffer_load_dwordx4 v0, s[40:43], s60 offen offset:0 sc1 lds\n"
    "v_mfma_f32_16x16x32_bf16 v[244:247], a[152:155], a[140:143], v[244:247]\n"
    "ds_read_b128 a[52:55], v22 offset:30720\n"
    "v_mfma_f32_16x16x32_bf16 v[244:247], a[188:191], a[176:179], v[244:247]\n"
    "ds_read_b128 a[0:3], v23 offset:16384\n"
    "v_mfma_f32_16x16x32_bf16 v[188:191], a[156:159], a[128:131], v[188:191]\n"
    "ds_read_b128 a[36:39], v23 offset:17408\n"
    "v_mfma_f32_16x16x32_bf16 v[188:191], a[192:195], a[164:167], v[188:191]\n"
    "buffer_load_dwordx4 v0, s[40:43], s60 offen offset:1024 sc1 lds\n"
    "v_mfma_f32_16x16x32_bf16 v[208:211], a[156:159], a[132:135], v[208:211]\n"
    "ds_read_b128 a[4:7], v23 offset:18432\n"
    "v_mfma_f32_16x16x32_bf16 v[208:211], a[192:195], a[168:171], v[208:211]\n"
    "ds_read_b128 a[40:43], v23 offset:19456\n"
    "v_mfma_f32_16x16x32_bf16 v[228:231], a[156:159], a[136:139], v[228:231]\n"
    "ds_read_b128 a[8:11], v23 offset:20480\n"
    "v_mfma_f32_16x16x32_bf16 v[228:231], a[192:195], a[172:175], v[228:231]\n"
    "buffer_load_dwordx4 v0, s[40:43], s60 offen offset:2048 sc1 lds\n"
    "v_mfma_f32_16x16x32_bf16 v[248:251], a[156:159], a[140:143], v[248:251]\n"
    "ds_read_b128 a[44:47], v23 offset:21504\n"
    "v_mfma_f32_16x16x32_bf16 v[248:251], a[192:195], a[176:179], v[248:251]\n"
    "ds_read_b128 a[12:15], v23 offset:22528\n"
    "v_mfma_f32_16x16x32_bf16 v[192:195], a[160:163], a[128:131], v[192:195]\n"
    "ds_read_b128 a[48:51], v23 offset:23552\n"
    "v_mfma_f32_16x16x32_bf16 v[192:195], a[196:199], a[164:167], v[192:195]\n"
    "buffer_load_dwordx4 v0, s[40:43], s60 offen offset:3072 sc1 lds\n"
    "v_mfma_f32_16x16x32_bf16 v[212:215], a[160:163], a[132:135], v[212:215]\n"
    "s_addk_i32 s60, 0x1000\n"
    "ds_read_b128 a[20:23], v22 offset:21504\n"
    "v_mfma_f32_16x16x32_bf16 v[212:215], a[196:199], a[168:171], v[212:215]\n"
    "s_addk_i32 m0, 0x1000\n"
    "ds_read_b128 a[56:59], v22 offset:31744\n"
    "v_mfma_f32_16x16x32_bf16 v[232:235], a[160:163], a[136:139], v[232:235]\n"
    "v_mfma_f32_16x16x32_bf16 v[232:235], a[196:199], a[172:175], v[232:235]\n"
    "buffer_load_dwordx4 v0, s[40:43], s60 offen offset:0 sc1 lds\n"
    "v_mfma_f32_16x16x32_bf16 v[252:255], a[160:163], a[140:143], v[252:255]\n"
    "s_add_u32 s60, s73, s59\n"
    "v_mfma_f32_16x16x32_bf16 v[252:255], a[196:199], a[176:179], v[252:255]\n"
    "s_waitcnt lgkmcnt(3)\n"
    "v_mfma_f32_16x16x32_bf16 v[176:179], a[16:19], a[0:3], v[176:179]\n"
    "s_add_u32 m0, s70, 0x14000\n"
    "ds_read_b128 a[24:27], v22 offset:22528\n"
    "v_mfma_f32_16x16x32_bf16 v[176:179], a[52:55], a[36:39], v[176:179]\n"
    "ds_read_b128 a[60:63], v22 offset:32768\n"
    "v_mfma_f32_16x16x32_bf16 v[196:199], a[16:19], a[4:7], v[196:199]\n"
    "v_mfma_f32_16x16x32_bf16 v[196:199], a[52:55], a[40:43], v[196:199]\n"
    "buffer_load_dwordx4 v0, s[40:43], s60 offen offset:0 sc1 lds\n"
    "v_mfma_f32_16x16x32_bf16 v[216:219], a[16:19], a[8:11], v[216:219]\n"
    "ds_read_b128 a[28:31], v22 offset:23552\n"
    "v_mfma_f32_16x16x32_bf16 v[216:219], a[52:55], a[44:47], v[216:219]\n"
    "ds_read_b128 a[64:67], v22 offset:33792\n"
    "v_mfma_f32_16x16x32_bf16 v[236:239], a[16:19], a[12:15], v[236:239]\n"
    "s_waitcnt lgkmcnt(4)\n"
    "v_mfma_f32_16x16x32_bf16 v[236:239], a[52:55], a[48:51], v[236:239]\n"
    "buffer_load_dwordx4 v0, s[40:43], s60 offen offset:1024 sc1 lds\n"
    "v_mfma_f32_16x16x32_bf16 v[180:183], a[20:23], a[0:3], v[180:183]\n"
    "s_add_u32 s60, s60, s74\n"
    "ds_read_b128 a[32:35], v22 offset:24576\n"
    "v_mfma_f32_16x16x32_bf16 v[180:183], a[56:59], a[36:39], v[180:183]\n"
    "s_add_u32 m0, m0, s81\n"
    "ds_read_b128 a[68:71], v22 offset:34816\n"
    "v_mfma_f32_16x16x32_bf16 v[200:203], a[20:23], a[4:7], v[200:203]\n"
    "v_mfma_f32_16x16x32_bf16 v[200:203], a[56:59], a[40:43], v[200:203]\n"
    "v_mfma_f32_16x16x32_bf16 v[220:223], a[20:23], a[8:11], v[220:223]\n"
    "buffer_load_dwordx4 v0, s[40:43], s60 offen offset:0 sc1 lds\n"
    "v_mfma_f32_16x16x32_bf16 v[220:223], a[56:59], a[44:47], v[220:223]\n"
    "v_mfma_f32_16x16x32_bf16 v[240:243], a[20:23], a[12:15], v[240:243]\n"
    "v_mfma_f32_16x16x32_bf16 v[240:243], a[56:59], a[48:51], v[240:243]\n"
    "s_waitcnt lgkmcnt(4)\n"
    "v_mfma_f32_16x16x32_bf16 v[184:187], a[24:27], a[0:3], v[184:187]\n"
    "buffer_load_dwordx4 v0, s[40:43], s60 offen offset:1024 sc1 lds\n"
    "v_mfma_f32_16x16x32_bf16 v[184:187], a[60:63], a[36:39], v[184:187]\n"
    "s_waitcnt lgkmcnt(0)\n"
    "v_mfma_f32_16x16x32_bf16 v[204:207], a[24:27], a[4:7], v[204:207]\n"
    "s_waitcnt vmcnt(18)\n"
    "v_xor_b32 v23, 0x14000, v23\n"
    "v_xor_b32 v22, 0x14000, v22\n"
    "v_mfma_f32_16x16x32_bf16 v[204:207], a[60:63], a[40:43], v[204:207]\n"
    "s_barrier\n"
    "v_mfma_f32_16x16x32_bf16 v[224:227], a[24:27], a[8:11], v[224:227]\n"
    "s_cmp_le_u32 s69, 0x1000\n"
    "ds_read_b128 a[144:147], v22 offset:0\n"
    "s_cselect_b32 s59, 4, 2\n"
    "s_lshl_b32 s59, s59, 11\n"
    "s_mul_i32 s60, s59, 40\n"
    "s_add_u32 s60, s60, s68\n"
    "s_add_u32 m0, s82, 0x21000\n"
    "v_mfma_f32_16x16x32_bf16 v[224:227], a[60:63], a[44:47], v[224:227]\n"
    "buffer_load_dwordx4 v0, s[40:43], s60 offen offset:0 sc1 lds\n"
    "v_mfma_f32_16x16x32_bf16 v[244:247], a[24:27], a[12:15], v[244:247]\n"
    "ds_read_b128 a[180:183], v22 offset:10240\n"
    "v_mfma_f32_16x16x32_bf16 v[244:247], a[60:63], a[48:51], v[244:247]\n"
    "ds_read_b128 a[128:131], v23 offset:0\n"
    "v_mfma_f32_16x16x32_bf16 v[188:191], a[28:31], a[0:3], v[188:191]\n"
    "ds_read_b128 a[164:167], v23 offset:1024\n"
    "v_mfma_f32_16x16x32_bf16 v[188:191], a[64:67], a[36:39], v[188:191]\n"
    "buffer_load_dwordx4 v0, s[40:43], s60 offen offset:1024 sc1 lds\n"
    "v_mfma_f32_16x16x32_bf16 v[208:211], a[28:31], a[4:7], v[208:211]\n"
    "ds_read_b128 a[132:135], v23 offset:2048\n"
    "v_mfma_f32_16x16x32_bf16 v[208:211], a[64:67], a[40:43], v[208:211]\n"
    "ds_read_b128 a[168:171], v23 offset:3072\n"
    "v_mfma_f32_16x16x32_bf16 v[228:231], a[28:31], a[8:11], v[228:231]\n"
    "ds_read_b128 a[136:139], v23 offset:4096\n"
    "v_mfma_f32_16x16x32_bf16 v[228:231], a[64:67], a[44:47], v[228:231]\n"
    "buffer_load_dwordx4 v0, s[40:43], s60 offen offset:2048 sc1 lds\n"
    "v_mfma_f32_16x16x32_bf16 v[248:251], a[28:31], a[12:15], v[248:251]\n"
    "ds_read_b128 a[172:175], v23 offset:5120\n"
    "v_mfma_f32_16x16x32_bf16 v[248:251], a[64:67], a[48:51], v[248:251]\n"
    "ds_read_b128 a[140:143], v23 offset:6144\n"
    "v_mfma_f32_16x16x32_bf16 v[192:195], a[32:35], a[0:3], v[192:195]\n"
    "ds_read_b128 a[176:179], v23 offset:7168\n"
    "v_mfma_f32_16x16x32_bf16 v[192:195], a[68:71], a[36:39], v[192:195]\n"
    "buffer_load_dwordx4 v0, s[40:43], s60 offen offset:3072 sc1 lds\n"
    "v_mfma_f32_16x16x32_bf16 v[212:215], a[32:35], a[4:7], v[212:215]\n"
    "s_addk_i32 s60, 0x1000\n"
    "ds_read_b128 a[148:151], v22 offset:1024\n"
    "v_mfma_f32_16x16x32_bf16 v[212:215], a[68:71], a[40:43], v[212:215]\n"
    "s_addk_i32 m0, 0x1000\n"
    "ds_read_b128 a[184:187], v22 offset:11264\n"
    "v_mfma_f32_16x16x32_bf16 v[232:235], a[32:35], a[8:11], v[232:235]\n"
    "v_mfma_f32_16x16x32_bf16 v[232:235], a[68:71], a[44:47], v[232:235]\n"
    "buffer_load_dwordx4 v0, s[40:43], s60 offen offset:0 sc1 lds\n"
    "v_mfma_f32_16x16x32_bf16 v[252:255], a[32:35], a[12:15], v[252:255]\n"
    "s_add_u32 s60, s73, s59\n"
    "v_mfma_f32_16x16x32_bf16 v[252:255], a[68:71], a[48:51], v[252:255]\n"
    "s_waitcnt lgkmcnt(3)\n"
    "v_mfma_f32_16x16x32_bf16 v[176:179], a[144:147], a[128:131], v[176:179]\n"
    "s_add_u32 m0, s70, 0x18000\n"
    "ds_read_b128 a[152:155], v22 offset:2048\n"
    "v_mfma_f32_16x16x32_bf16 v[176:179], a[180:183], a[164:167], v[176:179]\n"
    "ds_read_b128 a[188:191], v22 offset:12288\n"
    "v_mfma_f32_16x16x32_bf16 v[196:199], a[144:147], a[132:135], v[196:199]\n"
    "v_mfma_f32_16x16x32_bf16 v[196:199], a[180:183], a[168:171], v[196:199]\n"
    "buffer_load_dwordx4 v0, s[40:43], s60 offen offset:0 sc1 lds\n"
    "v_mfma_f32_16x16x32_bf16 v[216:219], a[144:147], a[136:139], v[216:219]\n"
    "ds_read_b128 a[156:159], v22 offset:3072\n"
    "v_mfma_f32_16x16x32_bf16 v[216:219], a[180:183], a[172:175], v[216:219]\n"
    "ds_read_b128 a[192:195], v22 offset:13312\n"
    "v_mfma_f32_16x16x32_bf16 v[236:239], a[144:147], a[140:143], v[236:239]\n"
    "s_waitcnt lgkmcnt(4)\n"
    "v_mfma_f32_16x16x32_bf16 v[236:239], a[180:183], a[176:179], v[236:239]\n"
    "buffer_load_dwordx4 v0, s[40:43], s60 offen offset:1024 sc1 lds\n"
    "v_mfma_f32_16x16x32_bf16 v[180:183], a[148:151], a[128:131], v[180:183]\n"
    "s_add_u32 s60, s60, s74\n"
    "ds_read_b128 a[160:163], v22 offset:4096\n"
    "v_mfma_f32_16x16x32_bf16 v[180:183], a[184:187], a[164:167], v[180:183]\n"
    "s_add_u32 m0, m0, s81\n"
    "ds_read_b128 a[196:199], v22 offset:14336\n"
    "v_mfma_f32_16x16x32_bf16 v[200:203], a[148:151], a[132:135], v[200:203]\n"
    "v_mfma_f32_16x16x32_bf16 v[200:203], a[184:187], a[168:171], v[200:203]\n"
    "v_mfma_f32_16x16x32_bf16 v[220:223], a[148:151], a[136:139], v[220:223]\n"
    "buffer_load_dwordx4 v0, s[40:43], s60 offen offset:0 sc1 lds\n"
    "v_mfma_f32_16x16x32_bf16 v[220:223], a[184:187], a[172:175], v[220:223]\n"
    "v_mfma_f32_16x16x32_bf16 v[240:243], a[148:151], a[140:143], v[240:243]\n"
    "v_mfma_f32_16x16x32_bf16 v[240:243], a[184:187], a[176:179], v[240:243]\n"
    "s_waitcnt lgkmcnt(4)\n"
    "v_mfma_f32_16x16x32_bf16 v[184:187], a[152:155], a[128:131], v[184:187]\n"
    "buffer_load_dwordx4 v0, s[40:43], s60 offen offset:1024 sc1 lds\n"
    "v_mfma_f32_16x16x32_bf16 v[184:187], a[188:191], a[164:167], v[184:187]\n"
    "s_waitcnt lgkmcnt(0)\n"
    "v_mfma_f32_16x16x32_bf16 v[204:207], a[152:155], a[132:135], v[204:207]\n"
    "s_waitcnt vmcnt(18)\n"
    "v_mfma_f32_16x16x32_bf16 v[204:207], a[188:191], a[168:171], v[204:207]\n"
    "s_barrier\n"
    "v_mfma_f32_16x16x32_bf16 v[224:227], a[152:155], a[136:139], v[224:227]\n"
    "s_addk_i32 s59, 0x800\n"
    "ds_read_b128 a[16:19], v22 offset:20480\n"
    "s_mul_i32 s60, s59, 40\n"
    "s_add_u32 s60, s60, s68\n"
    "s_add_u32 m0, s82, 0x8000\n"
    "v_mfma_f32_16x16x32_bf16 v[224:227], a[188:191], a[172:175], v[224:227]\n"
    "buffer_load_dwordx4 v0, s[40:43], s60 offen offset:0 sc1 lds\n"
    "v_mfma_f32_16x16x32_bf16 v[244:247], a[152:155], a[140:143], v[244:247]\n"
    "ds_read_b128 a[52:55], v22 offset:30720\n"
    "v_mfma_f32_16x16x32_bf16 v[244:247], a[188:191], a[176:179], v[244:247]\n"
    "ds_read_b128 a[0:3], v23 offset:16384\n"
    "v_mfma_f32_16x16x32_bf16 v[188:191], a[156:159], a[128:131], v[188:191]\n"
    "ds_read_b128 a[36:39], v23 offset:17408\n"
    "v_mfma_f32_16x16x32_bf16 v[188:191], a[192:195], a[164:167], v[188:191]\n"
    "buffer_load_dwordx4 v0, s[40:43], s60 offen offset:1024 sc1 lds\n"
    "v_mfma_f32_16x16x32_bf16 v[208:211], a[156:159], a[132:135], v[208:211]\n"
    "ds_read_b128 a[4:7], v23 offset:18432\n"
    "v_mfma_f32_16x16x32_bf16 v[208:211], a[192:195], a[168:171], v[208:211]\n"
    "ds_read_b128 a[40:43], v23 offset:19456\n"
    "v_mfma_f32_16x16x32_bf16 v[228:231], a[156:159], a[136:139], v[228:231]\n"
    "ds_read_b128 a[8:11], v23 offset:20480\n"
    "v_mfma_f32_16x16x32_bf16 v[228:231], a[192:195], a[172:175], v[228:231]\n"
    "buffer_load_dwordx4 v0, s[40:43], s60 offen offset:2048 sc1 lds\n"
    "v_mfma_f32_16x16x32_bf16 v[248:251], a[156:159], a[140:143], v[248:251]\n"
    "ds_read_b128 a[44:47], v23 offset:21504\n"
    "v_mfma_f32_16x16x32_bf16 v[248:251], a[192:195], a[176:179], v[248:251]\n"
    "ds_read_b128 a[12:15], v23 offset:22528\n"
    "v_mfma_f32_16x16x32_bf16 v[192:195], a[160:163], a[128:131], v[192:195]\n"
    "ds_read_b128 a[48:51], v23 offset:23552\n"
    "v_mfma_f32_16x16x32_bf16 v[192:195], a[196:199], a[164:167], v[192:195]\n"
    "buffer_load_dwordx4 v0, s[40:43], s60 offen offset:3072 sc1 lds\n"
    "v_mfma_f32_16x16x32_bf16 v[212:215], a[160:163], a[132:135], v[212:215]\n"
    "s_addk_i32 s60, 0x1000\n"
    "ds_read_b128 a[20:23], v22 offset:21504\n"
    "v_mfma_f32_16x16x32_bf16 v[212:215], a[196:199], a[168:171], v[212:215]\n"
    "s_addk_i32 m0, 0x1000\n"
    "ds_read_b128 a[56:59], v22 offset:31744\n"
    "v_mfma_f32_16x16x32_bf16 v[232:235], a[160:163], a[136:139], v[232:235]\n"
    "v_mfma_f32_16x16x32_bf16 v[232:235], a[196:199], a[172:175], v[232:235]\n"
    "buffer_load_dwordx4 v0, s[40:43], s60 offen offset:0 sc1 lds\n"
    "v_mfma_f32_16x16x32_bf16 v[252:255], a[160:163], a[140:143], v[252:255]\n"
    "s_add_u32 s60, s73, s59\n"
    "v_mfma_f32_16x16x32_bf16 v[252:255], a[196:199], a[176:179], v[252:255]\n"
    "s_waitcnt lgkmcnt(3)\n"
    "v_mfma_f32_16x16x32_bf16 v[96:99], a[16:19], a[0:3], v[96:99]\n"
    "s_add_u32 m0, s70, 0\n"
    "ds_read_b128 a[24:27], v22 offset:22528\n"
    "v_mfma_f32_16x16x32_bf16 v[96:99], a[52:55], a[36:39], v[96:99]\n"
    "ds_read_b128 a[60:63], v22 offset:32768\n"
    "v_mfma_f32_16x16x32_bf16 v[116:119], a[16:19], a[4:7], v[116:119]\n"
    "v_mfma_f32_16x16x32_bf16 v[116:119], a[52:55], a[40:43], v[116:119]\n"
    "buffer_load_dwordx4 v0, s[40:43], s60 offen offset:0 sc1 lds\n"
    "v_mfma_f32_16x16x32_bf16 v[136:139], a[16:19], a[8:11], v[136:139]\n"
    "ds_read_b128 a[28:31], v22 offset:23552\n"
    "v_mfma_f32_16x16x32_bf16 v[136:139], a[52:55], a[44:47], v[136:139]\n"
    "ds_read_b128 a[64:67], v22 offset:33792\n"
    "v_mfma_f32_16x16x32_bf16 v[156:159], a[16:19], a[12:15], v[156:159]\n"
    "s_waitcnt lgkmcnt(4)\n"
    "v_mfma_f32_16x16x32_bf16 v[156:159], a[52:55], a[48:51], v[156:159]\n"
    "buffer_load_dwordx4 v0, s[40:43], s60 offen offset:1024 sc1 lds\n"
    "v_mfma_f32_16x16x32_bf16 v[100:103], a[20:23], a[0:3], v[100:103]\n"
    "s_add_u32 s60, s60, s74\n"
    "ds_read_b128 a[32:35], v22 offset:24576\n"
    "v_cvt_pk_bf16_f32 v32, v196, v197\n"
    "v_mfma_f32_16x16x32_bf16 v[100:103], a[56:59], a[36:39], v[100:103]\n"
    "s_add_u32 m0, m0, s81\n"
    "ds_read_b128 a[68:71], v22 offset:34816\n"
    "v_cvt_pk_bf16_f32 v33, v198, v199\n"
    "v_mfma_f32_16x16x32_bf16 v[120:123], a[20:23], a[4:7], v[120:123]\n"
    "v_cvt_pk_bf16_f32 v34, v200, v201\n"
    "v_cvt_pk_bf16_f32 v35, v202, v203\n"
    "v_mfma_f32_16x16x32_bf16 v[120:123], a[56:59], a[40:43], v[120:123]\n"
    "v_cvt_pk_bf16_f32 v36, v204, v205\n"
    "v_cvt_pk_bf16_f32 v37, v206, v207\n"
    "v_mfma_f32_16x16x32_bf16 v[140:143], a[20:23], a[8:11], v[140:143]\n"
    "v_cvt_pk_bf16_f32 v38, v208, v209\n"
    "v_cvt_pk_bf16_f32 v39, v210, v211\n"
    "buffer_load_dwordx4 v0, s[40:43], s60 offen offset:0 sc1 lds\n"
    "v_mfma_f32_16x16x32_bf16 v[140:143], a[56:59], a[44:47], v[140:143]\n"
    "v_cvt_pk_bf16_f32 v40, v212, v213\n"
    "v_cvt_pk_bf16_f32 v41, v214, v215\n"
    "v_mfma_f32_16x16x32_bf16 v[160:163], a[20:23], a[12:15], v[160:163]\n"
    "v_cvt_pk_bf16_f32 v42, v216, v217\n"
    "v_cvt_pk_bf16_f32 v43, v218, v219\n"
    "v_mfma_f32_16x16x32_bf16 v[160:163], a[56:59], a[48:51], v[160:163]\n"
    "s_waitcnt lgkmcnt(4)\n"
    "v_cvt_pk_bf16_f32 v44, v220, v221\n"
    "v_cvt_pk_bf16_f32 v45, v222, v223\n"
    "v_mfma_f32_16x16x32_bf16 v[104:107], a[24:27], a[0:3], v[104:107]\n"
    "v_cvt_pk_bf16_f32 v46, v224, v225\n"
    "v_cvt_pk_bf16_f32 v47, v226, v227\n"
    "buffer_load_dwordx4 v0, s[40:43], s60 offen offset:1024 sc1 lds\n"
    "v_mfma_f32_16x16x32_bf16 v[104:107], a[60:63], a[36:39], v[104:107]\n"
    "s_waitcnt lgkmcnt(0)\n"
    "v_cvt_pk_bf16_f32 v48, v228, v229\n"
    "v_cvt_pk_bf16_f32 v49, v230, v231\n"
    "v_mfma_f32_16x16x32_bf16 v[124:127], a[24:27], a[4:7], v[124:127]\n"
    "s_waitcnt vmcnt(18)\n"
    "v_xor_b32 v23, 0x14000, v23\n"
    "v_xor_b32 v22, 0x14000, v22\n"
    "v_mfma_f32_16x16x32_bf16 v[124:127], a[60:63], a[40:43], v[124:127]\n"
    "s_barrier\n"
    "v_cvt_pk_bf16_f32 v50, v232, v233\n"
    "v_cvt_pk_bf16_f32 v51, v234, v235\n"
    "v_mfma_f32_16x16x32_bf16 v[144:147], a[24:27], a[8:11], v[144:147]\n"
    "s_cmp_le_u32 s69, 0x2000\n"
    "ds_read_b128 a[144:147], v22 offset:0\n"
    "v_cvt_pk_bf16_f32 v52, v236, v237\n"
    "s_cselect_b32 s59, 6, 4\n"
    "s_lshl_b32 s59, s59, 11\n"
    "s_mul_i32 s60, s59, 40\n"
    "s_add_u32 s60, s60, s68\n"
    "s_add_u32 m0, s82, 0xd000\n"
    "v_mfma_f32_16x16x32_bf16 v[144:147], a[60:63], a[44:47], v[144:147]\n"
    "v_cvt_pk_bf16_f32 v53, v238, v239\n"
    "v_cvt_pk_bf16_f32 v54, v240, v241\n"
    "buffer_load_dwordx4 v0, s[40:43], s60 offen offset:0 sc1 lds\n"
    "v_mfma_f32_16x16x32_bf16 v[164:167], a[24:27], a[12:15], v[164:167]\n"
    "ds_read_b128 a[180:183], v22 offset:10240\n"
    "v_cvt_pk_bf16_f32 v55, v242, v243\n"
    "v_mfma_f32_16x16x32_bf16 v[164:167], a[60:63], a[48:51], v[164:167]\n"
    "ds_read_b128 a[128:131], v23 offset:0\n"
    "v_cvt_pk_bf16_f32 v56, v244, v245\n"
    "v_mfma_f32_16x16x32_bf16 v[108:111], a[28:31], a[0:3], v[108:111]\n"
    "ds_read_b128 a[164:167], v23 offset:1024\n"
    "v_cvt_pk_bf16_f32 v57, v246, v247\n"
    "v_mfma_f32_16x16x32_bf16 v[108:111], a[64:67], a[36:39], v[108:111]\n"
    "v_cvt_pk_bf16_f32 v58, v248, v249\n"
    "v_cvt_pk_bf16_f32 v59, v250, v251\n"
    "buffer_load_dwordx4 v0, s[40:43], s60 offen offset:1024 sc1 lds\n"
    "v_mfma_f32_16x16x32_bf16 v[128:131], a[28:31], a[4:7], v[128:131]\n"
    "ds_read_b128 a[132:135], v23 offset:2048\n"
    "v_cvt_pk_bf16_f32 v60, v252, v253\n"
    "v_mfma_f32_16x16x32_bf16 v[128:131], a[64:67], a[40:43], v[128:131]\n"
    "ds_read_b128 a[168:171], v23 offset:3072\n"
    "v_cvt_pk_bf16_f32 v61, v254, v255\n"
    "v_mfma_f32_16x16x32_bf16 v[148:151], a[28:31], a[8:11], v[148:151]\n"
    "ds_read_b128 a[136:139], v23 offset:4096\n"
    "v_mfma_f32_16x16x32_bf16 v[148:151], a[64:67], a[44:47], v[148:151]\n"
    "buffer_load_dwordx4 v0, s[40:43], s60 offen offset:2048 sc1 lds\n"
    "v_mfma_f32_16x16x32_bf16 v[168:171], a[28:31], a[12:15], v[168:171]\n"
    "ds_read_b128 a[172:175], v23 offset:5120\n"
    "v_mfma_f32_16x16x32_bf16 v[168:171], a[64:67], a[48:51], v[168:171]\n"
    "ds_read_b128 a[140:143], v23 offset:6144\n"
    "v_mfma_f32_16x16x32_bf16 v[112:115], a[32:35], a[0:3], v[112:115]\n"
    "ds_read_b128 a[176:179], v23 offset:7168\n"
    "v_mfma_f32_16x16x32_bf16 v[112:115], a[68:71], a[36:39], v[112:115]\n"
    "buffer_load_dwordx4 v0, s[40:43], s60 offen offset:3072 sc1 lds\n"
    "v_mfma_f32_16x16x32_bf16 v[132:135], a[32:35], a[4:7], v[132:135]\n"
    "s_addk_i32 s60, 0x1000\n"
    "ds_read_b128 a[148:151], v22 offset:1024\n"
    "v_mfma_f32_16x16x32_bf16 v[132:135], a[68:71], a[40:43], v[132:135]\n"
    "s_addk_i32 m0, 0x1000\n"
    "ds_read_b128 a[184:187], v22 offset:11264\n"
    "v_mfma_f32_16x16x32_bf16 v[152:155], a[32:35], a[8:11], v[152:155]\n"
    "v_mfma_f32_16x16x32_bf16 v[152:155], a[68:71], a[44:47], v[152:155]\n"
    "buffer_load_dwordx4 v0, s[40:43], s60 offen offset:0 sc1 lds\n"
    "v_mfma_f32_16x16x32_bf16 v[172:175], a[32:35], a[12:15], v[172:175]\n"
    "v_mfma_f32_16x16x32_bf16 v[172:175], a[68:71], a[48:51], v[172:175]\n"
    "s_waitcnt lgkmcnt(3)\n"
    "v_mfma_f32_16x16x32_bf16 v[96:99], a[144:147], a[128:131], v[96:99]\n"
    "ds_read_b128 a[152:155], v22 offset:2048\n"
    "v_mfma_f32_16x16x32_bf16 v[96:99], a[180:183], a[164:167], v[96:99]\n"
    "ds_read_b128 a[188:191], v22 offset:12288\n"
    "v_mfma_f32_16x16x32_bf16 v[116:119], a[144:147], a[132:135], v[116:119]\n"
    "buffer_store_dwordx4 v[32:35], v0, s[40:43], s36 offen sc1\n"
    "s_add_u32 s60, s73, s59\n"
    "s_add_u32 m0, s70, 0x4000\n"
    "v_mfma_f32_16x16x32_bf16 v[116:119], a[180:183], a[168:171], v[116:119]\n"
    "buffer_load_dwordx4 v0, s[40:43], s60 offen offset:0 sc1 lds\n"
    "v_mfma_f32_16x16x32_bf16 v[136:139], a[144:147], a[136:139], v[136:139]\n"
    "ds_read_b128 a[156:159], v22 offset:3072\n"
    "v_mfma_f32_16x16x32_bf16 v[136:139], a[180:183], a[172:175], v[136:139]\n"
    "ds_read_b128 a[192:195], v22 offset:13312\n"
    "v_mfma_f32_16x16x32_bf16 v[156:159], a[144:147], a[140:143], v[156:159]\n"
    "s_waitcnt lgkmcnt(4)\n"
    "v_mfma_f32_16x16x32_bf16 v[156:159], a[180:183], a[176:179], v[156:159]\n"
    "buffer_load_dwordx4 v0, s[40:43], s60 offen offset:1024 sc1 lds\n"
    "v_mfma_f32_16x16x32_bf16 v[100:103], a[148:151], a[128:131], v[100:103]\n"
    "ds_read_b128 a[160:163], v22 offset:4096\n"
    "v_mfma_f32_16x16x32_bf16 v[100:103], a[184:187], a[164:167], v[100:103]\n"
    "ds_read_b128 a[196:199], v22 offset:14336\n"
    "v_mfma_f32_16x16x32_bf16 v[120:123], a[148:151], a[132:135], v[120:123]\n"
    "buffer_store_dwordx4 v[36:39], v0, s[40:43], s36 offen offset:1024 sc1\n"
    "v_mfma_f32_16x16x32_bf16 v[120:123], a[184:187], a[168:171], v[120:123]\n"
    "s_add_u32 s60, s60, s74\n"
    "s_add_u32 m0, m0, s81\n"
    "v_mfma_f32_16x16x32_bf16 v[140:143], a[148:151], a[136:139], v[140:143]\n"
    "buffer_load_dwordx4 v0, s[40:43], s60 offen offset:0 sc1 lds\n"
    "v_mfma_f32_16x16x32_bf16 v[140:143], a[184:187], a[172:175], v[140:143]\n"
    "v_mfma_f32_16x16x32_bf16 v[160:163], a[148:151], a[140:143], v[160:163]\n"
    "v_mfma_f32_16x16x32_bf16 v[160:163], a[184:187], a[176:179], v[160:163]\n"
    "s_waitcnt lgkmcnt(4)\n"
    "v_mfma_f32_16x16x32_bf16 v[104:107], a[152:155], a[128:131], v[104:107]\n"
    "buffer_load_dwordx4 v0, s[40:43], s60 offen offset:1024 sc1 lds\n"
    "v_mfma_f32_16x16x32_bf16 v[104:107], a[188:191], a[164:167], v[104:107]\n"
    "s_waitcnt lgkmcnt(0)\n"
    "v_mfma_f32_16x16x32_bf16 v[124:127], a[152:155], a[132:135], v[124:127]\n"
    "s_waitcnt vmcnt(20)\n"
    "v_mfma_f32_16x16x32_bf16 v[124:127], a[188:191], a[168:171], v[124:127]\n"
    "s_barrier\n"
    "v_mfma_f32_16x16x32_bf16 v[144:147], a[152:155], a[136:139], v[144:147]\n"
    "s_addk_i32 s59, 0x800\n"
    "ds_read_b128 a[16:19], v22 offset:20480\n"
    "s_mul_i32 s60, s59, 40\n"
    "s_add_u32 s60, s60, s68\n"
    "s_add_u32 m0, s82, 0x1c000\n"
    "v_mfma_f32_16x16x32_bf16 v[144:147], a[188:191], a[172:175], v[144:147]\n"
    "buffer_load_dwordx4 v0, s[40:43], s60 offen offset:0 sc1 lds\n"
    "v_mfma_f32_16x16x32_bf16 v[164:167], a[152:155], a[140:143], v[164:167]\n"
    "ds_read_b128 a[52:55], v22 offset:30720\n"
    "v_mfma_f32_16x16x32_bf16 v[164:167], a[188:191], a[176:179], v[164:167]\n"
    "ds_read_b128 a[0:3], v23 offset:16384\n"
    "v_mfma_f32_16x16x32_bf16 v[108:111], a[156:159], a[128:131], v[108:111]\n"
    "ds_read_b128 a[36:39], v23 offset:17408\n"
    "v_mfma_f32_16x16x32_bf16 v[108:111], a[192:195], a[164:167], v[108:111]\n"
    "buffer_load_dwordx4 v0, s[40:43], s60 offen offset:1024 sc1 lds\n"
    "v_mfma_f32_16x16x32_bf16 v[128:131], a[156:159], a[132:135], v[128:131]\n"
    "ds_read_b128 a[4:7], v23 offset:18432\n"
    "v_mfma_f32_16x16x32_bf16 v[128:131], a[192:195], a[168:171], v[128:131]\n"
    "ds_read_b128 a[40:43], v23 offset:19456\n"
    "v_mfma_f32_16x16x32_bf16 v[148:151], a[156:159], a[136:139], v[148:151]\n"
    "ds_read_b128 a[8:11], v23 offset:20480\n"
    "v_mfma_f32_16x16x32_bf16 v[148:151], a[192:195], a[172:175], v[148:151]\n"
    "buffer_store_dwordx2 v[40:41], v26, s[40:43], s36 offen offset:2048 sc1\n"
    "v_mfma_f32_16x16x32_bf16 v[168:171], a[156:159], a[140:143], v[168:171]\n"
    "s_addk_i32 s36, 0x1000\n"
    "ds_read_b128 a[44:47], v23 offset:21504\n"
    "v_mfma_f32_16x16x32_bf16 v[168:171], a[192:195], a[176:179], v[168:171]\n"
    "ds_read_b128 a[12:15], v23 offset:22528\n"
    "v_mfma_f32_16x16x32_bf16 v[112:115], a[160:163], a[128:131], v[112:115]\n"
    "ds_read_b128 a[48:51], v23 offset:23552\n"
    "v_mfma_f32_16x16x32_bf16 v[112:115], a[196:199], a[164:167], v[112:115]\n"
    "buffer_load_dwordx4 v0, s[40:43], s60 offen offset:2048 sc1 lds\n"
    "v_mfma_f32_16x16x32_bf16 v[132:135], a[160:163], a[132:135], v[132:135]\n"
    "ds_read_b128 a[20:23], v22 offset:21504\n"
    "v_mfma_f32_16x16x32_bf16 v[132:135], a[196:199], a[168:171], v[132:135]\n"
    "ds_read_b128 a[56:59], v22 offset:31744\n"
    "v_mfma_f32_16x16x32_bf16 v[152:155], a[160:163], a[136:139], v[152:155]\n"
    "buffer_store_dwordx4 v[42:45], v0, s[40:43], s37 offen sc1\n"
    "v_mfma_f32_16x16x32_bf16 v[152:155], a[196:199], a[172:175], v[152:155]\n"
    "buffer_load_dwordx4 v0, s[40:43], s60 offen offset:3072 sc1 lds\n"
    "v_mfma_f32_16x16x32_bf16 v[172:175], a[160:163], a[140:143], v[172:175]\n"
    "buffer_store_dwordx4 v[46:49], v0, s[40:43], s37 offen offset:1024 sc1\n"
    "v_mfma_f32_16x16x32_bf16 v[172:175], a[196:199], a[176:179], v[172:175]\n"
    "s_waitcnt lgkmcnt(3)\n"
    "v_mfma_f32_16x16x32_bf16 v[96:99], a[16:19], a[0:3], v[96:99]\n"
    "ds_read_b128 a[24:27], v22 offset:22528\n"
    "v_mfma_f32_16x16x32_bf16 v[96:99], a[52:55], a[36:39], v[96:99]\n"
    "ds_read_b128 a[60:63], v22 offset:32768\n"
    "v_mfma_f32_16x16x32_bf16 v[116:119], a[16:19], a[4:7], v[116:119]\n"
    "buffer_store_dwordx2 v[50:51], v26, s[40:43], s37 offen offset:2048 sc1\n"
    "s_addk_i32 s37, 0x1000\n"
    "s_addk_i32 s60, 0x1000\n"
    "s_addk_i32 m0, 0x1000\n"
    "v_mfma_f32_16x16x32_bf16 v[116:119], a[52:55], a[40:43], v[116:119]\n"
    "buffer_load_dwordx4 v0, s[40:43], s60 offen offset:0 sc1 lds\n"
    "v_mfma_f32_16x16x32_bf16 v[136:139], a[16:19], a[8:11], v[136:139]\n"
    "s_add_u32 s60, s73, s59\n"
    "ds_read_b128 a[28:31], v22 offset:23552\n"
    "v_mfma_f32_16x16x32_bf16 v[136:139], a[52:55], a[44:47], v[136:139]\n"
    "s_add_u32 m0, s70, 0x14000\n"
    "ds_read_b128 a[64:67], v22 offset:33792\n"
    "v_mfma_f32_16x16x32_bf16 v[156:159], a[16:19], a[12:15], v[156:159]\n"
    "s_waitcnt lgkmcnt(4)\n"
    "v_mfma_f32_16x16x32_bf16 v[156:159], a[52:55], a[48:51], v[156:159]\n"
    "buffer_load_dwordx4 v0, s[40:43], s60 offen offset:0 sc1 lds\n"
    "v_mfma_f32_16x16x32_bf16 v[100:103], a[20:23], a[0:3], v[100:103]\n"
    "ds_read_b128 a[32:35], v22 offset:24576\n"
    "v_mfma_f32_16x16x32_bf16 v[100:103], a[56:59], a[36:39], v[100:103]\n"
    "ds_read_b128 a[68:71], v22 offset:34816\n"
    "v_mfma_f32_16x16x32_bf16 v[120:123], a[20:23], a[4:7], v[120:123]\n"
    "buffer_store_dwordx4 v[52:55], v0, s[40:43], s67 offen sc1\n"
    "v_mfma_f32_16x16x32_bf16 v[120:123], a[56:59], a[40:43], v[120:123]\n"
    "v_mfma_f32_16x16x32_bf16 v[140:143], a[20:23], a[8:11], v[140:143]\n"
    "buffer_load_dwordx4 v0, s[40:43], s60 offen offset:1024 sc1 lds\n"
    "v_mfma_f32_16x16x32_bf16 v[140:143], a[56:59], a[44:47], v[140:143]\n"
    "s_add_u32 s60, s60, s74\n"
    "s_add_u32 m0, m0, s81\n"
    "v_mfma_f32_16x16x32_bf16 v[160:163], a[20:23], a[12:15], v[160:163]\n"
    "buffer_load_dwordx4 v0, s[40:43], s60 offen offset:0 sc1 lds\n"
    "v_mfma_f32_16x16x32_bf16 v[160:163], a[56:59], a[48:51], v[160:163]\n"
    "s_waitcnt lgkmcnt(4)\n"
    "v_mfma_f32_16x16x32_bf16 v[104:107], a[24:27], a[0:3], v[104:107]\n"
    "buffer_load_dwordx4 v0, s[40:43], s60 offen offset:1024 sc1 lds\n"
    "v_mfma_f32_16x16x32_bf16 v[104:107], a[60:63], a[36:39], v[104:107]\n"
    "s_waitcnt lgkmcnt(0)\n"
    "v_mfma_f32_16x16x32_bf16 v[124:127], a[24:27], a[4:7], v[124:127]\n"
    "s_waitcnt vmcnt(25)\n"
    "v_xor_b32 v23, 0x14000, v23\n"
    "v_xor_b32 v22, 0x14000, v22\n"
    "v_mfma_f32_16x16x32_bf16 v[124:127], a[60:63], a[40:43], v[124:127]\n"
    "s_barrier\n"
    "v_mfma_f32_16x16x32_bf16 v[144:147], a[24:27], a[8:11], v[144:147]\n"
    "s_cmp_le_u32 s69, 0x3000\n"
    "ds_read_b128 a[144:147], v22 offset:0\n"
    "s_cselect_b32 s59, 8, 6\n"
    "s_lshl_b32 s59, s59, 11\n"
    "s_mul_i32 s60, s59, 40\n"
    "s_add_u32 s60, s60, s68\n"
    "s_add_u32 m0, s82, 0x21000\n"
    "v_mfma_f32_16x16x32_bf16 v[144:147], a[60:63], a[44:47], v[144:147]\n"
    "buffer_load_dwordx4 v0, s[40:43], s60 offen offset:0 sc1 lds\n"
    "v_mfma_f32_16x16x32_bf16 v[164:167], a[24:27], a[12:15], v[164:167]\n"
    "ds_read_b128 a[180:183], v22 offset:10240\n"
    "v_mfma_f32_16x16x32_bf16 v[164:167], a[60:63], a[48:51], v[164:167]\n"
    "ds_read_b128 a[128:131], v23 offset:0\n"
    "v_mfma_f32_16x16x32_bf16 v[108:111], a[28:31], a[0:3], v[108:111]\n"
    "ds_read_b128 a[164:167], v23 offset:1024\n"
    "v_mfma_f32_16x16x32_bf16 v[108:111], a[64:67], a[36:39], v[108:111]\n"
    "buffer_load_dwordx4 v0, s[40:43], s60 offen offset:1024 sc1 lds\n"
    "v_mfma_f32_16x16x32_bf16 v[128:131], a[28:31], a[4:7], v[128:131]\n"
    "ds_read_b128 a[132:135], v23 offset:2048\n"
    "v_mfma_f32_16x16x32_bf16 v[128:131], a[64:67], a[40:43], v[128:131]\n"
    "ds_read_b128 a[168:171], v23 offset:3072\n"
    "v_mfma_f32_16x16x32_bf16 v[148:151], a[28:31], a[8:11], v[148:151]\n"
    "ds_read_b128 a[136:139], v23 offset:4096\n"
    "v_mfma_f32_16x16x32_bf16 v[148:151], a[64:67], a[44:47], v[148:151]\n"
    "buffer_store_dwordx4 v[56:59], v0, s[40:43], s67 offen offset:1024 sc1\n"
    "v_mfma_f32_16x16x32_bf16 v[168:171], a[28:31], a[12:15], v[168:171]\n"
    "ds_read_b128 a[172:175], v23 offset:5120\n"
    "v_mfma_f32_16x16x32_bf16 v[168:171], a[64:67], a[48:51], v[168:171]\n"
    "ds_read_b128 a[140:143], v23 offset:6144\n"
    "v_mfma_f32_16x16x32_bf16 v[112:115], a[32:35], a[0:3], v[112:115]\n"
    "ds_read_b128 a[176:179], v23 offset:7168\n"
    "v_mfma_f32_16x16x32_bf16 v[112:115], a[68:71], a[36:39], v[112:115]\n"
    "buffer_load_dwordx4 v0, s[40:43], s60 offen offset:2048 sc1 lds\n"
    "v_mfma_f32_16x16x32_bf16 v[132:135], a[32:35], a[4:7], v[132:135]\n"
    "ds_read_b128 a[148:151], v22 offset:1024\n"
    "v_mfma_f32_16x16x32_bf16 v[132:135], a[68:71], a[40:43], v[132:135]\n"
    "ds_read_b128 a[184:187], v22 offset:11264\n"
    "v_mfma_f32_16x16x32_bf16 v[152:155], a[32:35], a[8:11], v[152:155]\n"
    "buffer_store_dwordx2 v[60:61], v26, s[40:43], s67 offen offset:2048 sc1\n"
    "s_addk_i32 s67, 0x1000\n"
    "v_mfma_f32_16x16x32_bf16 v[152:155], a[68:71], a[44:47], v[152:155]\n"
    "buffer_load_dwordx4 v0, s[40:43], s60 offen offset:3072 sc1 lds\n"
    "s_addk_i32 s60, 0x1000\n"
    "s_addk_i32 m0, 0x1000\n"
    "v_mfma_f32_16x16x32_bf16 v[172:175], a[32:35], a[12:15], v[172:175]\n"
    "buffer_load_dwordx4 v0, s[40:43], s60 offen offset:0 sc1 lds\n"
    "v_mfma_f32_16x16x32_bf16 v[172:175], a[68:71], a[48:51], v[172:175]\n"
    "s_waitcnt lgkmcnt(3)\n"
    "v_mfma_f32_16x16x32_bf16 v[96:99], a[144:147], a[128:131], v[96:99]\n"
    "s_add_u32 s60, s73, s59\n"
    "ds_read_b128 a[152:155], v22 offset:2048\n"
    "v_mfma_f32_16x16x32_bf16 v[96:99], a[180:183], a[164:167], v[96:99]\n"
    "s_add_u32 m0, s70, 0x18000\n"
    "ds_read_b128 a[188:191], v22 offset:12288\n"
    "v_mfma_f32_16x16x32_bf16 v[116:119], a[144:147], a[132:135], v[116:119]\n"
    "v_mfma_f32_16x16x32_bf16 v[116:119], a[180:183], a[168:171], v[116:119]\n"
    "buffer_load_dwordx4 v0, s[40:43], s60 offen offset:0 sc1 lds\n"
    "v_mfma_f32_16x16x32_bf16 v[136:139], a[144:147], a[136:139], v[136:139]\n"
    "ds_read_b128 a[156:159], v22 offset:3072\n"
    "v_mfma_f32_16x16x32_bf16 v[136:139], a[180:183], a[172:175], v[136:139]\n"
    "ds_read_b128 a[192:195], v22 offset:13312\n"
    "v_mfma_f32_16x16x32_bf16 v[156:159], a[144:147], a[140:143], v[156:159]\n"
    "s_waitcnt lgkmcnt(4)\n"
    "v_mfma_f32_16x16x32_bf16 v[156:159], a[180:183], a[176:179], v[156:159]\n"
    "buffer_load_dwordx4 v0, s[40:43], s60 offen offset:1024 sc1 lds\n"
    "v_mfma_f32_16x16x32_bf16 v[100:103], a[148:151], a[128:131], v[100:103]\n"
    "s_add_u32 s60, s60, s74\n"
    "ds_read_b128 a[160:163], v22 offset:4096\n"
    "v_mfma_f32_16x16x32_bf16 v[100:103], a[184:187], a[164:167], v[100:103]\n"
    "s_add_u32 m0, m0, s81\n"
    "ds_read_b128 a[196:199], v22 offset:14336\n"
    "v_mfma_f32_16x16x32_bf16 v[120:123], a[148:151], a[132:135], v[120:123]\n"
    "v_mfma_f32_16x16x32_bf16 v[120:123], a[184:187], a[168:171], v[120:123]\n"
    "v_mfma_f32_16x16x32_bf16 v[140:143], a[148:151], a[136:139], v[140:143]\n"
    "buffer_load_dwordx4 v0, s[40:43], s60 offen offset:0 sc1 lds\n"
    "v_mfma_f32_16x16x32_bf16 v[140:143], a[184:187], a[172:175], v[140:143]\n"
    "v_mfma_f32_16x16x32_bf16 v[160:163], a[148:151], a[140:143], v[160:163]\n"
    "v_mfma_f32_16x16x32_bf16 v[160:163], a[184:187], a[176:179], v[160:163]\n"
    "s_waitcnt lgkmcnt(4)\n"
    "v_mfma_f32_16x16x32_bf16 v[104:107], a[152:155], a[128:131], v[104:107]\n"
    "buffer_load_dwordx4 v0, s[40:43], s60 offen offset:1024 sc1 lds\n"
    "v_mfma_f32_16x16x32_bf16 v[104:107], a[188:191], a[164:167], v[104:107]\n"
    "s_waitcnt lgkmcnt(0)\n"
    "v_mfma_f32_16x16x32_bf16 v[124:127], a[152:155], a[132:135], v[124:127]\n"
    "s_waitcnt vmcnt(25)\n"
    "v_mfma_f32_16x16x32_bf16 v[124:127], a[188:191], a[168:171], v[124:127]\n"
    "s_barrier\n"
    "v_mfma_f32_16x16x32_bf16 v[144:147], a[152:155], a[136:139], v[144:147]\n"
    "s_addk_i32 s59, 0x800\n"
    "ds_read_b128 a[16:19], v22 offset:20480\n"
    "s_mul_i32 s60, s59, 40\n"
    "s_add_u32 s60, s60, s68\n"
    "s_add_u32 m0, s82, 0x8000\n"
    "v_mfma_f32_16x16x32_bf16 v[144:147], a[188:191], a[172:175], v[144:147]\n"
    "buffer_load_dwordx4 v0, s[40:43], s60 offen offset:0 sc1 lds\n"
    "v_mfma_f32_16x16x32_bf16 v[164:167], a[152:155], a[140:143], v[164:167]\n"
    "ds_read_b128 a[52:55], v22 offset:30720\n"
    "v_mfma_f32_16x16x32_bf16 v[164:167], a[188:191], a[176:179], v[164:167]\n"
    "ds_read_b128 a[0:3], v23 offset:16384\n"
    "v_mfma_f32_16x16x32_bf16 v[108:111], a[156:159], a[128:131], v[108:111]\n"
    "ds_read_b128 a[36:39], v23 offset:17408\n"
    "v_mfma_f32_16x16x32_bf16 v[108:111], a[192:195], a[164:167], v[108:111]\n"
    "buffer_load_dwordx4 v0, s[40:43], s60 offen offset:1024 sc1 lds\n"
    "v_mfma_f32_16x16x32_bf16 v[128:131], a[156:159], a[132:135], v[128:131]\n"
    "ds_read_b128 a[4:7], v23 offset:18432\n"
    "v_mfma_f32_16x16x32_bf16 v[128:131], a[192:195], a[168:171], v[128:131]\n"
    "ds_read_b128 a[40:43], v23 offset:19456\n"
    "v_mfma_f32_16x16x32_bf16 v[148:151], a[156:159], a[136:139], v[148:151]\n"
    "ds_read_b128 a[8:11], v23 offset:20480\n"
    "v_mfma_f32_16x16x32_bf16 v[148:151], a[192:195], a[172:175], v[148:151]\n"
    "buffer_load_dwordx4 v0, s[40:43], s60 offen offset:2048 sc1 lds\n"
    "v_mfma_f32_16x16x32_bf16 v[168:171], a[156:159], a[140:143], v[168:171]\n"
    "ds_read_b128 a[44:47], v23 offset:21504\n"
    "v_mfma_f32_16x16x32_bf16 v[168:171], a[192:195], a[176:179], v[168:171]\n"
    "ds_read_b128 a[12:15], v23 offset:22528\n"
    "v_mfma_f32_16x16x32_bf16 v[112:115], a[160:163], a[128:131], v[112:115]\n"
    "ds_read_b128 a[48:51], v23 offset:23552\n"
    "v_mfma_f32_16x16x32_bf16 v[112:115], a[196:199], a[164:167], v[112:115]\n"
    "buffer_load_dwordx4 v0, s[40:43], s60 offen offset:3072 sc1 lds\n"
    "v_mfma_f32_16x16x32_bf16 v[132:135], a[160:163], a[132:135], v[132:135]\n"
    "s_addk_i32 s60, 0x1000\n"
    "ds_read_b128 a[20:23], v22 offset:21504\n"
    "v_mfma_f32_16x16x32_bf16 v[132:135], a[196:199], a[168:171], v[132:135]\n"
    "s_addk_i32 m0, 0x1000\n"
    "ds_read_b128 a[56:59], v22 offset:31744\n"
    "v_mfma_f32_16x16x32_bf16 v[152:155], a[160:163], a[136:139], v[152:155]\n"
    "v_mfma_f32_16x16x32_bf16 v[152:155], a[196:199], a[172:175], v[152:155]\n"
    "buffer_load_dwordx4 v0, s[40:43], s60 offen offset:0 sc1 lds\n"
    "v_mfma_f32_16x16x32_bf16 v[172:175], a[160:163], a[140:143], v[172:175]\n"
    "s_add_u32 s60, s73, s59\n"
    "v_mfma_f32_16x16x32_bf16 v[172:175], a[196:199], a[176:179], v[172:175]\n"
    "s_waitcnt lgkmcnt(3)\n"
    "v_mfma_f32_16x16x32_bf16 v[96:99], a[16:19], a[0:3], v[96:99]\n"
    "s_add_u32 m0, s70, 0\n"
    "ds_read_b128 a[24:27], v22 offset:22528\n"
    "v_mfma_f32_16x16x32_bf16 v[96:99], a[52:55], a[36:39], v[96:99]\n"
    "ds_read_b128 a[60:63], v22 offset:32768\n"
    "v_mfma_f32_16x16x32_bf16 v[116:119], a[16:19], a[4:7], v[116:119]\n"
    "v_mfma_f32_16x16x32_bf16 v[116:119], a[52:55], a[40:43], v[116:119]\n"
    "buffer_load_dwordx4 v0, s[40:43], s60 offen offset:0 sc1 lds\n"
    "v_mfma_f32_16x16x32_bf16 v[136:139], a[16:19], a[8:11], v[136:139]\n"
    "ds_read_b128 a[28:31], v22 offset:23552\n"
    "v_mfma_f32_16x16x32_bf16 v[136:139], a[52:55], a[44:47], v[136:139]\n"
    "ds_read_b128 a[64:67], v22 offset:33792\n"
    "v_mfma_f32_16x16x32_bf16 v[156:159], a[16:19], a[12:15], v[156:159]\n"
    "s_waitcnt lgkmcnt(4)\n"
    "v_mfma_f32_16x16x32_bf16 v[156:159], a[52:55], a[48:51], v[156:159]\n"
    "buffer_load_dwordx4 v0, s[40:43], s60 offen offset:1024 sc1 lds\n"
    "v_mfma_f32_16x16x32_bf16 v[100:103], a[20:23], a[0:3], v[100:103]\n"
    "s_add_u32 s60, s60, s74\n"
    "ds_read_b128 a[32:35], v22 offset:24576\n"
    "v_mfma_f32_16x16x32_bf16 v[100:103], a[56:59], a[36:39], v[100:103]\n"
    "s_add_u32 m0, m0, s81\n"
    "ds_read_b128 a[68:71], v22 offset:34816\n"
    "v_mfma_f32_16x16x32_bf16 v[120:123], a[20:23], a[4:7], v[120:123]\n"
    "v_mfma_f32_16x16x32_bf16 v[120:123], a[56:59], a[40:43], v[120:123]\n"
    "v_mfma_f32_16x16x32_bf16 v[140:143], a[20:23], a[8:11], v[140:143]\n"
    "buffer_load_dwordx4 v0, s[40:43], s60 offen offset:0 sc1 lds\n"
    "v_mfma_f32_16x16x32_bf16 v[140:143], a[56:59], a[44:47], v[140:143]\n"
    "v_mfma_f32_16x16x32_bf16 v[160:163], a[20:23], a[12:15], v[160:163]\n"
    "v_mfma_f32_16x16x32_bf16 v[160:163], a[56:59], a[48:51], v[160:163]\n"
    "s_waitcnt lgkmcnt(4)\n"
    "v_mfma_f32_16x16x32_bf16 v[104:107], a[24:27], a[0:3], v[104:107]\n"
    "buffer_load_dwordx4 v0, s[40:43], s60 offen offset:1024 sc1 lds\n"
    "v_mfma_f32_16x16x32_bf16 v[104:107], a[60:63], a[36:39], v[104:107]\n"
    "s_waitcnt lgkmcnt(0)\n"
    "v_mfma_f32_16x16x32_bf16 v[124:127], a[24:27], a[4:7], v[124:127]\n"
    "s_waitcnt vmcnt(20)\n"
    "v_xor_b32 v23, 0x14000, v23\n"
    "v_xor_b32 v22, 0x14000, v22\n"
    "v_mfma_f32_16x16x32_bf16 v[124:127], a[60:63], a[40:43], v[124:127]\n"
    "s_barrier\n"
    "v_mfma_f32_16x16x32_bf16 v[144:147], a[24:27], a[8:11], v[144:147]\n"
    "s_cmp_le_u32 s69, 0x4000\n"
    "ds_read_b128 a[144:147], v22 offset:0\n"
    "s_cselect_b32 s59, 10, 8\n"
    "s_lshl_b32 s59, s59, 11\n"
    "s_mul_i32 s60, s59, 40\n"
    "s_add_u32 s60, s60, s68\n"
    "s_add_u32 m0, s82, 0xd000\n"
    "v_mfma_f32_16x16x32_bf16 v[144:147], a[60:63], a[44:47], v[144:147]\n"
    "buffer_load_dwordx4 v0, s[40:43], s60 offen offset:0 sc1 lds\n"
    "v_mfma_f32_16x16x32_bf16 v[164:167], a[24:27], a[12:15], v[164:167]\n"
    "ds_read_b128 a[180:183], v22 offset:10240\n"
    "v_mfma_f32_16x16x32_bf16 v[164:167], a[60:63], a[48:51], v[164:167]\n"
    "ds_read_b128 a[128:131], v23 offset:0\n"
    "v_mfma_f32_16x16x32_bf16 v[108:111], a[28:31], a[0:3], v[108:111]\n"
    "ds_read_b128 a[164:167], v23 offset:1024\n"
    "v_mfma_f32_16x16x32_bf16 v[108:111], a[64:67], a[36:39], v[108:111]\n"
    "buffer_load_dwordx4 v0, s[40:43], s60 offen offset:1024 sc1 lds\n"
    "v_mfma_f32_16x16x32_bf16 v[128:131], a[28:31], a[4:7], v[128:131]\n"
    "ds_read_b128 a[132:135], v23 offset:2048\n"
    "v_mfma_f32_16x16x32_bf16 v[128:131], a[64:67], a[40:43], v[128:131]\n"
    "ds_read_b128 a[168:171], v23 offset:3072\n"
    "v_mfma_f32_16x16x32_bf16 v[148:151], a[28:31], a[8:11], v[148:151]\n"
    "ds_read_b128 a[136:139], v23 offset:4096\n"
    "v_mfma_f32_16x16x32_bf16 v[148:151], a[64:67], a[44:47], v[148:151]\n"
    "buffer_load_dwordx4 v0, s[40:43], s60 offen offset:2048 sc1 lds\n"
    "v_mfma_f32_16x16x32_bf16 v[168:171], a[28:31], a[12:15], v[168:171]\n"
    "ds_read_b128 a[172:175], v23 offset:5120\n"
    "v_mfma_f32_16x16x32_bf16 v[168:171], a[64:67], a[48:51], v[168:171]\n"
    "ds_read_b128 a[140:143], v23 offset:6144\n"
    "v_mfma_f32_16x16x32_bf16 v[112:115], a[32:35], a[0:3], v[112:115]\n"
    "ds_read_b128 a[176:179], v23 offset:7168\n"
    "v_mfma_f32_16x16x32_bf16 v[112:115], a[68:71], a[36:39], v[112:115]\n"
    "buffer_load_dwordx4 v0, s[40:43], s60 offen offset:3072 sc1 lds\n"
    "v_mfma_f32_16x16x32_bf16 v[132:135], a[32:35], a[4:7], v[132:135]\n"
    "s_addk_i32 s60, 0x1000\n"
    "ds_read_b128 a[148:151], v22 offset:1024\n"
    "v_mfma_f32_16x16x32_bf16 v[132:135], a[68:71], a[40:43], v[132:135]\n"
    "s_addk_i32 m0, 0x1000\n"
    "ds_read_b128 a[184:187], v22 offset:11264\n"
    "v_mfma_f32_16x16x32_bf16 v[152:155], a[32:35], a[8:11], v[152:155]\n"
    "v_mfma_f32_16x16x32_bf16 v[152:155], a[68:71], a[44:47], v[152:155]\n"
    "buffer_load_dwordx4 v0, s[40:43], s60 offen offset:0 sc1 lds\n"
    "v_mfma_f32_16x16x32_bf16 v[172:175], a[32:35], a[12:15], v[172:175]\n"
    "s_add_u32 s60, s73, s59\n"
    "v_mfma_f32_16x16x32_bf16 v[172:175], a[68:71], a[48:51], v[172:175]\n"
    "s_waitcnt lgkmcnt(3)\n"
    "v_mfma_f32_16x16x32_bf16 v[96:99], a[144:147], a[128:131], v[96:99]\n"
    "s_add_u32 m0, s70, 0x4000\n"
    "ds_read_b128 a[152:155], v22 offset:2048\n"
    "v_mfma_f32_16x16x32_bf16 v[96:99], a[180:183], a[164:167], v[96:99]\n"
    "ds_read_b128 a[188:191], v22 offset:12288\n"
    "v_mfma_f32_16x16x32_bf16 v[116:119], a[144:147], a[132:135], v[116:119]\n"
    "v_mfma_f32_16x16x32_bf16 v[116:119], a[180:183], a[168:171], v[116:119]\n"
    "buffer_load_dwordx4 v0, s[40:43], s60 offen offset:0 sc1 lds\n"
    "v_mfma_f32_16x16x32_bf16 v[136:139], a[144:147], a[136:139], v[136:139]\n"
    "ds_read_b128 a[156:159], v22 offset:3072\n"
    "v_mfma_f32_16x16x32_bf16 v[136:139], a[180:183], a[172:175], v[136:139]\n"
    "ds_read_b128 a[192:195], v22 offset:13312\n"
    "v_mfma_f32_16x16x32_bf16 v[156:159], a[144:147], a[140:143], v[156:159]\n"
    "s_waitcnt lgkmcnt(4)\n"
    "v_mfma_f32_16x16x32_bf16 v[156:159], a[180:183], a[176:179], v[156:159]\n"
    "buffer_load_dwordx4 v0, s[40:43], s60 offen offset:1024 sc1 lds\n"
    "v_mfma_f32_16x16x32_bf16 v[100:103], a[148:151], a[128:131], v[100:103]\n"
    "s_add_u32 s60, s60, s74\n"
    "ds_read_b128 a[160:163], v22 offset:4096\n"
    "v_mfma_f32_16x16x32_bf16 v[100:103], a[184:187], a[164:167], v[100:103]\n"
    "s_add_u32 m0, m0, s81\n"
    "ds_read_b128 a[196:199], v22 offset:14336\n"
    "v_mfma_f32_16x16x32_bf16 v[120:123], a[148:151], a[132:135], v[120:123]\n"
    "v_mfma_f32_16x16x32_bf16 v[120:123], a[184:187], a[168:171], v[120:123]\n"
    "v_mfma_f32_16x16x32_bf16 v[140:143], a[148:151], a[136:139], v[140:143]\n"
    "buffer_load_dwordx4 v0, s[40:43], s60 offen offset:0 sc1 lds\n"
    "v_mfma_f32_16x16x32_bf16 v[140:143], a[184:187], a[172:175], v[140:143]\n"
    "v_mfma_f32_16x16x32_bf16 v[160:163], a[148:151], a[140:143], v[160:163]\n"
    "v_mfma_f32_16x16x32_bf16 v[160:163], a[184:187], a[176:179], v[160:163]\n"
    "s_waitcnt lgkmcnt(4)\n"
    "v_mfma_f32_16x16x32_bf16 v[104:107], a[152:155], a[128:131], v[104:107]\n"
    "buffer_load_dwordx4 v0, s[40:43], s60 offen offset:1024 sc1 lds\n"
    "v_mfma_f32_16x16x32_bf16 v[104:107], a[188:191], a[164:167], v[104:107]\n"
    "s_waitcnt lgkmcnt(0)\n"
    "v_mfma_f32_16x16x32_bf16 v[124:127], a[152:155], a[132:135], v[124:127]\n"
    "s_waitcnt vmcnt(18)\n"
    "v_mfma_f32_16x16x32_bf16 v[124:127], a[188:191], a[168:171], v[124:127]\n"
    "s_barrier\n"
    "v_mfma_f32_16x16x32_bf16 v[144:147], a[152:155], a[136:139], v[144:147]\n"
    "s_addk_i32 s59, 0x800\n"
    "ds_read_b128 a[16:19], v22 offset:20480\n"
    "s_mul_i32 s60, s59, 40\n"
    "s_add_u32 s60, s60, s68\n"
    "s_add_u32 m0, s82, 0x1c000\n"
    "v_mfma_f32_16x16x32_bf16 v[144:147], a[188:191], a[172:175], v[144:147]\n"
    "buffer_load_dwordx4 v0, s[40:43], s60 offen offset:0 sc1 lds\n"
    "v_mfma_f32_16x16x32_bf16 v[164:167], a[152:155], a[140:143], v[164:167]\n"
    "s_mov_b32 s89, 0xffff0000\n"
    "ds_read_b128 a[52:55], v22 offset:30720\n"
    "v_mov_b32 v16, 0x3f80\n"
    "v_mfma_f32_16x16x32_bf16 v[164:167], a[188:191], a[176:179], v[164:167]\n"
    "ds_read_b128 a[0:3], v23 offset:16384\n"
    "v_mov_b32 v17, 1.0\n"
    "v_mfma_f32_16x16x32_bf16 v[108:111], a[156:159], a[128:131], v[108:111]\n"
    "ds_read_b128 a[36:39], v23 offset:17408\n"
    "v_and_b32 v18, 16, v12\n"
    "v_mfma_f32_16x16x32_bf16 v[108:111], a[192:195], a[164:167], v[108:111]\n"
    "buffer_load_dwordx4 v0, s[40:43], s60 offen offset:1024 sc1 lds\n"
    "v_mfma_f32_16x16x32_bf16 v[128:131], a[156:159], a[132:135], v[128:131]\n"
    "ds_read_b128 a[4:7], v23 offset:18432\n"
    "v_mul_u32_u24 v18, 3, v18\n"
    "v_mfma_f32_16x16x32_bf16 v[128:131], a[192:195], a[168:171], v[128:131]\n"
    "ds_read_b128 a[40:43], v23 offset:19456\n"
    "v_lshrrev_b32 v18, 1, v18\n"
    "v_mfma_f32_16x16x32_bf16 v[148:151], a[156:159], a[136:139], v[148:151]\n"
    "ds_read_b128 a[8:11], v23 offset:20480\n"
    "v_add_u32 v18, v18, v24\n"
    "v_mfma_f32_16x16x32_bf16 v[148:151], a[192:195], a[172:175], v[148:151]\n"
    "buffer_load_dwordx4 v0, s[40:43], s60 offen offset:2048 sc1 lds\n"
    "v_mfma_f32_16x16x32_bf16 v[168:171], a[156:159], a[140:143], v[168:171]\n"
    "ds_read_b128 a[44:47], v23 offset:21504\n"
    "v_accvgpr_read_b32 v28, a240\n"
    "v_mfma_f32_16x16x32_bf16 v[168:171], a[192:195], a[176:179], v[168:171]\n"
    "ds_read_b128 a[12:15], v23 offset:22528\n"
    "v_lshlrev_b32 v64, 16, v28\n"
    "v_mfma_f32_16x16x32_bf16 v[112:115], a[160:163], a[128:131], v[112:115]\n"
    "ds_read_b128 a[48:51], v23 offset:23552\n"
    "v_and_b32 v65, s89, v28\n"
    "v_mfma_f32_16x16x32_bf16 v[112:115], a[196:199], a[164:167], v[112:115]\n"
    "buffer_load_dwordx4 v0, s[40:43], s60 offen offset:3072 sc1 lds\n"
    "v_mfma_f32_16x16x32_bf16 v[132:135], a[160:163], a[132:135], v[132:135]\n"
    "s_addk_i32 s60, 0x1000\n"
    "ds_read_b128 a[20:23], v22 offset:21504\n"
    "v_accvgpr_read_b32 v28, a241\n"
    "v_mfma_f32_16x16x32_bf16 v[132:135], a[196:199], a[168:171], v[132:135]\n"
    "s_addk_i32 m0, 0x1000\n"
    "ds_read_b128 a[56:59], v22 offset:31744\n"
    "v_lshlrev_b32 v66, 16, v28\n"
    "v_mfma_f32_16x16x32_bf16 v[152:155], a[160:163], a[136:139], v[152:155]\n"
    "v_and_b32 v67, s89, v28\n"
    "v_accvgpr_read_b32 v28, a242\n"
    "v_mfma_f32_16x16x32_bf16 v[152:155], a[196:199], a[172:175], v[152:155]\n"
    "buffer_load_dwordx4 v0, s[40:43], s60 offen offset:0 sc1 lds\n"
    "v_mfma_f32_16x16x32_bf16 v[172:175], a[160:163], a[140:143], v[172:175]\n"
    "s_add_u32 s60, s73, s59\n"
    "v_lshlrev_b32 v68, 16, v28\n"
    "v_and_b32 v69, s89, v28\n"
    "v_mfma_f32_16x16x32_bf16 v[172:175], a[196:199], a[176:179], v[172:175]\n"
    "s_waitcnt lgkmcnt(3)\n"
    "v_accvgpr_read_b32 v28, a243\n"
    "v_lshlrev_b32 v70, 16, v28\n"
    "v_mfma_f32_16x16x32_bf16 v[96:99], a[16:19], a[0:3], v[96:99]\n"
    "s_add_u32 m0, s70, 0x14000\n"
    "ds_read_b128 a[24:27], v22 offset:22528\n"
    "v_and_b32 v71, s89, v28\n"
    "v_mfma_f32_16x16x32_bf16 v[96:99], a[52:55], a[36:39], v[96:99]\n"
    "ds_read_b128 a[60:63], v22 offset:32768\n"
    "v_accvgpr_read_b32 v28, a244\n"
    "v_mfma_f32_16x16x32_bf16 v[116:119], a[16:19], a[4:7], v[116:119]\n"
    "v_lshlrev_b32 v72, 16, v28\n"
    "v_and_b32 v73, s89, v28\n"
    "v_mfma_f32_16x16x32_bf16 v[116:119], a[52:55], a[40:43], v[116:119]\n"
    "buffer_load_dwordx4 v0, s[40:43], s60 offen offset:0 sc1 lds\n"
    "v_mfma_f32_16x16x32_bf16 v[136:139], a[16:19], a[8:11], v[136:139]\n"
    "ds_read_b128 a[28:31], v22 offset:23552\n"
    "v_accvgpr_read_b32 v28, a245\n"
    "v_mfma_f32_16x16x32_bf16 v[136:139], a[52:55], a[44:47], v[136:139]\n"
    "ds_read_b128 a[64:67], v22 offset:33792\n"
    "v_lshlrev_b32 v74, 16, v28\n"
    "v_mfma_f32_16x16x32_bf16 v[156:159], a[16:19], a[12:15], v[156:159]\n"
    "s_waitcnt lgkmcnt(4)\n"
    "v_and_b32 v75, s89, v28\n"
    "v_accvgpr_read_b32 v28, a246\n"
    "v_mfma_f32_16x16x32_bf16 v[156:159], a[52:55], a[48:51], v[156:159]\n"
    "buffer_load_dwordx4 v0, s[40:43], s60 offen offset:1024 sc1 lds\n"
    "v_mfma_f32_16x16x32_bf16 v[100:103], a[20:23], a[0:3], v[100:103]\n"
    "s_add_u32 s60, s60, s74\n"
    "ds_read_b128 a[32:35], v22 offset:24576\n"
    "v_lshlrev_b32 v76, 16, v28\n"
    "v_mfma_f32_16x16x32_bf16 v[100:103], a[56:59], a[36:39], v[100:103]\n"
    "s_add_u32 m0, m0, s81\n"
    "ds_read_b128 a[68:71], v22 offset:34816\n"
    "v_and_b32 v77, s89, v28\n"
    "v_mfma_f32_16x16x32_bf16 v[120:123], a[20:23], a[4:7], v[120:123]\n"
    "v_accvgpr_read_b32 v28, a247\n"
    "v_lshlrev_b32 v78, 16, v28\n"
    "v_mfma_f32_16x16x32_bf16 v[120:123], a[56:59], a[40:43], v[120:123]\n"
    "v_and_b32 v79, s89, v28\n"
    "v_accvgpr_read_b32 v28, a248\n"
    "v_mfma_f32_16x16x32_bf16 v[140:143], a[20:23], a[8:11], v[140:143]\n"
    "buffer_load_dwordx4 v0, s[40:43], s60 offen offset:0 sc1 lds\n"
    "v_mfma_f32_16x16x32_bf16 v[140:143], a[56:59], a[44:47], v[140:143]\n"
    "v_lshlrev_b32 v80, 16, v28\n"
    "v_and_b32 v81, s89, v28\n"
    "v_mfma_f32_16x16x32_bf16 v[160:163], a[20:23], a[12:15], v[160:163]\n"
    "v_accvgpr_read_b32 v28, a249\n"
    "v_lshlrev_b32 v82, 16, v28\n"
    "v_mfma_f32_16x16x32_bf16 v[160:163], a[56:59], a[48:51], v[160:163]\n"
    "s_waitcnt lgkmcnt(4)\n"
    "v_and_b32 v83, s89, v28\n"
    "v_mfma_f32_16x16x32_bf16 v[104:107], a[24:27], a[0:3], v[104:107]\n"
    "buffer_load_dwordx4 v0, s[40:43], s60 offen offset:1024 sc1 lds\n"
    "v_mfma_f32_16x16x32_bf16 v[104:107], a[60:63], a[36:39], v[104:107]\n"
    "s_waitcnt lgkmcnt(0)\n"
    "v_mfma_f32_16x16x32_bf16 v[124:127], a[24:27], a[4:7], v[124:127]\n"
    "s_waitcnt vmcnt(18)\n"
    "v_xor_b32 v23, 0x14000, v23\n"
    "v_xor_b32 v22, 0x14000, v22\n"
    "v_mfma_f32_16x16x32_bf16 v[124:127], a[60:63], a[40:43], v[124:127]\n"
    "s_barrier\n"
    "v_mfma_f32_16x16x32_bf16 v[144:147], a[24:27], a[8:11], v[144:147]\n"
    "s_cmp_le_u32 s69, 0x5000\n"
    "ds_read_b128 a[144:147], v22 offset:0\n"
    "s_cselect_b32 s59, 12, 10\n"
    "s_lshl_b32 s59, s59, 11\n"
    "s_mul_i32 s60, s59, 40\n"
    "s_add_u32 s60, s60, s68\n"
    "s_add_u32 m0, s82, 0x21000\n"
    "v_mfma_f32_16x16x32_bf16 v[144:147], a[60:63], a[44:47], v[144:147]\n"
    "buffer_load_dwordx4 v0, s[40:43], s60 offen offset:0 sc1 lds\n"
    "v_mfma_f32_16x16x32_bf16 v[164:167], a[24:27], a[12:15], v[164:167]\n"
    "s_mov_b64 s[44:45], %[out]\n"
    "ds_read_b128 a[180:183], v22 offset:10240\n"
    "v_mfma_f32_16x16x32_bf16 v[164:167], a[60:63], a[48:51], v[164:167]\n"
    "s_mov_b32 s46, 0x280000\n"
    "ds_read_b128 a[128:131], v23 offset:0\n"
    "v_mfma_f32_16x16x32_bf16 v[108:111], a[28:31], a[0:3], v[108:111]\n"
    "ds_read_b128 a[164:167], v23 offset:1024\n"
    "v_mfma_f32_16x16x32_bf16 v[108:111], a[64:67], a[36:39], v[108:111]\n"
    "buffer_load_dwordx4 v0, s[40:43], s60 offen offset:1024 sc1 lds\n"
    "v_mfma_f32_16x16x32_bf16 v[128:131], a[28:31], a[4:7], v[128:131]\n"
    "ds_read_b128 a[132:135], v23 offset:2048\n"
    "v_mfma_f32_16x16x32_bf16 v[128:131], a[64:67], a[40:43], v[128:131]\n"
    "ds_read_b128 a[168:171], v23 offset:3072\n"
    "v_mfma_f32_16x16x32_bf16 v[148:151], a[28:31], a[8:11], v[148:151]\n"
    "ds_read_b128 a[136:139], v23 offset:4096\n"
    "v_mfma_f32_16x16x32_bf16 v[148:151], a[64:67], a[44:47], v[148:151]\n"
    "buffer_load_dwordx4 v0, s[40:43], s60 offen offset:2048 sc1 lds\n"
    "v_mfma_f32_16x16x32_bf16 v[168:171], a[28:31], a[12:15], v[168:171]\n"
    "ds_read_b128 a[172:175], v23 offset:5120\n"
    "v_mfma_f32_16x16x32_bf16 v[168:171], a[64:67], a[48:51], v[168:171]\n"
    "ds_read_b128 a[140:143], v23 offset:6144\n"
    "v_mfma_f32_16x16x32_bf16 v[112:115], a[32:35], a[0:3], v[112:115]\n"
    "ds_read_b128 a[176:179], v23 offset:7168\n"
    "v_mfma_f32_16x16x32_bf16 v[112:115], a[68:71], a[36:39], v[112:115]\n"
    "buffer_store_dword v13, v25, s[40:43], 0 offen offset:0 sc1\n"
    "v_mfma_f32_16x16x32_bf16 v[132:135], a[32:35], a[4:7], v[132:135]\n"
    "ds_read_b128 a[148:151], v22 offset:1024\n"
    "v_mfma_f32_16x16x32_bf16 v[132:135], a[68:71], a[40:43], v[132:135]\n"
    "ds_read_b128 a[184:187], v22 offset:11264\n"
    "v_mfma_f32_16x16x32_bf16 v[152:155], a[32:35], a[8:11], v[152:155]\n"
    "buffer_load_dwordx4 v0, s[40:43], s60 offen offset:3072 sc1 lds\n"
    "s_addk_i32 s60, 0x1000\n"
    "s_addk_i32 m0, 0x1000\n"
    "v_mfma_f32_16x16x32_bf16 v[152:155], a[68:71], a[44:47], v[152:155]\n"
    "buffer_load_dwordx4 v0, s[40:43], s60 offen offset:0 sc1 lds\n"
    "v_mfma_f32_16x16x32_bf16 v[172:175], a[32:35], a[12:15], v[172:175]\n"
    "s_add_u32 s60, s73, s59\n"
    "v_mfma_f32_16x16x32_bf16 v[172:175], a[68:71], a[48:51], v[172:175]\n"
    "s_waitcnt lgkmcnt(3)\n"
    "v_mfma_f32_16x16x32_bf16 v[96:99], a[144:147], a[128:131], v[96:99]\n"
    "s_add_u32 m0, s70, 0x18000\n"
    "ds_read_b128 a[152:155], v22 offset:2048\n"
    "v_mfma_f32_16x16x32_bf16 v[96:99], a[180:183], a[164:167], v[96:99]\n"
    "ds_read_b128 a[188:191], v22 offset:12288\n"
    "v_mfma_f32_16x16x32_bf16 v[116:119], a[144:147], a[132:135], v[116:119]\n"
    "v_mfma_f32_16x16x32_bf16 v[116:119], a[180:183], a[168:171], v[116:119]\n"
    "buffer_load_dwordx4 v0, s[40:43], s60 offen offset:0 sc1 lds\n"
    "v_mfma_f32_16x16x32_bf16 v[136:139], a[144:147], a[136:139], v[136:139]\n"
    "ds_read_b128 a[156:159], v22 offset:3072\n"
    "v_mfma_f32_16x16x32_bf16 v[136:139], a[180:183], a[172:175], v[136:139]\n"
    "ds_read_b128 a[192:195], v22 offset:13312\n"
    "v_mfma_f32_16x16x32_bf16 v[156:159], a[144:147], a[140:143], v[156:159]\n"
    "s_waitcnt lgkmcnt(4)\n"
    "v_mfma_f32_16x16x32_bf16 v[156:159], a[180:183], a[176:179], v[156:159]\n"
    "buffer_load_dwordx4 v0, s[40:43], s60 offen offset:1024 sc1 lds\n"
    "v_mfma_f32_16x16x32_bf16 v[100:103], a[148:151], a[128:131], v[100:103]\n"
    "s_add_u32 s60, s60, s74\n"
    "ds_read_b128 a[160:163], v22 offset:4096\n"
    "v_mfma_f32_16x16x32_bf16 v[100:103], a[184:187], a[164:167], v[100:103]\n"
    "s_add_u32 m0, m0, s81\n"
    "ds_read_b128 a[196:199], v22 offset:14336\n"
    "v_mfma_f32_16x16x32_bf16 v[120:123], a[148:151], a[132:135], v[120:123]\n"
    "v_mfma_f32_16x16x32_bf16 v[120:123], a[184:187], a[168:171], v[120:123]\n"
    "v_mfma_f32_16x16x32_bf16 v[140:143], a[148:151], a[136:139], v[140:143]\n"
    "buffer_load_dwordx4 v0, s[40:43], s60 offen offset:0 sc1 lds\n"
    "v_mfma_f32_16x16x32_bf16 v[140:143], a[184:187], a[172:175], v[140:143]\n"
    "v_mfma_f32_16x16x32_bf16 v[160:163], a[148:151], a[140:143], v[160:163]\n"
    "v_mfma_f32_16x16x32_bf16 v[160:163], a[184:187], a[176:179], v[160:163]\n"
    "s_waitcnt lgkmcnt(4)\n"
    "v_mfma_f32_16x16x32_bf16 v[104:107], a[152:155], a[128:131], v[104:107]\n"
    "buffer_load_dwordx4 v0, s[40:43], s60 offen offset:1024 sc1 lds\n"
    "v_mfma_f32_16x16x32_bf16 v[104:107], a[188:191], a[164:167], v[104:107]\n"
    "s_waitcnt lgkmcnt(0)\n"
    "v_mfma_f32_16x16x32_bf16 v[124:127], a[152:155], a[132:135], v[124:127]\n"
    "s_waitcnt vmcnt(19)\n"
    "v_mfma_f32_16x16x32_bf16 v[124:127], a[188:191], a[168:171], v[124:127]\n"
    "s_barrier\n"
    "v_mfma_f32_16x16x32_bf16 v[144:147], a[152:155], a[136:139], v[144:147]\n"
    "s_addk_i32 s59, 0x800\n"
    "ds_read_b128 a[16:19], v22 offset:20480\n"
    "s_mul_i32 s60, s59, 40\n"
    "s_add_u32 s60, s60, s68\n"
    "s_add_u32 m0, s82, 0x8000\n"
    "v_mfma_f32_16x16x32_bf16 v[144:147], a[188:191], a[172:175], v[144:147]\n"
    "buffer_load_dwordx4 v0, s[40:43], s60 offen offset:0 sc1 lds\n"
    "v_mfma_f32_16x16x32_bf16 v[164:167], a[152:155], a[140:143], v[164:167]\n"
    "ds_read_b128 a[52:55], v22 offset:30720\n"
    "v_mfma_f32_16x16x32_bf16 v[164:167], a[188:191], a[176:179], v[164:167]\n"
    "ds_read_b128 a[0:3], v23 offset:16384\n"
    "v_mfma_f32_16x16x32_bf16 v[108:111], a[156:159], a[128:131], v[108:111]\n"
    "ds_read_b128 a[36:39], v23 offset:17408\n"
    "v_mfma_f32_16x16x32_bf16 v[108:111], a[192:195], a[164:167], v[108:111]\n"
    "buffer_load_dwordx4 v0, s[40:43], s60 offen offset:1024 sc1 lds\n"
    "v_mfma_f32_16x16x32_bf16 v[128:131], a[156:159], a[132:135], v[128:131]\n"
    "ds_read_b128 a[4:7], v23 offset:18432\n"
    "v_mfma_f32_16x16x32_bf16 v[128:131], a[192:195], a[168:171], v[128:131]\n"
    "ds_read_b128 a[40:43], v23 offset:19456\n"
    "v_mfma_f32_16x16x32_bf16 v[148:151], a[156:159], a[136:139], v[148:151]\n"
    "ds_read_b128 a[8:11], v23 offset:20480\n"
    "v_mfma_f32_16x16x32_bf16 v[148:151], a[192:195], a[172:175], v[148:151]\n"
    "buffer_load_dwordx4 v0, s[40:43], s60 offen offset:2048 sc1 lds\n"
    "v_mfma_f32_16x16x32_bf16 v[168:171], a[156:159], a[140:143], v[168:171]\n"
    "ds_read_b128 a[44:47], v23 offset:21504\n"
    "v_mfma_f32_16x16x32_bf16 v[168:171], a[192:195], a[176:179], v[168:171]\n"
    "ds_read_b128 a[12:15], v23 offset:22528\n"
    "v_mfma_f32_16x16x32_bf16 v[112:115], a[160:163], a[128:131], v[112:115]\n"
    "ds_read_b128 a[48:51], v23 offset:23552\n"
    "v_mfma_f32_16x16x32_bf16 v[112:115], a[196:199], a[164:167], v[112:115]\n"
    "buffer_load_dwordx4 v0, s[40:43], s60 offen offset:3072 sc1 lds\n"
    "v_mfma_f32_16x16x32_bf16 v[132:135], a[160:163], a[132:135], v[132:135]\n"
    "s_addk_i32 s60, 0x1000\n"
    "ds_read_b128 a[20:23], v22 offset:21504\n"
    "v_mfma_f32_16x16x32_bf16 v[132:135], a[196:199], a[168:171], v[132:135]\n"
    "s_addk_i32 m0, 0x1000\n"
    "ds_read_b128 a[56:59], v22 offset:31744\n"
    "v_mfma_f32_16x16x32_bf16 v[152:155], a[160:163], a[136:139], v[152:155]\n"
    "v_mfma_f32_16x16x32_bf16 v[152:155], a[196:199], a[172:175], v[152:155]\n"
    "buffer_load_dwordx4 v0, s[40:43], s60 offen offset:0 sc1 lds\n"
    "v_mfma_f32_16x16x32_bf16 v[172:175], a[160:163], a[140:143], v[172:175]\n"
    "s_add_u32 s60, s73, s59\n"
    "v_mfma_f32_16x16x32_bf16 v[172:175], a[196:199], a[176:179], v[172:175]\n"
    "s_waitcnt lgkmcnt(3)\n"
    "v_mfma_f32_16x16x32_bf16 v[96:99], a[16:19], a[0:3], v[96:99]\n"
    "s_add_u32 m0, s70, 0\n"
    "ds_read_b128 a[24:27], v22 offset:22528\n"
    "v_mfma_f32_16x16x32_bf16 v[96:99], a[52:55], a[36:39], v[96:99]\n"
    "ds_read_b128 a[60:63], v22 offset:32768\n"
    "v_mfma_f32_16x16x32_bf16 v[116:119], a[16:19], a[4:7], v[116:119]\n"
    "v_mfma_f32_16x16x32_bf16 v[116:119], a[52:55], a[40:43], v[116:119]\n"
    "buffer_load_dwordx4 v0, s[40:43], s60 offen offset:0 sc1 lds\n"
    "v_mfma_f32_16x16x32_bf16 v[136:139], a[16:19], a[8:11], v[136:139]\n"
    "ds_read_b128 a[28:31], v22 offset:23552\n"
    "v_mfma_f32_16x16x32_bf16 v[136:139], a[52:55], a[44:47], v[136:139]\n"
    "ds_read_b128 a[64:67], v22 offset:33792\n"
    "v_mfma_f32_16x16x32_bf16 v[156:159], a[16:19], a[12:15], v[156:159]\n"
    "s_waitcnt lgkmcnt(4)\n"
    "v_mfma_f32_16x16x32_bf16 v[156:159], a[52:55], a[48:51], v[156:159]\n"
    "buffer_load_dwordx4 v0, s[40:43], s60 offen offset:1024 sc1 lds\n"
    "v_mfma_f32_16x16x32_bf16 v[100:103], a[20:23], a[0:3], v[100:103]\n"
    "s_add_u32 s60, s60, s74\n"
    "ds_read_b128 a[32:35], v22 offset:24576\n"
    "v_mfma_f32_16x16x32_bf16 v[100:103], a[56:59], a[36:39], v[100:103]\n"
    "s_add_u32 m0, m0, s81\n"
    "ds_read_b128 a[68:71], v22 offset:34816\n"
    "v_mfma_f32_16x16x32_bf16 v[120:123], a[20:23], a[4:7], v[120:123]\n"
    "v_mfma_f32_16x16x32_bf16 v[120:123], a[56:59], a[40:43], v[120:123]\n"
    "v_mfma_f32_16x16x32_bf16 v[140:143], a[20:23], a[8:11], v[140:143]\n"
    "buffer_load_dwordx4 v0, s[40:43], s60 offen offset:0 sc1 lds\n"
    "v_mfma_f32_16x16x32_bf16 v[140:143], a[56:59], a[44:47], v[140:143]\n"
    "v_mfma_f32_16x16x32_bf16 v[160:163], a[20:23], a[12:15], v[160:163]\n"
    "v_mfma_f32_16x16x32_bf16 v[160:163], a[56:59], a[48:51], v[160:163]\n"
    "s_waitcnt lgkmcnt(4)\n"
    "v_mfma_f32_16x16x32_bf16 v[104:107], a[24:27], a[0:3], v[104:107]\n"
    "buffer_load_dwordx4 v0, s[40:43], s60 offen offset:1024 sc1 lds\n"
    "v_mfma_f32_16x16x32_bf16 v[104:107], a[60:63], a[36:39], v[104:107]\n"
    "s_waitcnt lgkmcnt(0)\n"
    "v_mfma_f32_16x16x32_bf16 v[124:127], a[24:27], a[4:7], v[124:127]\n"
    "s_waitcnt vmcnt(19)\n"
    "v_xor_b32 v23, 0x14000, v23\n"
    "v_xor_b32 v22, 0x14000, v22\n"
    "v_mfma_f32_16x16x32_bf16 v[124:127], a[60:63], a[40:43], v[124:127]\n"
    "s_barrier\n"
    "v_mfma_f32_16x16x32_bf16 v[144:147], a[24:27], a[8:11], v[144:147]\n"
    "s_cmp_le_u32 s69, 0x6000\n"
    "ds_read_b128 a[144:147], v22 offset:0\n"
    "s_cselect_b32 s59, 14, 12\n"
    "s_lshl_b32 s59, s59, 11\n"
    "s_mul_i32 s60, s59, 40\n"
    "s_add_u32 s60, s60, s68\n"
    "s_add_u32 m0, s82, 0xd000\n"
    "v_mfma_f32_16x16x32_bf16 v[144:147], a[60:63], a[44:47], v[144:147]\n"
    "buffer_load_dwordx4 v0, s[40:43], s60 offen offset:0 sc1 lds\n"
    "v_mfma_f32_16x16x32_bf16 v[164:167], a[24:27], a[12:15], v[164:167]\n"
    "ds_read_b128 a[180:183], v22 offset:10240\n"
    "v_mfma_f32_16x16x32_bf16 v[164:167], a[60:63], a[48:51], v[164:167]\n"
    "ds_read_b128 a[128:131], v23 offset:0\n"
    "v_mfma_f32_16x16x32_bf16 v[108:111], a[28:31], a[0:3], v[108:111]\n"
    "ds_read_b128 a[164:167], v23 offset:1024\n"
    "v_mfma_f32_16x16x32_bf16 v[108:111], a[64:67], a[36:39], v[108:111]\n"
    "buffer_load_dwordx4 v0, s[40:43], s60 offen offset:1024 sc1 lds\n"
    "v_mfma_f32_16x16x32_bf16 v[128:131], a[28:31], a[4:7], v[128:131]\n"
    "ds_read_b128 a[132:135], v23 offset:2048\n"
    "v_mfma_f32_16x16x32_bf16 v[128:131], a[64:67], a[40:43], v[128:131]\n"
    "ds_read_b128 a[168:171], v23 offset:3072\n"
    "v_mfma_f32_16x16x32_bf16 v[148:151], a[28:31], a[8:11], v[148:151]\n"
    "ds_read_b128 a[136:139], v23 offset:4096\n"
    "v_mfma_f32_16x16x32_bf16 v[148:151], a[64:67], a[44:47], v[148:151]\n"
    "buffer_load_dwordx4 v0, s[40:43], s60 offen offset:2048 sc1 lds\n"
    "v_mfma_f32_16x16x32_bf16 v[168:171], a[28:31], a[12:15], v[168:171]\n"
    "ds_read_b128 a[172:175], v23 offset:5120\n"
    "v_mfma_f32_16x16x32_bf16 v[168:171], a[64:67], a[48:51], v[168:171]\n"
    "ds_read_b128 a[140:143], v23 offset:6144\n"
    "v_mfma_f32_16x16x32_bf16 v[112:115], a[32:35], a[0:3], v[112:115]\n"
    "ds_read_b128 a[176:179], v23 offset:7168\n"
    "v_mfma_f32_16x16x32_bf16 v[112:115], a[68:71], a[36:39], v[112:115]\n"
    "buffer_load_dwordx4 v0, s[40:43], s60 offen offset:3072 sc1 lds\n"
    "v_mfma_f32_16x16x32_bf16 v[132:135], a[32:35], a[4:7], v[132:135]\n"
    "s_addk_i32 s60, 0x1000\n"
    "ds_read_b128 a[148:151], v22 offset:1024\n"
    "v_mfma_f32_16x16x32_bf16 v[132:135], a[68:71], a[40:43], v[132:135]\n"
    "s_addk_i32 m0, 0x1000\n"
    "ds_read_b128 a[184:187], v22 offset:11264\n"
    "v_mfma_f32_16x16x32_bf16 v[152:155], a[32:35], a[8:11], v[152:155]\n"
    "v_mfma_f32_16x16x32_bf16 v[152:155], a[68:71], a[44:47], v[152:155]\n"
    "buffer_load_dwordx4 v0, s[40:43], s60 offen offset:0 sc1 lds\n"
    "v_mfma_f32_16x16x32_bf16 v[172:175], a[32:35], a[12:15], v[172:175]\n"
    "s_add_u32 s60, s73, s59\n"
    "v_mfma_f32_16x16x32_bf16 v[172:175], a[68:71], a[48:51], v[172:175]\n"
    "s_waitcnt lgkmcnt(3)\n"
    "v_mfma_f32_16x16x32_bf16 v[96:99], a[144:147], a[128:131], v[96:99]\n"
    "s_add_u32 m0, s70, 0x4000\n"
    "ds_read_b128 a[152:155], v22 offset:2048\n"
    "v_mfma_f32_16x16x32_bf16 v[96:99], a[180:183], a[164:167], v[96:99]\n"
    "ds_read_b128 a[188:191], v22 offset:12288\n"
    "v_mfma_f32_16x16x32_bf16 v[116:119], a[144:147], a[132:135], v[116:119]\n"
    "v_mfma_f32_16x16x32_bf16 v[116:119], a[180:183], a[168:171], v[116:119]\n"
    "buffer_load_dwordx4 v0, s[40:43], s60 offen offset:0 sc1 lds\n"
    "v_mfma_f32_16x16x32_bf16 v[136:139], a[144:147], a[136:139], v[136:139]\n"
    "ds_read_b128 a[156:159], v22 offset:3072\n"
    "v_mfma_f32_16x16x32_bf16 v[136:139], a[180:183], a[172:175], v[136:139]\n"
    "ds_read_b128 a[192:195], v22 offset:13312\n"
    "v_mfma_f32_16x16x32_bf16 v[156:159], a[144:147], a[140:143], v[156:159]\n"
    "s_waitcnt lgkmcnt(4)\n"
    "v_mfma_f32_16x16x32_bf16 v[156:159], a[180:183], a[176:179], v[156:159]\n"
    "buffer_load_dwordx4 v0, s[40:43], s60 offen offset:1024 sc1 lds\n"
    "v_mfma_f32_16x16x32_bf16 v[100:103], a[148:151], a[128:131], v[100:103]\n"
    "s_add_u32 s60, s60, s74\n"
    "ds_read_b128 a[160:163], v22 offset:4096\n"
    "v_mfma_f32_16x16x32_bf16 v[100:103], a[184:187], a[164:167], v[100:103]\n"
    "s_add_u32 m0, m0, s81\n"
    "ds_read_b128 a[196:199], v22 offset:14336\n"
    "v_mfma_f32_16x16x32_bf16 v[120:123], a[148:151], a[132:135], v[120:123]\n"
    "v_mfma_f32_16x16x32_bf16 v[120:123], a[184:187], a[168:171], v[120:123]\n"
    "v_mfma_f32_16x16x32_bf16 v[140:143], a[148:151], a[136:139], v[140:143]\n"
    "buffer_load_dwordx4 v0, s[40:43], s60 offen offset:0 sc1 lds\n"
    "v_mfma_f32_16x16x32_bf16 v[140:143], a[184:187], a[172:175], v[140:143]\n"
    "v_mfma_f32_16x16x32_bf16 v[160:163], a[148:151], a[140:143], v[160:163]\n"
    "v_mfma_f32_16x16x32_bf16 v[160:163], a[184:187], a[176:179], v[160:163]\n"
    "s_waitcnt lgkmcnt(4)\n"
    "v_mfma_f32_16x16x32_bf16 v[104:107], a[152:155], a[128:131], v[104:107]\n"
    "buffer_load_dwordx4 v0, s[40:43], s60 offen offset:1024 sc1 lds\n"
    "v_mfma_f32_16x16x32_bf16 v[104:107], a[188:191], a[164:167], v[104:107]\n"
    "s_waitcnt lgkmcnt(0)\n"
    "v_mfma_f32_16x16x32_bf16 v[124:127], a[152:155], a[132:135], v[124:127]\n"
    "s_waitcnt vmcnt(18)\n"
    "v_mfma_f32_16x16x32_bf16 v[124:127], a[188:191], a[168:171], v[124:127]\n"
    "s_barrier\n"
    "v_mfma_f32_16x16x32_bf16 v[144:147], a[152:155], a[136:139], v[144:147]\n"
    "s_addk_i32 s59, 0x800\n"
    "ds_read_b128 a[16:19], v22 offset:20480\n"
    "s_mul_i32 s60, s59, 40\n"
    "s_add_u32 s60, s60, s68\n"
    "s_add_u32 m0, s82, 0x1c000\n"
    "v_mfma_f32_16x16x32_bf16 v[144:147], a[188:191], a[172:175], v[144:147]\n"
    "buffer_load_dwordx4 v0, s[40:43], s60 offen offset:0 sc1 lds\n"
    "v_mfma_f32_16x16x32_bf16 v[164:167], a[152:155], a[140:143], v[164:167]\n"
    "ds_read_b128 a[52:55], v22 offset:30720\n"
    "v_mfma_f32_16x16x32_bf16 v[164:167], a[188:191], a[176:179], v[164:167]\n"
    "ds_read_b128 a[0:3], v23 offset:16384\n"
    "v_mfma_f32_16x16x32_bf16 v[108:111], a[156:159], a[128:131], v[108:111]\n"
    "ds_read_b128 a[36:39], v23 offset:17408\n"
    "v_mfma_f32_16x16x32_bf16 v[108:111], a[192:195], a[164:167], v[108:111]\n"
    "buffer_load_dwordx4 v0, s[40:43], s60 offen offset:1024 sc1 lds\n"
    "v_mfma_f32_16x16x32_bf16 v[128:131], a[156:159], a[132:135], v[128:131]\n"
    "ds_read_b128 a[4:7], v23 offset:18432\n"
    "v_mfma_f32_16x16x32_bf16 v[128:131], a[192:195], a[168:171], v[128:131]\n"
    "ds_read_b128 a[40:43], v23 offset:19456\n"
    "v_mfma_f32_16x16x32_bf16 v[148:151], a[156:159], a[136:139], v[148:151]\n"
    "ds_read_b128 a[8:11], v23 offset:20480\n"
    "v_mfma_f32_16x16x32_bf16 v[148:151], a[192:195], a[172:175], v[148:151]\n"
    "buffer_load_dwordx4 v0, s[40:43], s60 offen offset:2048 sc1 lds\n"
    "v_mfma_f32_16x16x32_bf16 v[168:171], a[156:159], a[140:143], v[168:171]\n"
    "ds_read_b128 a[44:47], v23 offset:21504\n"
    "v_mfma_f32_16x16x32_bf16 v[168:171], a[192:195], a[176:179], v[168:171]\n"
    "ds_read_b128 a[12:15], v23 offset:22528\n"
    "v_mfma_f32_16x16x32_bf16 v[112:115], a[160:163], a[128:131], v[112:115]\n"
    "ds_read_b128 a[48:51], v23 offset:23552\n"
    "v_mfma_f32_16x16x32_bf16 v[112:115], a[196:199], a[164:167], v[112:115]\n"
    "buffer_load_dwordx4 v0, s[40:43], s60 offen offset:3072 sc1 lds\n"
    "v_mfma_f32_16x16x32_bf16 v[132:135], a[160:163], a[132:135], v[132:135]\n"
    "s_addk_i32 s60, 0x1000\n"
    "ds_read_b128 a[20:23], v22 offset:21504\n"
    "v_mfma_f32_16x16x32_bf16 v[132:135], a[196:199], a[168:171], v[132:135]\n"
    "s_addk_i32 m0, 0x1000\n"
    "ds_read_b128 a[56:59], v22 offset:31744\n"
    "v_mfma_f32_16x16x32_bf16 v[152:155], a[160:163], a[136:139], v[152:155]\n"
    "v_mfma_f32_16x16x32_bf16 v[152:155], a[196:199], a[172:175], v[152:155]\n"
    "buffer_load_dwordx4 v0, s[40:43], s60 offen offset:0 sc1 lds\n"
    "v_mfma_f32_16x16x32_bf16 v[172:175], a[160:163], a[140:143], v[172:175]\n"
    "s_add_u32 s60, s73, s59\n"
    "v_mfma_f32_16x16x32_bf16 v[172:175], a[196:199], a[176:179], v[172:175]\n"
    "s_waitcnt lgkmcnt(3)\n"
    "v_mfma_f32_16x16x32_bf16 v[96:99], a[16:19], a[0:3], v[96:99]\n"
    "s_add_u32 m0, s70, 0x14000\n"
    "ds_read_b128 a[24:27], v22 offset:22528\n"
    "v_mfma_f32_16x16x32_bf16 v[96:99], a[52:55], a[36:39], v[96:99]\n"
    "ds_read_b128 a[60:63], v22 offset:32768\n"
    "v_mfma_f32_16x16x32_bf16 v[116:119], a[16:19], a[4:7], v[116:119]\n"
    "v_mfma_f32_16x16x32_bf16 v[116:119], a[52:55], a[40:43], v[116:119]\n"
    "buffer_load_dwordx4 v0, s[40:43], s60 offen offset:0 sc1 lds\n"
    "v_mfma_f32_16x16x32_bf16 v[136:139], a[16:19], a[8:11], v[136:139]\n"
    "ds_read_b128 a[28:31], v22 offset:23552\n"
    "v_mfma_f32_16x16x32_bf16 v[136:139], a[52:55], a[44:47], v[136:139]\n"
    "ds_read_b128 a[64:67], v22 offset:33792\n"
    "v_mfma_f32_16x16x32_bf16 v[156:159], a[16:19], a[12:15], v[156:159]\n"
    "s_waitcnt lgkmcnt(4)\n"
    "v_mfma_f32_16x16x32_bf16 v[156:159], a[52:55], a[48:51], v[156:159]\n"
    "buffer_load_dwordx4 v0, s[40:43], s60 offen offset:1024 sc1 lds\n"
    "v_mfma_f32_16x16x32_bf16 v[100:103], a[20:23], a[0:3], v[100:103]\n"
    "s_add_u32 s60, s60, s74\n"
    "ds_read_b128 a[32:35], v22 offset:24576\n"
    "v_mfma_f32_16x16x32_bf16 v[100:103], a[56:59], a[36:39], v[100:103]\n"
    "s_add_u32 m0, m0, s81\n"
    "ds_read_b128 a[68:71], v22 offset:34816\n"
    "v_mfma_f32_16x16x32_bf16 v[120:123], a[20:23], a[4:7], v[120:123]\n"
    "v_mfma_f32_16x16x32_bf16 v[120:123], a[56:59], a[40:43], v[120:123]\n"
    "v_mfma_f32_16x16x32_bf16 v[140:143], a[20:23], a[8:11], v[140:143]\n"
    "buffer_load_dwordx4 v0, s[40:43], s60 offen offset:0 sc1 lds\n"
    "v_mfma_f32_16x16x32_bf16 v[140:143], a[56:59], a[44:47], v[140:143]\n"
    "v_mfma_f32_16x16x32_bf16 v[160:163], a[20:23], a[12:15], v[160:163]\n"
    "v_mfma_f32_16x16x32_bf16 v[160:163], a[56:59], a[48:51], v[160:163]\n"
    "s_waitcnt lgkmcnt(4)\n"
    "v_mfma_f32_16x16x32_bf16 v[104:107], a[24:27], a[0:3], v[104:107]\n"
    "buffer_load_dwordx4 v0, s[40:43], s60 offen offset:1024 sc1 lds\n"
    "v_mfma_f32_16x16x32_bf16 v[104:107], a[60:63], a[36:39], v[104:107]\n"
    "s_waitcnt lgkmcnt(0)\n"
    "v_mfma_f32_16x16x32_bf16 v[124:127], a[24:27], a[4:7], v[124:127]\n"
    "s_waitcnt vmcnt(18)\n"
    "v_xor_b32 v23, 0x14000, v23\n"
    "v_xor_b32 v22, 0x14000, v22\n"
    "v_mfma_f32_16x16x32_bf16 v[124:127], a[60:63], a[40:43], v[124:127]\n"
    "s_barrier\n"
    "v_mfma_f32_16x16x32_bf16 v[144:147], a[24:27], a[8:11], v[144:147]\n"
    "ds_read_b128 a[144:147], v22 offset:0\n"
    "v_mfma_f32_16x16x32_bf16 v[144:147], a[60:63], a[44:47], v[144:147]\n"
    "v_mfma_f32_16x16x32_bf16 v[164:167], a[24:27], a[12:15], v[164:167]\n"
    "ds_read_b128 a[180:183], v22 offset:10240\n"
    "v_mfma_f32_16x16x32_bf16 v[164:167], a[60:63], a[48:51], v[164:167]\n"
    "ds_read_b128 a[128:131], v23 offset:0\n"
    "v_mfma_f32_16x16x32_bf16 v[108:111], a[28:31], a[0:3], v[108:111]\n"
    "ds_read_b128 a[164:167], v23 offset:1024\n"
    "v_mfma_f32_16x16x32_bf16 v[108:111], a[64:67], a[36:39], v[108:111]\n"
    "buffer_load_dword v1, v10, s[40:43], s34 offen offset:0 sc1\n"
    "v_mfma_f32_16x16x32_bf16 v[128:131], a[28:31], a[4:7], v[128:131]\n"
    "ds_read_b128 a[132:135], v23 offset:2048\n"
    "v_mfma_f32_16x16x32_bf16 v[128:131], a[64:67], a[40:43], v[128:131]\n"
    "ds_read_b128 a[168:171], v23 offset:3072\n"
    "v_mfma_f32_16x16x32_bf16 v[148:151], a[28:31], a[8:11], v[148:151]\n"
    "ds_read_b128 a[136:139], v23 offset:4096\n"
    "v_mfma_f32_16x16x32_bf16 v[148:151], a[64:67], a[44:47], v[148:151]\n"
    "v_mfma_f32_16x16x32_bf16 v[168:171], a[28:31], a[12:15], v[168:171]\n"
    "ds_read_b128 a[172:175], v23 offset:5120\n"
    "v_mfma_f32_16x16x32_bf16 v[168:171], a[64:67], a[48:51], v[168:171]\n"
    "ds_read_b128 a[140:143], v23 offset:6144\n"
    "v_mfma_f32_16x16x32_bf16 v[112:115], a[32:35], a[0:3], v[112:115]\n"
    "ds_read_b128 a[176:179], v23 offset:7168\n"
    "v_mfma_f32_16x16x32_bf16 v[112:115], a[68:71], a[36:39], v[112:115]\n"
    "v_mfma_f32_16x16x32_bf16 v[132:135], a[32:35], a[4:7], v[132:135]\n"
    "ds_read_b128 a[148:151], v22 offset:1024\n"
    "v_mfma_f32_16x16x32_bf16 v[132:135], a[68:71], a[40:43], v[132:135]\n"
    "ds_read_b128 a[184:187], v22 offset:11264\n"
    "v_mfma_f32_16x16x32_bf16 v[152:155], a[32:35], a[8:11], v[152:155]\n"
    "v_mfma_f32_16x16x32_bf16 v[152:155], a[68:71], a[44:47], v[152:155]\n"
    "v_mfma_f32_16x16x32_bf16 v[172:175], a[32:35], a[12:15], v[172:175]\n"
    "v_mfma_f32_16x16x32_bf16 v[172:175], a[68:71], a[48:51], v[172:175]\n"
    "s_waitcnt lgkmcnt(3)\n"
    "v_mfma_f32_16x16x32_bf16 v[96:99], a[144:147], a[128:131], v[96:99]\n"
    "ds_read_b128 a[152:155], v22 offset:2048\n"
    "v_mfma_f32_16x16x32_bf16 v[96:99], a[180:183], a[164:167], v[96:99]\n"
    "ds_read_b128 a[188:191], v22 offset:12288\n"
    "v_mfma_f32_16x16x32_bf16 v[116:119], a[144:147], a[132:135], v[116:119]\n"
    "v_mfma_f32_16x16x32_bf16 v[116:119], a[180:183], a[168:171], v[116:119]\n"
    "v_mfma_f32_16x16x32_bf16 v[136:139], a[144:147], a[136:139], v[136:139]\n"
    "ds_read_b128 a[156:159], v22 offset:3072\n"
    "v_mfma_f32_16x16x32_bf16 v[136:139], a[180:183], a[172:175], v[136:139]\n"
    "ds_read_b128 a[192:195], v22 offset:13312\n"
    "v_mfma_f32_16x16x32_bf16 v[156:159], a[144:147], a[140:143], v[156:159]\n"
    "s_waitcnt lgkmcnt(4)\n"
    "v_mfma_f32_16x16x32_bf16 v[156:159], a[180:183], a[176:179], v[156:159]\n"
    "v_mfma_f32_16x16x32_bf16 v[100:103], a[148:151], a[128:131], v[100:103]\n"
    "ds_read_b128 a[160:163], v22 offset:4096\n"
    "v_mfma_f32_16x16x32_bf16 v[100:103], a[184:187], a[164:167], v[100:103]\n"
    "ds_read_b128 a[196:199], v22 offset:14336\n"
    "v_mfma_f32_16x16x32_bf16 v[120:123], a[148:151], a[132:135], v[120:123]\n"
    "v_mfma_f32_16x16x32_bf16 v[120:123], a[184:187], a[168:171], v[120:123]\n"
    "v_mfma_f32_16x16x32_bf16 v[140:143], a[148:151], a[136:139], v[140:143]\n"
    "v_mfma_f32_16x16x32_bf16 v[140:143], a[184:187], a[172:175], v[140:143]\n"
    "v_mfma_f32_16x16x32_bf16 v[160:163], a[148:151], a[140:143], v[160:163]\n"
    "v_mfma_f32_16x16x32_bf16 v[160:163], a[184:187], a[176:179], v[160:163]\n"
    "s_waitcnt lgkmcnt(4)\n"
    "v_mfma_f32_16x16x32_bf16 v[104:107], a[152:155], a[128:131], v[104:107]\n"
    "v_mfma_f32_16x16x32_bf16 v[104:107], a[188:191], a[164:167], v[104:107]\n"
    "s_waitcnt lgkmcnt(0)\n"
    "v_mfma_f32_16x16x32_bf16 v[124:127], a[152:155], a[132:135], v[124:127]\n"
    "s_waitcnt vmcnt(0)\n"
    "v_mfma_f32_16x16x32_bf16 v[124:127], a[188:191], a[168:171], v[124:127]\n"
    "s_barrier\n"
    "v_cmp_ne_u32 vcc, v1, v13\n"
    "s_nop 1\n"
    "s_cbranch_vccnz DF_SPIN_%=\n"
    "DF_OK_%=:\n"
    "v_mfma_f32_16x16x32_bf16 v[144:147], a[152:155], a[136:139], v[144:147]\n"
    "s_add_u32 s62, s33, 0\n"
    "ds_read_b128 a[16:19], v22 offset:20480\n"
    "v_mfma_f32_16x16x32_bf16 v[144:147], a[188:191], a[172:175], v[144:147]\n"
    "buffer_load_dwordx4 v[216:219], v0, s[40:43], s62 offen sc1\n"
    "v_mfma_f32_16x16x32_bf16 v[164:167], a[152:155], a[140:143], v[164:167]\n"
    "ds_read_b128 a[52:55], v22 offset:30720\n"
    "v_mfma_f32_16x16x32_bf16 v[164:167], a[188:191], a[176:179], v[164:167]\n"
    "ds_read_b128 a[0:3], v23 offset:16384\n"
    "v_mfma_f32_16x16x32_bf16 v[108:111], a[156:159], a[128:131], v[108:111]\n"
    "ds_read_b128 a[36:39], v23 offset:17408\n"
    "v_mfma_f32_16x16x32_bf16 v[108:111], a[192:195], a[164:167], v[108:111]\n"
    "buffer_load_dwordx4 v[220:223], v0, s[40:43], s62 offen offset:1024 sc1\n"
    "v_mfma_f32_16x16x32_bf16 v[128:131], a[156:159], a[132:135], v[128:131]\n"
    "ds_read_b128 a[4:7], v23 offset:18432\n"
    "v_mfma_f32_16x16x32_bf16 v[128:131], a[192:195], a[168:171], v[128:131]\n"
    "ds_read_b128 a[40:43], v23 offset:19456\n"
    "v_mfma_f32_16x16x32_bf16 v[148:151], a[156:159], a[136:139], v[148:151]\n"
    "ds_read_b128 a[8:11], v23 offset:20480\n"
    "v_mfma_f32_16x16x32_bf16 v[148:151], a[192:195], a[172:175], v[148:151]\n"
    "buffer_load_dwordx2 v[224:225], v26, s[40:43], s62 offen offset:2048 sc1\n"
    "v_mfma_f32_16x16x32_bf16 v[168:171], a[156:159], a[140:143], v[168:171]\n"
    "s_add_u32 s62, s33, 8192\n"
    "ds_read_b128 a[44:47], v23 offset:21504\n"
    "v_mfma_f32_16x16x32_bf16 v[168:171], a[192:195], a[176:179], v[168:171]\n"
    "ds_read_b128 a[12:15], v23 offset:22528\n"
    "v_mfma_f32_16x16x32_bf16 v[112:115], a[160:163], a[128:131], v[112:115]\n"
    "ds_read_b128 a[48:51], v23 offset:23552\n"
    "v_mfma_f32_16x16x32_bf16 v[112:115], a[196:199], a[164:167], v[112:115]\n"
    "buffer_load_dwordx4 v[226:229], v0, s[40:43], s62 offen sc1\n"
    "v_mfma_f32_16x16x32_bf16 v[132:135], a[160:163], a[132:135], v[132:135]\n"
    "ds_read_b128 a[20:23], v22 offset:21504\n"
    "v_mfma_f32_16x16x32_bf16 v[132:135], a[196:199], a[168:171], v[132:135]\n"
    "ds_read_b128 a[56:59], v22 offset:31744\n"
    "v_mfma_f32_16x16x32_bf16 v[152:155], a[160:163], a[136:139], v[152:155]\n"
    "buffer_load_dwordx4 v[230:233], v0, s[40:43], s62 offen offset:1024 sc1\n"
    "v_mfma_f32_16x16x32_bf16 v[152:155], a[196:199], a[172:175], v[152:155]\n"
    "v_mfma_f32_16x16x32_bf16 v[172:175], a[160:163], a[140:143], v[172:175]\n"
    "v_mfma_f32_16x16x32_bf16 v[172:175], a[196:199], a[176:179], v[172:175]\n"
    "s_waitcnt lgkmcnt(3)\n"
    "v_mfma_f32_16x16x32_bf16 v[96:99], a[16:19], a[0:3], v[96:99]\n"
    "ds_read_b128 a[24:27], v22 offset:22528\n"
    "v_mfma_f32_16x16x32_bf16 v[96:99], a[52:55], a[36:39], v[96:99]\n"
    "ds_read_b128 a[60:63], v22 offset:32768\n"
    "v_mfma_f32_16x16x32_bf16 v[116:119], a[16:19], a[4:7], v[116:119]\n"
    "buffer_load_dwordx2 v[234:235], v26, s[40:43], s62 offen offset:2048 sc1\n"
    "v_mfma_f32_16x16x32_bf16 v[116:119], a[52:55], a[40:43], v[116:119]\n"
    "s_add_u32 s62, s33, 16384\n"
    "v_mfma_f32_16x16x32_bf16 v[136:139], a[16:19], a[8:11], v[136:139]\n"
    "ds_read_b128 a[28:31], v22 offset:23552\n"
    "v_mfma_f32_16x16x32_bf16 v[136:139], a[52:55], a[44:47], v[136:139]\n"
    "ds_read_b128 a[64:67], v22 offset:33792\n"
    "v_mfma_f32_16x16x32_bf16 v[156:159], a[16:19], a[12:15], v[156:159]\n"
    "s_waitcnt lgkmcnt(4)\n"
    "v_mfma_f32_16x16x32_bf16 v[156:159], a[52:55], a[48:51], v[156:159]\n"
    "buffer_load_dwordx4 v[236:239], v0, s[40:43], s62 offen sc1\n"
    "v_mfma_f32_16x16x32_bf16 v[100:103], a[20:23], a[0:3], v[100:103]\n"
    "ds_read_b128 a[32:35], v22 offset:24576\n"
    "v_mfma_f32_16x16x32_bf16 v[100:103], a[56:59], a[36:39], v[100:103]\n"
    "ds_read_b128 a[68:71], v22 offset:34816\n"
    "v_mfma_f32_16x16x32_bf16 v[120:123], a[20:23], a[4:7], v[120:123]\n"
    "buffer_load_dwordx4 v[240:243], v0, s[40:43], s62 offen offset:1024 sc1\n"
    "v_mfma_f32_16x16x32_bf16 v[120:123], a[56:59], a[40:43], v[120:123]\n"
    "v_mfma_f32_16x16x32_bf16 v[140:143], a[20:23], a[8:11], v[140:143]\n"
    "buffer_load_dwordx2 v[244:245], v26, s[40:43], s62 offen offset:2048 sc1\n"
    "v_mfma_f32_16x16x32_bf16 v[140:143], a[56:59], a[44:47], v[140:143]\n"
    "v_mfma_f32_16x16x32_bf16 v[160:163], a[20:23], a[12:15], v[160:163]\n"
    "v_mfma_f32_16x16x32_bf16 v[160:163], a[56:59], a[48:51], v[160:163]\n"
    "s_waitcnt lgkmcnt(4)\n"
    "v_mfma_f32_16x16x32_bf16 v[104:107], a[24:27], a[0:3], v[104:107]\n"
    "v_mfma_f32_16x16x32_bf16 v[104:107], a[60:63], a[36:39], v[104:107]\n"
    "s_waitcnt lgkmcnt(0)\n"
    "v_mfma_f32_16x16x32_bf16 v[124:127], a[24:27], a[4:7], v[124:127]\n"
    "s_waitcnt vmcnt(10)\n"
    "v_xor_b32 v23, 0x14000, v23\n"
    "v_xor_b32 v22, 0x14000, v22\n"
    "v_mfma_f32_16x16x32_bf16 v[124:127], a[60:63], a[40:43], v[124:127]\n"
    "s_barrier\n"
    "v_mfma_f32_16x16x32_bf16 v[144:147], a[24:27], a[8:11], v[144:147]\n"
    "ds_read_b128 a[144:147], v22 offset:0\n"
    "v_mfma_f32_16x16x32_bf16 v[144:147], a[60:63], a[44:47], v[144:147]\n"
    "v_mfma_f32_16x16x32_bf16 v[164:167], a[24:27], a[12:15], v[164:167]\n"
    "ds_read_b128 a[180:183], v22 offset:10240\n"
    "v_mfma_f32_16x16x32_bf16 v[164:167], a[60:63], a[48:51], v[164:167]\n"
    "ds_read_b128 a[132:135], v23 offset:2048\n"
    "v_mfma_f32_16x16x32_bf16 v[108:111], a[28:31], a[0:3], v[108:111]\n"
    "ds_read_b128 a[168:171], v23 offset:3072\n"
    "v_mfma_f32_16x16x32_bf16 v[108:111], a[64:67], a[36:39], v[108:111]\n"
    "v_mfma_f32_16x16x32_bf16 v[128:131], a[28:31], a[4:7], v[128:131]\n"
    "ds_read_b128 a[148:151], v22 offset:1024\n"
    "v_mfma_f32_16x16x32_bf16 v[128:131], a[64:67], a[40:43], v[128:131]\n"
    "ds_read_b128 a[184:187], v22 offset:11264\n"
    "v_mfma_f32_16x16x32_bf16 v[148:151], a[28:31], a[8:11], v[148:151]\n"
    "ds_read_b128 a[152:155], v22 offset:2048\n"
    "v_mfma_f32_16x16x32_bf16 v[148:151], a[64:67], a[44:47], v[148:151]\n"
    "v_mfma_f32_16x16x32_bf16 v[168:171], a[28:31], a[12:15], v[168:171]\n"
    "ds_read_b128 a[188:191], v22 offset:12288\n"
    "v_mfma_f32_16x16x32_bf16 v[168:171], a[64:67], a[48:51], v[168:171]\n"
    "ds_read_b128 a[156:159], v22 offset:3072\n"
    "v_mfma_f32_16x16x32_bf16 v[112:115], a[32:35], a[0:3], v[112:115]\n"
    "ds_read_b128 a[192:195], v22 offset:13312\n"
    "v_mfma_f32_16x16x32_bf16 v[112:115], a[68:71], a[36:39], v[112:115]\n"
    "v_mfma_f32_16x16x32_bf16 v[132:135], a[32:35], a[4:7], v[132:135]\n"
    "ds_read_b128 a[160:163], v22 offset:4096\n"
    "v_mfma_f32_16x16x32_bf16 v[132:135], a[68:71], a[40:43], v[132:135]\n"
    "ds_read_b128 a[196:199], v22 offset:14336\n"
    "v_mfma_f32_16x16x32_bf16 v[152:155], a[32:35], a[8:11], v[152:155]\n"
    "ds_read_b128 a[136:139], v23 offset:4096\n"
    "v_mfma_f32_16x16x32_bf16 v[152:155], a[68:71], a[44:47], v[152:155]\n"
    "v_mfma_f32_16x16x32_bf16 v[172:175], a[32:35], a[12:15], v[172:175]\n"
    "ds_read_b128 a[172:175], v23 offset:5120\n"
    "ds_read_b128 a[140:143], v23 offset:6144\n"
    "v_mfma_f32_16x16x32_bf16 v[172:175], a[68:71], a[48:51], v[172:175]\n"
    "s_waitcnt lgkmcnt(11)\n"
    "ds_read_b128 a[176:179], v23 offset:7168\n"
    "ds_read_b128 a[128:131], v23 offset:0\n"
    "ds_read_b128 a[164:167], v23 offset:1024\n"
    "v_mfma_f32_16x16x32_bf16 v[116:119], a[144:147], a[132:135], v[116:119]\n"
    "v_mfma_f32_16x16x32_bf16 v[116:119], a[180:183], a[168:171], v[116:119]\n"
    "s_waitcnt lgkmcnt(12)\n"
    "v_mfma_f32_16x16x32_bf16 v[120:123], a[148:151], a[132:135], v[120:123]\n"
    "v_mfma_f32_16x16x32_bf16 v[120:123], a[184:187], a[168:171], v[120:123]\n"
    "s_waitcnt lgkmcnt(10)\n"
    "v_mfma_f32_16x16x32_bf16 v[124:127], a[152:155], a[132:135], v[124:127]\n"
    "v_mfma_f32_16x16x32_bf16 v[124:127], a[188:191], a[168:171], v[124:127]\n"
    "s_waitcnt lgkmcnt(9)\n"
    "v_mfma_f32_16x16x32_bf16 v[128:131], a[156:159], a[132:135], v[128:131]\n"
    "s_waitcnt lgkmcnt(6)\n"
    "v_mfma_f32_16x16x32_bf16 v[128:131], a[192:195], a[168:171], v[128:131]\n"
    "v_mfma_f32_16x16x32_bf16 v[132:135], a[160:163], a[132:135], v[132:135]\n"
    "v_mfma_f32_16x16x32_bf16 v[132:135], a[196:199], a[168:171], v[132:135]\n"
    "s_waitcnt lgkmcnt(4)\n"
    "v_mfma_f32_16x16x32_bf16 v[136:139], a[144:147], a[136:139], v[136:139]\n"
    "v_mfma_f32_16x16x32_bf16 v[136:139], a[180:183], a[172:175], v[136:139]\n"
    "v_mfma_f32_16x16x32_bf16 v[140:143], a[148:151], a[136:139], v[140:143]\n"
    "v_mfma_f32_16x16x32_bf16 v[140:143], a[184:187], a[172:175], v[140:143]\n"
    "v_mfma_f32_16x16x32_bf16 v[144:147], a[152:155], a[136:139], v[144:147]\n"
    "v_mfma_f32_16x16x32_bf16 v[144:147], a[188:191], a[172:175], v[144:147]\n"
    "v_cvt_pk_bf16_f32 v116, v116, v117\n"
    "v_cvt_pk_bf16_f32 v117, v118, v119\n"
    "v_mfma_f32_16x16x32_bf16 v[148:151], a[156:159], a[136:139], v[148:151]\n"
    "v_cvt_pk_bf16_f32 v118, v120, v121\n"
    "v_cvt_pk_bf16_f32 v119, v122, v123\n"
    "v_mfma_f32_16x16x32_bf16 v[148:151], a[192:195], a[172:175], v[148:151]\n"
    "s_waitcnt lgkmcnt(0)\n"
    "v_cvt_pk_bf16_f32 v120, v124, v125\n"
    "v_cvt_pk_bf16_f32 v121, v126, v127\n"
    "v_mfma_f32_16x16x32_bf16 v[152:155], a[160:163], a[136:139], v[152:155]\n"
    "v_cvt_pk_bf16_f32 v122, v128, v129\n"
    "v_cvt_pk_bf16_f32 v123, v130, v131\n"
    "v_mfma_f32_16x16x32_bf16 v[152:155], a[196:199], a[172:175], v[152:155]\n"
    "v_cvt_pk_bf16_f32 v124, v132, v133\n"
    "v_cvt_pk_bf16_f32 v125, v134, v135\n"
    "v_mfma_f32_16x16x32_bf16 v[156:159], a[144:147], a[140:143], v[156:159]\n"
    "buffer_store_dwordx4 v[116:119], v0, s[40:43], s36 offen sc1\n"
    "v_mfma_f32_16x16x32_bf16 v[156:159], a[180:183], a[176:179], v[156:159]\n"
    "buffer_store_dwordx4 v[120:123], v0, s[40:43], s36 offen offset:1024 sc1\n"
    "v_mfma_f32_16x16x32_bf16 v[160:163], a[148:151], a[140:143], v[160:163]\n"
    "buffer_store_dwordx2 v[124:125], v26, s[40:43], s36 offen offset:2048 sc1\n"
    "v_mfma_f32_16x16x32_bf16 v[160:163], a[184:187], a[176:179], v[160:163]\n"
    "v_mfma_f32_16x16x32_bf16 v[164:167], a[152:155], a[140:143], v[164:167]\n"
    "v_mfma_f32_16x16x32_bf16 v[164:167], a[188:191], a[176:179], v[164:167]\n"
    "v_cvt_pk_bf16_f32 v136, v136, v137\n"
    "v_cvt_pk_bf16_f32 v137, v138, v139\n"
    "v_mfma_f32_16x16x32_bf16 v[168:171], a[156:159], a[140:143], v[168:171]\n"
    "v_cvt_pk_bf16_f32 v138, v140, v141\n"
    "v_cvt_pk_bf16_f32 v139, v142, v143\n"
    "v_mfma_f32_16x16x32_bf16 v[168:171], a[192:195], a[176:179], v[168:171]\n"
    "v_cvt_pk_bf16_f32 v140, v144, v145\n"
    "v_cvt_pk_bf16_f32 v141, v146, v147\n"
    "v_mfma_f32_16x16x32_bf16 v[172:175], a[160:163], a[140:143], v[172:175]\n"
    "v_cvt_pk_bf16_f32 v142, v148, v149\n"
    "v_cvt_pk_bf16_f32 v143, v150, v151\n"
    "v_mfma_f32_16x16x32_bf16 v[172:175], a[196:199], a[176:179], v[172:175]\n"
    "v_cvt_pk_bf16_f32 v144, v152, v153\n"
    "v_cvt_pk_bf16_f32 v145, v154, v155\n"
    "v_mfma_f32_16x16x32_bf16 v[96:99], a[144:147], a[128:131], v[96:99]\n"
    "buffer_store_dwordx4 v[136:139], v0, s[40:43], s37 offen sc1\n"
    "v_mfma_f32_16x16x32_bf16 v[96:99], a[180:183], a[164:167], v[96:99]\n"
    "buffer_store_dwordx4 v[140:143], v0, s[40:43], s37 offen offset:1024 sc1\n"
    "v_mfma_f32_16x16x32_bf16 v[100:103], a[148:151], a[128:131], v[100:103]\n"
    "buffer_store_dwordx2 v[144:145], v26, s[40:43], s37 offen offset:2048 sc1\n"
    "v_mfma_f32_16x16x32_bf16 v[100:103], a[184:187], a[164:167], v[100:103]\n"
    "v_mfma_f32_16x16x32_bf16 v[104:107], a[152:155], a[128:131], v[104:107]\n"
    "v_mfma_f32_16x16x32_bf16 v[104:107], a[188:191], a[164:167], v[104:107]\n"
    "v_cvt_pk_bf16_f32 v156, v156, v157\n"
    "v_cvt_pk_bf16_f32 v157, v158, v159\n"
    "v_mfma_f32_16x16x32_bf16 v[108:111], a[156:159], a[128:131], v[108:111]\n"
    "v_cvt_pk_bf16_f32 v158, v160, v161\n"
    "v_cvt_pk_bf16_f32 v159, v162, v163\n"
    "v_mfma_f32_16x16x32_bf16 v[108:111], a[192:195], a[164:167], v[108:111]\n"
    "v_cvt_pk_bf16_f32 v160, v164, v165\n"
    "v_cvt_pk_bf16_f32 v161, v166, v167\n"
    "v_mfma_f32_16x16x32_bf16 v[112:115], a[160:163], a[128:131], v[112:115]\n"
    "v_cvt_pk_bf16_f32 v162, v168, v169\n"
    "v_cvt_pk_bf16_f32 v163, v170, v171\n"
    "v_mfma_f32_16x16x32_bf16 v[112:115], a[196:199], a[164:167], v[112:115]\n"
    "v_cvt_pk_bf16_f32 v164, v172, v173\n"
    "v_cvt_pk_bf16_f32 v165, v174, v175\n"
    "s_nop 1\n"
    "buffer_store_dwordx4 v[156:159], v0, s[40:43], s67 offen sc1\n"
    "buffer_store_dwordx4 v[160:163], v0, s[40:43], s67 offen offset:1024 sc1\n"
    "buffer_store_dwordx2 v[164:165], v26, s[40:43], s67 offen offset:2048 sc1\n"
    "s_waitcnt vmcnt(15)\n"
    "v_dot2_f32_bf16 v176, v216, v16, v176\n"
    "v_dot2_f32_bf16 v177, v216, v17, v177\n"
    "v_dot2_f32_bf16 v178, v217, v16, v178\n"
    "v_dot2_f32_bf16 v179, v217, v17, v179\n"
    "v_dot2_f32_bf16 v180, v218, v16, v180\n"
    "v_dot2_f32_bf16 v181, v218, v17, v181\n"
    "v_dot2_f32_bf16 v182, v219, v16, v182\n"
    "v_dot2_f32_bf16 v183, v219, v17, v183\n"
    "v_dot2_f32_bf16 v184, v220, v16, v184\n"
    "v_dot2_f32_bf16 v185, v220, v17, v185\n"
    "v_dot2_f32_bf16 v186, v221, v16, v186\n"
    "v_dot2_f32_bf16 v187, v221, v17, v187\n"
    "v_dot2_f32_bf16 v188, v222, v16, v188\n"
    "v_dot2_f32_bf16 v189, v222, v17, v189\n"
    "v_dot2_f32_bf16 v190, v223, v16, v190\n"
    "v_dot2_f32_bf16 v191, v223, v17, v191\n"
    "v_dot2_f32_bf16 v192, v224, v16, v192\n"
    "v_dot2_f32_bf16 v193, v224, v17, v193\n"
    "v_dot2_f32_bf16 v194, v225, v16, v194\n"
    "v_dot2_f32_bf16 v195, v225, v17, v195\n"
    "s_waitcnt vmcnt(12)\n"
    "v_dot2_f32_bf16 v176, v226, v16, v176\n"
    "v_dot2_f32_bf16 v177, v226, v17, v177\n"
    "v_dot2_f32_bf16 v178, v227, v16, v178\n"
    "v_dot2_f32_bf16 v179, v227, v17, v179\n"
    "v_dot2_f32_bf16 v180, v228, v16, v180\n"
    "v_dot2_f32_bf16 v181, v228, v17, v181\n"
    "v_dot2_f32_bf16 v182, v229, v16, v182\n"
    "v_dot2_f32_bf16 v183, v229, v17, v183\n"
    "v_dot2_f32_bf16 v184, v230, v16, v184\n"
    "v_dot2_f32_bf16 v185, v230, v17, v185\n"
    "v_dot2_f32_bf16 v186, v231, v16, v186\n"
    "v_dot2_f32_bf16 v187, v231, v17, v187\n"
    "v_dot2_f32_bf16 v188, v232, v16, v188\n"
    "v_dot2_f32_bf16 v189, v232, v17, v189\n"
    "v_dot2_f32_bf16 v190, v233, v16, v190\n"
    "v_dot2_f32_bf16 v191, v233, v17, v191\n"
    "v_dot2_f32_bf16 v192, v234, v16, v192\n"
    "v_dot2_f32_bf16 v193, v234, v17, v193\n"
    "v_dot2_f32_bf16 v194, v235, v16, v194\n"
    "v_dot2_f32_bf16 v195, v235, v17, v195\n"
    "s_waitcnt vmcnt(9)\n"
    "v_dot2_f32_bf16 v176, v236, v16, v176\n"
    "v_dot2_f32_bf16 v177, v236, v17, v177\n"
    "v_dot2_f32_bf16 v178, v237, v16, v178\n"
    "v_dot2_f32_bf16 v179, v237, v17, v179\n"
    "v_dot2_f32_bf16 v180, v238, v16, v180\n"
    "v_dot2_f32_bf16 v181, v238, v17, v181\n"
    "v_dot2_f32_bf16 v182, v239, v16, v182\n"
    "v_dot2_f32_bf16 v183, v239, v17, v183\n"
    "v_dot2_f32_bf16 v184, v240, v16, v184\n"
    "v_dot2_f32_bf16 v185, v240, v17, v185\n"
    "v_dot2_f32_bf16 v186, v241, v16, v186\n"
    "v_dot2_f32_bf16 v187, v241, v17, v187\n"
    "v_dot2_f32_bf16 v188, v242, v16, v188\n"
    "v_dot2_f32_bf16 v189, v242, v17, v189\n"
    "v_dot2_f32_bf16 v190, v243, v16, v190\n"
    "v_dot2_f32_bf16 v191, v243, v17, v191\n"
    "v_dot2_f32_bf16 v192, v244, v16, v192\n"
    "v_dot2_f32_bf16 v193, v244, v17, v193\n"
    "v_dot2_f32_bf16 v194, v245, v16, v194\n"
    "v_dot2_f32_bf16 v195, v245, v17, v195\n"
    "s_mov_b32 s61, 0xc2b17218\n"
    "s_nop 1\n"
    "v_mul_f32 v196, s61, v176\n"
    "v_mul_f32 v197, s61, v177\n"
    "v_mul_f32 v198, s61, v178\n"
    "v_mul_f32 v199, s61, v179\n"
    "v_mul_f32 v200, s61, v180\n"
    "v_mul_f32 v201, s61, v181\n"
    "v_mul_f32 v202, s61, v182\n"
    "v_mul_f32 v203, s61, v183\n"
    "v_mul_f32 v204, s61, v184\n"
    "v_mul_f32 v205, s61, v185\n"
    "v_mul_f32 v206, s61, v186\n"
    "v_mul_f32 v207, s61, v187\n"
    "v_mul_f32 v208, s61, v188\n"
    "v_mul_f32 v209, s61, v189\n"
    "v_mul_f32 v210, s61, v190\n"
    "v_mul_f32 v211, s61, v191\n"
    "v_mul_f32 v212, s61, v192\n"
    "v_mul_f32 v213, s61, v193\n"
    "v_mul_f32 v214, s61, v194\n"
    "v_mul_f32 v215, s61, v195\n"
    "v_exp_f32 v196, v196\n"
    "v_exp_f32 v197, v197\n"
    "v_exp_f32 v198, v198\n"
    "v_exp_f32 v199, v199\n"
    "v_exp_f32 v200, v200\n"
    "v_exp_f32 v201, v201\n"
    "v_exp_f32 v202, v202\n"
    "v_exp_f32 v203, v203\n"
    "v_exp_f32 v204, v204\n"
    "v_exp_f32 v205, v205\n"
    "v_exp_f32 v206, v206\n"
    "v_exp_f32 v207, v207\n"
    "v_exp_f32 v208, v208\n"
    "v_exp_f32 v209, v209\n"
    "v_exp_f32 v210, v210\n"
    "s_waitcnt vmcnt(0)\n"
    "buffer_store_dword v13, v25, s[40:43], 0 offen offset:16 sc1\n"
    "v_exp_f32 v211, v211\n"
    "v_exp_f32 v212, v212\n"
    "v_exp_f32 v213, v213\n"
    "v_exp_f32 v214, v214\n"
    "v_exp_f32 v215, v215\n"
    "s_mov_b32 s61, 0x3c853526\n"
    "v_fma_f32 v196, v196, s61, s61\n"
    "v_fma_f32 v197, v197, s61, s61\n"
    "v_fma_f32 v198, v198, s61, s61\n"
    "v_fma_f32 v199, v199, s61, s61\n"
    "v_fma_f32 v200, v200, s61, s61\n"
    "v_fma_f32 v201, v201, s61, s61\n"
    "v_fma_f32 v202, v202, s61, s61\n"
    "v_fma_f32 v203, v203, s61, s61\n"
    "v_fma_f32 v204, v204, s61, s61\n"
    "v_fma_f32 v205, v205, s61, s61\n"
    "v_fma_f32 v206, v206, s61, s61\n"
    "v_fma_f32 v207, v207, s61, s61\n"
    "v_fma_f32 v208, v208, s61, s61\n"
    "v_fma_f32 v209, v209, s61, s61\n"
    "v_fma_f32 v210, v210, s61, s61\n"
    "v_fma_f32 v211, v211, s61, s61\n"
    "v_fma_f32 v212, v212, s61, s61\n"
    "v_fma_f32 v213, v213, s61, s61\n"
    "v_fma_f32 v214, v214, s61, s61\n"
    "v_fma_f32 v215, v215, s61, s61\n"
    "v_rcp_f32 v196, v196\n"
    "v_rcp_f32 v197, v197\n"
    "v_rcp_f32 v198, v198\n"
    "v_rcp_f32 v199, v199\n"
    "v_rcp_f32 v200, v200\n"
    "v_rcp_f32 v201, v201\n"
    "v_rcp_f32 v202, v202\n"
    "v_rcp_f32 v203, v203\n"
    "v_rcp_f32 v204, v204\n"
    "v_rcp_f32 v205, v205\n"
    "v_rcp_f32 v206, v206\n"
    "v_rcp_f32 v207, v207\n"
    "v_rcp_f32 v208, v208\n"
    "v_rcp_f32 v209, v209\n"
    "v_rcp_f32 v210, v210\n"
    "v_rcp_f32 v211, v211\n"
    "v_rcp_f32 v212, v212\n"
    "v_rcp_f32 v213, v213\n"
    "v_rcp_f32 v214, v214\n"
    "v_rcp_f32 v215, v215\n"
    "v_nop\n"
    "v_mul_f32 v176, v176, v196\n"
    "v_mul_f32 v177, v177, v197\n"
    "v_mul_f32 v178, v178, v198\n"
    "v_mul_f32 v179, v179, v199\n"
    "v_mul_f32 v180, v180, v200\n"
    "v_mul_f32 v181, v181, v201\n"
    "v_mul_f32 v182, v182, v202\n"
    "v_mul_f32 v183, v183, v203\n"
    "v_mul_f32 v184, v184, v204\n"
    "v_mul_f32 v185, v185, v205\n"
    "v_mul_f32 v186, v186, v206\n"
    "v_mul_f32 v187, v187, v207\n"
    "v_mul_f32 v188, v188, v208\n"
    "v_mul_f32 v189, v189, v209\n"
    "v_mul_f32 v190, v190, v210\n"
    "v_mul_f32 v191, v191, v211\n"
    "v_mul_f32 v192, v192, v212\n"
    "v_mul_f32 v193, v193, v213\n"
    "v_mul_f32 v194, v194, v214\n"
    "v_mul_f32 v195, v195, v215\n"
    "buffer_load_dword v1, v10, s[40:43], s34 offen offset:16 sc1\n"
    "s_waitcnt vmcnt(0)\n"
    "v_cmp_ne_u32 vcc, v1, v13\n"
    "s_nop 1\n"
    "s_cbranch_vccnz SF_SPIN_%=\n"
    "SF_OK_%=:\n"
    "s_add_u32 s62, s33, 4096\n"
    "buffer_load_dwordx4 v[32:35], v0, s[40:43], s62 offen sc1\n"
    "buffer_load_dwordx4 v[36:39], v0, s[40:43], s62 offen offset:1024 sc1\n"
    "buffer_load_dwordx2 v[40:41], v26, s[40:43], s62 offen offset:2048 sc1\n"
    "s_add_u32 s62, s33, 12288\n"
    "buffer_load_dwordx4 v[42:45], v0, s[40:43], s62 offen sc1\n"
    "buffer_load_dwordx4 v[46:49], v0, s[40:43], s62 offen offset:1024 sc1\n"
    "buffer_load_dwordx2 v[50:51], v26, s[40:43], s62 offen offset:2048 sc1\n"
    "s_add_u32 s62, s33, 20480\n"
    "buffer_load_dwordx4 v[52:55], v0, s[40:43], s62 offen sc1\n"
    "buffer_load_dwordx4 v[56:59], v0, s[40:43], s62 offen offset:1024 sc1\n"
    "buffer_load_dwordx2 v[60:61], v26, s[40:43], s62 offen offset:2048 sc1\n"
    "s_or_b32 s59, %[bid], s58\n"
    "s_cmp_eq_u32 s59, 0\n"
    "s_cselect_b32 s59, 0, 0xc0000000\n"
    "v_add_u32 v13, 0x61c88650, v13\n"
    "buffer_store_dword v13, off, s[40:43], s59 offset:0 sc1\n"
    "s_waitcnt vmcnt(7)\n"
    "v_dot2_f32_bf16 v96, v32, v16, v96\n"
    "v_dot2_f32_bf16 v97, v32, v17, v97\n"
    "v_dot2_f32_bf16 v98, v33, v16, v98\n"
    "v_dot2_f32_bf16 v99, v33, v17, v99\n"
    "v_dot2_f32_bf16 v100, v34, v16, v100\n"
    "v_dot2_f32_bf16 v101, v34, v17, v101\n"
    "v_dot2_f32_bf16 v102, v35, v16, v102\n"
    "v_dot2_f32_bf16 v103, v35, v17, v103\n"
    "v_dot2_f32_bf16 v104, v36, v16, v104\n"
    "v_dot2_f32_bf16 v105, v36, v17, v105\n"
    "v_dot2_f32_bf16 v106, v37, v16, v106\n"
    "v_dot2_f32_bf16 v107, v37, v17, v107\n"
    "v_dot2_f32_bf16 v108, v38, v16, v108\n"
    "v_dot2_f32_bf16 v109, v38, v17, v109\n"
    "v_dot2_f32_bf16 v110, v39, v16, v110\n"
    "v_dot2_f32_bf16 v111, v39, v17, v111\n"
    "v_dot2_f32_bf16 v112, v40, v16, v112\n"
    "v_dot2_f32_bf16 v113, v40, v17, v113\n"
    "v_dot2_f32_bf16 v114, v41, v16, v114\n"
    "v_dot2_f32_bf16 v115, v41, v17, v115\n"
    "s_waitcnt vmcnt(4)\n"
    "v_dot2_f32_bf16 v96, v42, v16, v96\n"
    "v_dot2_f32_bf16 v97, v42, v17, v97\n"
    "v_dot2_f32_bf16 v98, v43, v16, v98\n"
    "v_dot2_f32_bf16 v99, v43, v17, v99\n"
    "v_dot2_f32_bf16 v100, v44, v16, v100\n"
    "v_dot2_f32_bf16 v101, v44, v17, v101\n"
    "v_dot2_f32_bf16 v102, v45, v16, v102\n"
    "v_dot2_f32_bf16 v103, v45, v17, v103\n"
    "v_dot2_f32_bf16 v104, v46, v16, v104\n"
    "v_dot2_f32_bf16 v105, v46, v17, v105\n"
    "v_dot2_f32_bf16 v106, v47, v16, v106\n"
    "v_dot2_f32_bf16 v107, v47, v17, v107\n"
    "v_dot2_f32_bf16 v108, v48, v16, v108\n"
    "v_dot2_f32_bf16 v109, v48, v17, v109\n"
    "v_dot2_f32_bf16 v110, v49, v16, v110\n"
    "v_dot2_f32_bf16 v111, v49, v17, v111\n"
    "v_dot2_f32_bf16 v112, v50, v16, v112\n"
    "v_dot2_f32_bf16 v113, v50, v17, v113\n"
    "v_dot2_f32_bf16 v114, v51, v16, v114\n"
    "v_dot2_f32_bf16 v115, v51, v17, v115\n"
    "s_waitcnt vmcnt(1)\n"
    "v_dot2_f32_bf16 v96, v52, v16, v96\n"
    "v_dot2_f32_bf16 v97, v52, v17, v97\n"
    "v_dot2_f32_bf16 v98, v53, v16, v98\n"
    "v_dot2_f32_bf16 v99, v53, v17, v99\n"
    "v_dot2_f32_bf16 v100, v54, v16, v100\n"
    "v_dot2_f32_bf16 v101, v54, v17, v101\n"
    "v_dot2_f32_bf16 v102, v55, v16, v102\n"
    "v_dot2_f32_bf16 v103, v55, v17, v103\n"
    "v_dot2_f32_bf16 v104, v56, v16, v104\n"
    "v_dot2_f32_bf16 v105, v56, v17, v105\n"
    "v_dot2_f32_bf16 v106, v57, v16, v106\n"
    "v_dot2_f32_bf16 v107, v57, v17, v107\n"
    "v_dot2_f32_bf16 v108, v58, v16, v108\n"
    "v_dot2_f32_bf16 v109, v58, v17, v109\n"
    "v_dot2_f32_bf16 v110, v59, v16, v110\n"
    "v_dot2_f32_bf16 v111, v59, v17, v111\n"
    "v_dot2_f32_bf16 v112, v60, v16, v112\n"
    "v_dot2_f32_bf16 v113, v60, v17, v113\n"
    "v_dot2_f32_bf16 v114, v61, v16, v114\n"
    "v_dot2_f32_bf16 v115, v61, v17, v115\n"
    "s_nop 2\n"
    "v_fma_f32 v64, v96, v176, v64\n"
    "v_fma_f32 v65, v97, v177, v65\n"
    "v_fma_f32 v66, v98, v178, v66\n"
    "v_fma_f32 v67, v99, v179, v67\n"
    "v_cvt_pk_bf16_f32 v84, v64, v65\n"
    "v_cvt_pk_bf16_f32 v85, v66, v67\n"
    "v_fma_f32 v68, v100, v180, v68\n"
    "v_fma_f32 v69, v101, v181, v69\n"
    "v_fma_f32 v70, v102, v182, v70\n"
    "v_fma_f32 v71, v103, v183, v71\n"
    "v_cvt_pk_bf16_f32 v86, v68, v69\n"
    "v_cvt_pk_bf16_f32 v87, v70, v71\n"
    "s_nop 0\n"
    "v_permlane16_swap_b32 v84, v86\n"
    "v_permlane16_swap_b32 v85, v87\n"
    "buffer_store_dwordx4 v[84:87], v18, s[44:47], s35 offen offset:0\n"
    "v_fma_f32 v72, v104, v184, v72\n"
    "v_fma_f32 v73, v105, v185, v73\n"
    "v_fma_f32 v74, v106, v186, v74\n"
    "v_fma_f32 v75, v107, v187, v75\n"
    "v_cvt_pk_bf16_f32 v88, v72, v73\n"
    "v_cvt_pk_bf16_f32 v89, v74, v75\n"
    "v_fma_f32 v76, v108, v188, v76\n"
    "v_fma_f32 v77, v109, v189, v77\n"
    "v_fma_f32 v78, v110, v190, v78\n"
    "v_fma_f32 v79, v111, v191, v79\n"
    "v_cvt_pk_bf16_f32 v90, v76, v77\n"
    "v_cvt_pk_bf16_f32 v91, v78, v79\n"
    "s_nop 0\n"
    "v_permlane16_swap_b32 v88, v90\n"
    "v_permlane16_swap_b32 v89, v91\n"
    "buffer_store_dwordx4 v[88:91], v18, s[44:47], s35 offen offset:64\n"
    "v_fma_f32 v80, v112, v192, v80\n"
    "v_fma_f32 v81, v113, v193, v81\n"
    "v_fma_f32 v82, v114, v194, v82\n"
    "v_fma_f32 v83, v115, v195, v83\n"
    "v_cvt_pk_bf16_f32 v92, v80, v81\n"
    "v_cvt_pk_bf16_f32 v93, v82, v83\n"
    "buffer_store_dwordx2 v[92:93], v24, s[44:47], s35 offen offset:128\n"
    "s_branch THE_END_%=\n"
    "LNF0_SPIN_%=:\n"
    "s_mov_b32 s38, 0x40000\n"
    "LNF0_SPIN2_%=:\n"
    "s_sleep 1\n"
    "buffer_load_dword v1, v10, s[40:43], s62 offen offset:0 sc1\n"
    "s_waitcnt vmcnt(0)\n"
    "v_cmp_ne_u32 vcc, v1, v13\n"
    "s_nop 1\n"
    "s_cbranch_vccz LNF0_OK_%=\n"
    "s_sub_u32 s38, s38, 1\n"
    "s_cbranch_scc0 LNF0_SPIN2_%=\n"
    "s_branch LNF0_OK_%=\n"
    "LNF1_SPIN_%=:\n"
    "s_mov_b32 s38, 0x40000\n"
    "LNF1_SPIN2_%=:\n"
    "s_sleep 1\n"
    "buffer_load_dword v1, v10, s[40:43], s62 offen offset:0 sc1\n"
    "s_waitcnt vmcnt(0)\n"
    "v_cmp_ne_u32 vcc, v1, v13\n"
    "s_nop 1\n"
    "s_cbranch_vccz LNF1_OK_%=\n"
    "s_sub_u32 s38, s38, 1\n"
    "s_cbranch_scc0 LNF1_SPIN2_%=\n"
    "s_branch LNF1_OK_%=\n"
    "KVF_SPIN_%=:\n"
    "s_mov_b32 s38, 0x40000\n"
    "KVF_SPIN2_%=:\n"
    "s_sleep 1\n"
    "buffer_load_dword v1, v10, s[40:43], s63 offen offset:0 sc1\n"
    "s_waitcnt vmcnt(0)\n"
    "v_cmp_ne_u32 vcc, v1, v13\n"
    "s_nop 1\n"
    "s_cbranch_vccz KVF_OK_%=\n"
    "s_sub_u32 s38, s38, 1\n"
    "s_cbranch_scc0 KVF_SPIN2_%=\n"
    "s_branch KVF_OK_%=\n"
    "HF_SPIN_%=:\n"
    "s_mov_b32 s38, 0x40000\n"
    "HF_SPIN2_%=:\n"
    "s_sleep 1\n"
    "buffer_load_dword v1, v10, s[40:43], s62 offen offset:0 sc1\n"
    "s_waitcnt vmcnt(0)\n"
    "v_cmp_ne_u32 vcc, v1, v13\n"
    "s_nop 1\n"
    "s_cbranch_vccz HF_OK_%=\n"
    "s_sub_u32 s38, s38, 1\n"
    "s_cbranch_scc0 HF_SPIN2_%=\n"
    "s_branch HF_OK_%=\n"
    "CF_SPIN_%=:\n"
    "s_mov_b32 s38, 0x40000\n"
    "CF_SPIN2_%=:\n"
    "s_sleep 1\n"
    "buffer_load_dword v1, v10, s[40:43], s32 offen offset:0 sc1\n"
    "s_waitcnt vmcnt(0)\n"
    "v_cmp_ne_u32 vcc, v1, v13\n"
    "s_nop 1\n"
    "s_cbranch_vccz CF_OK_%=\n"
    "s_sub_u32 s38, s38, 1\n"
    "s_cbranch_scc0 CF_SPIN2_%=\n"
    "s_branch CF_OK_%=\n"
    "DF_SPIN_%=:\n"
    "s_mov_b32 s38, 0x40000\n"
    "DF_SPIN2_%=:\n"
    "s_sleep 1\n"
    "buffer_load_dword v1, v10, s[40:43], s34 offen offset:0 sc1\n"
    "s_waitcnt vmcnt(0)\n"
    "v_cmp_ne_u32 vcc, v1, v13\n"
    "s_nop 1\n"
    "s_cbranch_vccz DF_OK_%=\n"
    "s_sub_u32 s38, s38, 1\n"
    "s_cbranch_scc0 DF_SPIN2_%=\n"
    "s_branch DF_OK_%=\n"
    "SF_SPIN_%=:\n"
    "s_mov_b32 s38, 0x40000\n"
    "SF_SPIN2_%=:\n"
    "s_sleep 1\n"
    "buffer_load_dword v1, v10, s[40:43], s34 offen offset:16 sc1\n"
    "s_waitcnt vmcnt(0)\n"
    "v_cmp_ne_u32 vcc, v1, v13\n"
    "s_nop 1\n"
    "s_cbranch_vccz SF_OK_%=\n"
    "s_sub_u32 s38, s38, 1\n"
    "s_cbranch_scc0 SF_SPIN2_%=\n"
    "s_branch SF_OK_%=\n"
    "MAXPASS_%=:\n"
    "s_waitcnt vmcnt(0) lgkmcnt(0)\n"
    "s_barrier\n"
    "s_mov_b32 s90, 1\n"
    "s_mov_b32 s65, 0xc0000000\n"
    "s_mov_b32 s52, 0xc0000000\n"
    "v_mov_b32 v16, v2\n"
    "v_mov_b32 v17, v2\n"
    "v_mov_b32 v18, v2\n"
    "v_mov_b32 v19, v2\n"
    "v_lshrrev_b32 v240, 4, v12\n"
    "v_lshlrev_b32 v240, 2, v240\n"
    "v_and_b32 v28, 15, v12\n"
    "s_lshl_b32 s59, s75, 4\n"
    "v_add_u32 v241, s59, v28\n"
    "s_lshl_b32 s59, s76, 4\n"
    "v_add_u32 v242, s59, v28\n"
    "s_mov_b32 s61, s91\n"
    "s_lshl_b32 s59, s57, 2\n"
    "s_sub_u32 s63, 63, s59\n"
    "MAXLOOP_%=:\n"
    "s_add_u32 s62, s61, 0x1000\n"
    "buffer_load_dwordx4 a[160:163], v0, s[40:43], s61 offen offset:0 sc1\n"
    "buffer_load_dwordx4 a[164:167], v0, s[40:43], s61 offen offset:1024 sc1\n"
    "buffer_load_dwordx4 a[168:171], v0, s[40:43], s61 offen offset:2048 sc1\n"
    "buffer_load_dwordx4 a[172:175], v0, s[40:43], s61 offen offset:3072 sc1\n"
    "buffer_load_dwordx4 a[176:179], v0, s[40:43], s62 offen offset:0 sc1\n"
    "buffer_load_dwordx4 a[180:183], v0, s[40:43], s62 offen offset:1024 sc1\n"
    "buffer_load_dwordx4 a[184:187], v0, s[40:43], s62 offen offset:2048 sc1\n"
    "buffer_load_dwordx4 a[188:191], v0, s[40:43], s62 offen offset:3072 sc1\n"
    "s_waitcnt vmcnt(0)\n"
    "v_mfma_f32_16x16x32_bf16 v[96:99], a[160:163], v[216:219], 0\n"
    "v_mfma_f32_16x16x32_bf16 v[96:99], a[164:167], v[40:43], v[96:99]\n"
    "v_mfma_f32_16x16x32_bf16 v[96:99], a[168:171], v[56:59], v[96:99]\n"
    "v_mfma_f32_16x16x32_bf16 v[96:99], a[172:175], v[72:75], v[96:99]\n"
    "v_mfma_f32_16x16x32_bf16 v[100:103], a[176:179], v[216:219], 0\n"
    "v_mfma_f32_16x16x32_bf16 v[100:103], a[180:183], v[40:43], v[100:103]\n"
    "v_mfma_f32_16x16x32_bf16 v[100:103], a[184:187], v[56:59], v[100:103]\n"
    "v_mfma_f32_16x16x32_bf16 v[100:103], a[188:191], v[72:75], v[100:103]\n"
    "v_mfma_f32_16x16x32_bf16 v[104:107], a[160:163], v[220:223], 0\n"
    "v_mfma_f32_16x16x32_bf16 v[104:107], a[164:167], v[44:47], v[104:107]\n"
    "v_mfma_f32_16x16x32_bf16 v[104:107], a[168:171], v[60:63], v[104:107]\n"
    "v_mfma_f32_16x16x32_bf16 v[104:107], a[172:175], v[76:79], v[104:107]\n"
    "v_mfma_f32_16x16x32_bf16 v[108:111], a[176:179], v[220:223], 0\n"
    "v_mfma_f32_16x16x32_bf16 v[108:111], a[180:183], v[44:47], v[108:111]\n"
    "v_mfma_f32_16x16x32_bf16 v[108:111], a[184:187], v[60:63], v[108:111]\n"
    "v_mfma_f32_16x16x32_bf16 v[108:111], a[188:191], v[76:79], v[108:111]\n"
    "v_mfma_f32_16x16x32_bf16 v[112:115], a[160:163], v[208:211], 0\n"
    "v_mfma_f32_16x16x32_bf16 v[112:115], a[164:167], v[32:35], v[112:115]\n"
    "v_mfma_f32_16x16x32_bf16 v[112:115], a[168:171], v[48:51], v[112:115]\n"
    "v_mfma_f32_16x16x32_bf16 v[112:115], a[172:175], v[64:67], v[112:115]\n"
    "v_mfma_f32_16x16x32_bf16 v[116:119], a[176:179], v[208:211], 0\n"
    "v_mfma_f32_16x16x32_bf16 v[116:119], a[180:183], v[32:35], v[116:119]\n"
    "v_mfma_f32_16x16x32_bf16 v[116:119], a[184:187], v[48:51], v[116:119]\n"
    "v_mfma_f32_16x16x32_bf16 v[116:119], a[188:191], v[64:67], v[116:119]\n"
    "v_mfma_f32_16x16x32_bf16 v[120:123], a[160:163], v[212:215], 0\n"
    "v_mfma_f32_16x16x32_bf16 v[120:123], a[164:167], v[36:39], v[120:123]\n"
    "v_mfma_f32_16x16x32_bf16 v[120:123], a[168:171], v[52:55], v[120:123]\n"
    "v_mfma_f32_16x16x32_bf16 v[120:123], a[172:175], v[68:71], v[120:123]\n"
    "v_mfma_f32_16x16x32_bf16 v[124:127], a[176:179], v[212:215], 0\n"
    "v_mfma_f32_16x16x32_bf16 v[124:127], a[180:183], v[36:39], v[124:127]\n"
    "v_mfma_f32_16x16x32_bf16 v[124:127], a[184:187], v[52:55], v[124:127]\n"
    "v_mfma_f32_16x16x32_bf16 v[124:127], a[188:191], v[68:71], v[124:127]\n"
    "s_nop 7\n"
    "v_add_u32 v28, 0, v240\n"
    "v_cmp_le_i32 vcc, v28, v242\n"
    "s_nop 1\n"
    "v_cndmask_b32 v29, v2, v96, vcc\n"
    "v_max_f32 v16, v16, v29\n"
    "v_add_u32 v28, 1, v240\n"
    "v_cmp_le_i32 vcc, v28, v242\n"
    "s_nop 1\n"
    "v_cndmask_b32 v29, v2, v97, vcc\n"
    "v_max_f32 v16, v16, v29\n"
    "v_add_u32 v28, 2, v240\n"
    "v_cmp_le_i32 vcc, v28, v242\n"
    "s_nop 1\n"
    "v_cndmask_b32 v29, v2, v98, vcc\n"
    "v_max_f32 v16, v16, v29\n"
    "v_add_u32 v28, 3, v240\n"
    "v_cmp_le_i32 vcc, v28, v242\n"
    "s_nop 1\n"
    "v_cndmask_b32 v29, v2, v99, vcc\n"
    "v_max_f32 v16, v16, v29\n"
    "v_add_u32 v28, 16, v240\n"
    "v_cmp_le_i32 vcc, v28, v242\n"
    "s_nop 1\n"
    "v_cndmask_b32 v29, v2, v100, vcc\n"
    "v_max_f32 v16, v16, v29\n"
    "v_add_u32 v28, 17, v240\n"
    "v_cmp_le_i32 vcc, v28, v242\n"
    "s_nop 1\n"
    "v_cndmask_b32 v29, v2, v101, vcc\n"
    "v_max_f32 v16, v16, v29\n"
    "v_add_u32 v28, 18, v240\n"
    "v_cmp_le_i32 vcc, v28, v242\n"
    "s_nop 1\n"
    "v_cndmask_b32 v29, v2, v102, vcc\n"
    "v_max_f32 v16, v16, v29\n"
    "v_add_u32 v28, 19, v240\n"
    "v_cmp_le_i32 vcc, v28, v242\n"
    "s_nop 1\n"
    "v_cndmask_b32 v29, v2, v103, vcc\n"
    "v_max_f32 v16, v16, v29\n"
    "v_add_u32 v28, -16, v240\n"
    "v_cmp_le_i32 vcc, v28, v242\n"
    "s_nop 1\n"
    "v_cndmask_b32 v29, v2, v104, vcc\n"
    "v_max_f32 v17, v17, v29\n"
    "v_add_u32 v28, -15, v240\n"
    "v_cmp_le_i32 vcc, v28, v242\n"
    "s_nop 1\n"
    "v_cndmask_b32 v29, v2, v105, vcc\n"
    "v_max_f32 v17, v17, v29\n"
    "v_add_u32 v28, -14, v240\n"
    "v_cmp_le_i32 vcc, v28, v242\n"
    "s_nop 1\n"
    "v_cndmask_b32 v29, v2, v106, vcc\n"
    "v_max_f32 v17, v17, v29\n"
    "v_add_u32 v28, -13, v240\n"
    "v_cmp_le_i32 vcc, v28, v242\n"
    "s_nop 1\n"
    "v_cndmask_b32 v29, v2, v107, vcc\n"
    "v_max_f32 v17, v17, v29\n"
    "v_add_u32 v28, 0, v240\n"
    "v_cmp_le_i32 vcc, v28, v242\n"
    "s_nop 1\n"
    "v_cndmask_b32 v29, v2, v108, vcc\n"
    "v_max_f32 v17, v17, v29\n"
    "v_add_u32 v28, 1, v240\n"
    "v_cmp_le_i32 vcc, v28, v242\n"
    "s_nop 1\n"
    "v_cndmask_b32 v29, v2, v109, vcc\n"
    "v_max_f32 v17, v17, v29\n"
    "v_add_u32 v28, 2, v240\n"
    "v_cmp_le_i32 vcc, v28, v242\n"
    "s_nop 1\n"
    "v_cndmask_b32 v29, v2, v110, vcc\n"
    "v_max_f32 v17, v17, v29\n"
    "v_add_u32 v28, 3, v240\n"
    "v_cmp_le_i32 vcc, v28, v242\n"
    "s_nop 1\n"
    "v_cndmask_b32 v29, v2, v111, vcc\n"
    "v_max_f32 v17, v17, v29\n"
    "v_add_u32 v28, 0, v240\n"
    "v_cmp_le_i32 vcc, v28, v241\n"
    "s_nop 1\n"
    "v_cndmask_b32 v29, v2, v112, vcc\n"
    "v_max_f32 v18, v18, v29\n"
    "v_add_u32 v28, 1, v240\n"
    "v_cmp_le_i32 vcc, v28, v241\n"
    "s_nop 1\n"
    "v_cndmask_b32 v29, v2, v113, vcc\n"
    "v_max_f32 v18, v18, v29\n"
    "v_add_u32 v28, 2, v240\n"
    "v_cmp_le_i32 vcc, v28, v241\n"
    "s_nop 1\n"
    "v_cndmask_b32 v29, v2, v114, vcc\n"
    "v_max_f32 v18, v18, v29\n"
    "v_add_u32 v28, 3, v240\n"
    "v_cmp_le_i32 vcc, v28, v241\n"
    "s_nop 1\n"
    "v_cndmask_b32 v29, v2, v115, vcc\n"
    "v_max_f32 v18, v18, v29\n"
    "v_add_u32 v28, 16, v240\n"
    "v_cmp_le_i32 vcc, v28, v241\n"
    "s_nop 1\n"
    "v_cndmask_b32 v29, v2, v116, vcc\n"
    "v_max_f32 v18, v18, v29\n"
    "v_add_u32 v28, 17, v240\n"
    "v_cmp_le_i32 vcc, v28, v241\n"
    "s_nop 1\n"
    "v_cndmask_b32 v29, v2, v117, vcc\n"
    "v_max_f32 v18, v18, v29\n"
    "v_add_u32 v28, 18, v240\n"
    "v_cmp_le_i32 vcc, v28, v241\n"
    "s_nop 1\n"
    "v_cndmask_b32 v29, v2, v118, vcc\n"
    "v_max_f32 v18, v18, v29\n"
    "v_add_u32 v28, 19, v240\n"
    "v_cmp_le_i32 vcc, v28, v241\n"
    "s_nop 1\n"
    "v_cndmask_b32 v29, v2, v119, vcc\n"
    "v_max_f32 v18, v18, v29\n"
    "v_add_u32 v28, -16, v240\n"
    "v_cmp_le_i32 vcc, v28, v241\n"
    "s_nop 1\n"
    "v_cndmask_b32 v29, v2, v120, vcc\n"
    "v_max_f32 v19, v19, v29\n"
    "v_add_u32 v28, -15, v240\n"
    "v_cmp_le_i32 vcc, v28, v241\n"
    "s_nop 1\n"
    "v_cndmask_b32 v29, v2, v121, vcc\n"
    "v_max_f32 v19, v19, v29\n"
    "v_add_u32 v28, -14, v240\n"
    "v_cmp_le_i32 vcc, v28, v241\n"
    "s_nop 1\n"
    "v_cndmask_b32 v29, v2, v122, vcc\n"
    "v_max_f32 v19, v19, v29\n"
    "v_add_u32 v28, -13, v240\n"
    "v_cmp_le_i32 vcc, v28, v241\n"
    "s_nop 1\n"
    "v_cndmask_b32 v29, v2, v123, vcc\n"
    "v_max_f32 v19, v19, v29\n"
    "v_add_u32 v28, 0, v240\n"
    "v_cmp_le_i32 vcc, v28, v241\n"
    "s_nop 1\n"
    "v_cndmask_b32 v29, v2, v124, vcc\n"
    "v_max_f32 v19, v19, v29\n"
    "v_add_u32 v28, 1, v240\n"
    "v_cmp_le_i32 vcc, v28, v241\n"
    "s_nop 1\n"
    "v_cndmask_b32 v29, v2, v125, vcc\n"
    "v_max_f32 v19, v19, v29\n"
    "v_add_u32 v28, 2, v240\n"
    "v_cmp_le_i32 vcc, v28, v241\n"
    "s_nop 1\n"
    "v_cndmask_b32 v29, v2, v126, vcc\n"
    "v_max_f32 v19, v19, v29\n"
    "v_add_u32 v28, 3, v240\n"
    "v_cmp_le_i32 vcc, v28, v241\n"
    "s_nop 1\n"
    "v_cndmask_b32 v29, v2, v127, vcc\n"
    "v_max_f32 v19, v19, v29\n"
    "v_add_u32 v240, 32, v240\n"
    "s_addk_i32 s61, 0x4000\n"
    "s_sub_u32 s63, s63, 1\n"
    "s_cbranch_scc0 MAXLOOP_%=\n"
    "v_mov_b32 v28, v16\n"
    "v_mov_b32 v29, v17\n"
    "v_nop\n"
    "v_permlane16_swap_b32 v16, v28\n"
    "v_permlane16_swap_b32 v17, v29\n"
    "v_max_f32 v16, v16, v28\n"
    "v_max_f32 v17, v17, v29\n"
    "v_mov_b32 v28, v16\n"
    "v_mov_b32 v29, v17\n"
    "v_nop\n"
    "v_permlane32_swap_b32 v16, v28\n"
    "v_permlane32_swap_b32 v17, v29\n"
    "v_max_f32 v16, v16, v28\n"
    "v_max_f32 v17, v17, v29\n"
    "v_mov_b32 v28, v18\n"
    "v_mov_b32 v29, v19\n"
    "v_nop\n"
    "v_permlane16_swap_b32 v18, v28\n"
    "v_permlane16_swap_b32 v19, v29\n"
    "v_max_f32 v18, v18, v28\n"
    "v_max_f32 v19, v19, v29\n"
    "v_mov_b32 v28, v18\n"
    "v_mov_b32 v29, v19\n"
    "v_nop\n"
    "v_permlane32_swap_b32 v18, v28\n"
    "v_permlane32_swap_b32 v19, v29\n"
    "v_max_f32 v18, v18, v28\n"
    "v_max_f32 v19, v19, v29\n"
    "s_and_b32 s59, s58, 1\n"
    "s_lshl_b32 s59, s59, 12\n"
    "s_lshl_b32 s60, s58, 12\n"
    "s_add_u32 s88, s91, s60\n"
    "s_lshl_b32 s60, s57, 16\n"
    "s_add_u32 s86, s88, s60\n"
    "s_add_u32 s86, s86, 0x10000\n"
    "s_add_u32 s87, s88, 0x100000\n"
    "s_sub_u32 s87, s87, s60\n"
    "s_sub_u32 s78, s87, 0x10000\n"
    "s_add_u32 s60, s88, 0x70000\n"
    "s_cmp_ge_u32 s57, 6\n"
    "s_cselect_b32 s26, 1, s60\n"
    "s_cselect_b32 s27, s88, s86\n"
    "s_cmp_eq_u32 s57, 7\n"
    "s_cselect_b32 s85, 1, s78\n"
    "s_cselect_b32 s60, s88, s60\n"
    "s_add_u32 s61, s60, 0x8000\n"
    "s_add_u32 s64, s59, 0x2000\n"
    "s_cmp_lt_u32 s58, 2\n"
    "s_cselect_b32 s84, s60, 0xc0000000\n"
    "s_cselect_b32 s80, s61, s60\n"
    "s_cselect_b32 s64, s64, s59\n"
    "s_mov_b32 s79, 0\n"
    "s_mov_b32 s83, s84\n"
    "s_max_u32 m0, s64, s79\n"
    "s_nop 0\n"
    "buffer_load_dwordx4 v0, s[40:43], s83 offen offset:0 sc1 lds\n"
    "s_addk_i32 s64, 0x4000\n"
    "s_nop 0\n"
    "buffer_load_dwordx4 v0, s[40:43], s83 offen offset:1024 sc1 lds\n"
    "s_and_b32 s64, s64, 0x1ffff\n"
    "s_nop 0\n"
    "buffer_load_dwordx4 v0, s[40:43], s83 offen offset:2048 sc1 lds\n"
    "s_addk_i32 s84, 0x4000\n"
    "s_nop 0\n"
    "buffer_load_dwordx4 v0, s[40:43], s83 offen offset:3072 sc1 lds\n"
    "s_mov_b32 s83, s84\n"
    "s_max_u32 m0, s64, s79\n"
    "s_nop 0\n"
    "buffer_load_dwordx4 v0, s[40:43], s83 offen offset:0 sc1 lds\n"
    "s_addk_i32 s64, 0x4000\n"
    "s_nop 0\n"
    "buffer_load_dwordx4 v0, s[40:43], s83 offen offset:1024 sc1 lds\n"
    "s_and_b32 s64, s64, 0x1ffff\n"
    "s_nop 0\n"
    "buffer_load_dwordx4 v0, s[40:43], s83 offen offset:2048 sc1 lds\n"
    "s_mov_b32 s84, s80\n"
    "s_nop 0\n"
    "buffer_load_dwordx4 v0, s[40:43], s83 offen offset:3072 sc1 lds\n"
    "s_mov_b32 s83, s84\n"
    "s_max_u32 m0, s64, s79\n"
    "s_nop 0\n"
    "buffer_load_dwordx4 v0, s[40:43], s83 offen offset:0 sc1 lds\n"
    "s_addk_i32 s64, 0x4000\n"
    "s_nop 0\n"
    "buffer_load_dwordx4 v0, s[40:43], s83 offen offset:1024 sc1 lds\n"
    "s_and_b32 s64, s64, 0x1ffff\n"
    "s_nop 0\n"
    "buffer_load_dwordx4 v0, s[40:43], s83 offen offset:2048 sc1 lds\n"
    "s_addk_i32 s84, 0x4000\n"
    "s_nop 0\n"
    "buffer_load_dwordx4 v0, s[40:43], s83 offen offset:3072 sc1 lds\n"
    "s_mov_b32 s83, s84\n"
    "s_max_u32 m0, s64, s79\n"
    "s_nop 0\n"
    "buffer_load_dwordx4 v0, s[40:43], s83 offen offset:0 sc1 lds\n"
    "s_addk_i32 s64, 0x4000\n"
    "s_nop 0\n"
    "buffer_load_dwordx4 v0, s[40:43], s83 offen offset:1024 sc1 lds\n"
    "s_and_b32 s64, s64, 0x1ffff\n"
    "s_nop 0\n"
    "buffer_load_dwordx4 v0, s[40:43], s83 offen offset:2048 sc1 lds\n"
    "s_addk_i32 s84, 0x4000\n"
    "s_nop 0\n"
    "buffer_load_dwordx4 v0, s[40:43], s83 offen offset:3072 sc1 lds\n"
    "s_mov_b32 s83, s84\n"
    "s_max_u32 m0, s64, s79\n"
    "s_nop 0\n"
    "buffer_load_dwordx4 v0, s[40:43], s83 offen offset:0 sc1 lds\n"
    "s_addk_i32 s64, 0x4000\n"
    "s_nop 0\n"
    "buffer_load_dwordx4 v0, s[40:43], s83 offen offset:1024 sc1 lds\n"
    "s_and_b32 s64, s64, 0x1ffff\n"
    "s_nop 0\n"
    "buffer_load_dwordx4 v0, s[40:43], s83 offen offset:2048 sc1 lds\n"
    "s_addk_i32 s84, 0x4000\n"
    "s_nop 0\n"
    "buffer_load_dwordx4 v0, s[40:43], s83 offen offset:3072 sc1 lds\n"
    "s_mov_b32 s83, s84\n"
    "s_max_u32 m0, s64, s79\n"
    "s_nop 0\n"
    "buffer_load_dwordx4 v0, s[40:43], s83 offen offset:0 sc1 lds\n"
    "s_addk_i32 s64, 0x4000\n"
    "s_nop 0\n"
    "buffer_load_dwordx4 v0, s[40:43], s83 offen offset:1024 sc1 lds\n"
    "s_and_b32 s64, s64, 0x1ffff\n"
    "s_nop 0\n"
    "buffer_load_dwordx4 v0, s[40:43], s83 offen offset:2048 sc1 lds\n"
    "s_addk_i32 s84, 0x4000\n"
    "s_nop 0\n"
    "buffer_load_dwordx4 v0, s[40:43], s83 offen offset:3072 sc1 lds\n"
    "s_mov_b32 s83, s84\n"
    "s_max_u32 m0, s64, s79\n"
    "s_nop 0\n"
    "buffer_load_dwordx4 v0, s[40:43], s83 offen offset:0 sc1 lds\n"
    "s_addk_i32 s64, 0x4000\n"
    "s_nop 0\n"
    "buffer_load_dwordx4 v0, s[40:43], s83 offen offset:1024 sc1 lds\n"
    "s_and_b32 s64, s64, 0x1ffff\n"
    "s_nop 0\n"
    "buffer_load_dwordx4 v0, s[40:43], s83 offen offset:2048 sc1 lds\n"
    "s_addk_i32 s84, 0x4000\n"
    "s_nop 0\n"
    "buffer_load_dwordx4 v0, s[40:43], s83 offen offset:3072 sc1 lds\n"
    "v_mov_b32 v4, v0\n"
    "v_add_u32 v5, 0x8000, v0\n"
    "v_accvgpr_write_b32 a0, 0\n"
    "v_accvgpr_write_b32 a1, 0\n"
    "v_accvgpr_write_b32 a2, 0\n"
    "v_accvgpr_write_b32 a3, 0\n"
    "s_nop 1\n"
    "v_mfma_f32_32x32x16_bf16 a[112:127], a[0:3], a[0:3], 0\n"
    "v_mfma_f32_32x32x16_bf16 a[96:111], a[0:3], a[0:3], 0\n"
    "v_mfma_f32_32x32x16_bf16 a[80:95], a[0:3], a[0:3], 0\n"
    "v_mfma_f32_32x32x16_bf16 a[64:79], a[0:3], a[0:3], 0\n"
    "v_mfma_f32_32x32x16_bf16 a[48:63], a[0:3], a[0:3], 0\n"
    "v_mfma_f32_32x32x16_bf16 a[32:47], a[0:3], a[0:3], 0\n"
    "v_mfma_f32_32x32x16_bf16 a[16:31], a[0:3], a[0:3], 0\n"
    "v_mfma_f32_32x32x16_bf16 a[0:15], a[0:3], a[0:3], 0\n"
    "s_waitcnt vmcnt(0)\n"
    "s_barrier\n"
    "ds_read_b128 a[160:163], v4 offset:8192\n"
    "ds_read_b128 a[164:167], v4 offset:9216\n"
    "ds_read_b128 a[168:171], v4 offset:10240\n"
    "ds_read_b128 a[172:175], v4 offset:11264\n"
    "ds_read_b128 a[176:179], v4 offset:12288\n"
    "ds_read_b128 a[180:183], v4 offset:13312\n"
    "ds_read_b128 a[184:187], v4 offset:14336\n"
    "ds_read_b128 a[188:191], v4 offset:15360\n"
    "s_waitcnt lgkmcnt(0)\n"
    "v_mfma_f32_16x16x32_bf16 v[96:99], a[160:163], v[216:219], 0\n"
    "v_mfma_f32_16x16x32_bf16 v[96:99], a[164:167], v[40:43], v[96:99]\n"
    "v_mfma_f32_16x16x32_bf16 v[96:99], a[168:171], v[56:59], v[96:99]\n"
    "v_mfma_f32_16x16x32_bf16 v[96:99], a[172:175], v[72:75], v[96:99]\n"
    "v_mfma_f32_16x16x32_bf16 v[100:103], a[176:179], v[216:219], 0\n"
    "v_mfma_f32_16x16x32_bf16 v[100:103], a[180:183], v[40:43], v[100:103]\n"
    "v_mfma_f32_16x16x32_bf16 v[100:103], a[184:187], v[56:59], v[100:103]\n"
    "v_mfma_f32_16x16x32_bf16 v[100:103], a[188:191], v[72:75], v[100:103]\n"
    "v_mfma_f32_16x16x32_bf16 v[104:107], a[160:163], v[220:223], 0\n"
    "v_mfma_f32_16x16x32_bf16 v[104:107], a[164:167], v[44:47], v[104:107]\n"
    "v_mfma_f32_16x16x32_bf16 v[104:107], a[168:171], v[60:63], v[104:107]\n"
    "v_mfma_f32_16x16x32_bf16 v[104:107], a[172:175], v[76:79], v[104:107]\n"
    "v_mfma_f32_16x16x32_bf16 v[108:111], a[176:179], v[220:223], 0\n"
    "v_mfma_f32_16x16x32_bf16 v[108:111], a[180:183], v[44:47], v[108:111]\n"
    "v_mfma_f32_16x16x32_bf16 v[108:111], a[184:187], v[60:63], v[108:111]\n"
    "v_mfma_f32_16x16x32_bf16 v[108:111], a[188:191], v[76:79], v[108:111]\n"
    "s_nop 7\n"
    "s_branch ATT_RESTART_%=\n"
    "THE_END_%=:\n"
    :
    : [x] "s"(x), [ckv] "s"(ckv), [wq] "s"(wq), [wuk] "s"(wuk), [wuv] "s"(wuv), [wo] "s"(wo), [wg] "s"(wg), [wu] "s"(wu), [wd] "s"(wd),
      [out] "s"(out), [ws] "s"(ws), [bid] "s"(bid), [wid] "s"(wid)
    : MEGA_CLOBBERS);
}

extern "C" void kernel_launch(void* const* d_in, const int* in_sizes, int n_in, void* d_out, int out_size, void* d_ws, size_t ws_size, hipStream_t stream) {
  (void)in_sizes; (void)n_in; (void)out_size; (void)ws_size;
  (void)hipFuncSetAttribute((const void*)mega_kernel, hipFuncAttributeMaxDynamicSharedMemorySize, 163840);
  mega_kernel<<<256, 256, 163840, stream>>>(d_in[0], d_in[1], d_in[2], d_in[3], d_in[4], d_in[5], d_in[7], d_in[8], d_in[9], d_out, d_ws);
}
```
